```python
import torch

def _softmax(x, axis=-1):
    return torch.softmax(x, dim=axis)
def _gelu(x):
    return torch.nn.functional.gelu(x, approximate="tanh")
def _silu(x):
    return torch.nn.functional.silu(x)
def _sigmoid(x):
    return torch.sigmoid(x)
def _layernorm(x, axis=-1, eps=1e-5):
    m = x.mean(dim=axis, keepdim=True)
    v = x.var(dim=axis, keepdim=True, unbiased=False)
    return (x - m) / torch.sqrt(v + eps)
def _scatter_add(tgt, idx, src, axis=0):
    return tgt.clone().index_add_(axis, idx, src)
def _segment_sum(src, seg, n):
    out = torch.zeros((n,) + src.shape[1:], dtype=src.dtype)
    return out.index_add_(0, seg, src)
def _pad(x, pw):
    flat = tuple(v for pair in reversed(pw) for v in pair)
    return torch.nn.functional.pad(x, flat)

kernel_name = 'parallel_sum__mha_block_cached__glu_gated_glu__decode'

def setup_inputs():

    _w = (lambda a: (lambda t: t.to(torch.bfloat16) if t.is_floating_point() else t)(torch.as_tensor(a)))
    def _A_setup():
        B, Sq, D = 1, 1, 512
        H, Sk, Dh = 32, 32768, 128
        return {
            "x": _w((lambda *s: torch.randn(*s, dtype=torch.bfloat16) * 0.05)(B, Sq, D)),
            "k_cache": _w((lambda *s: torch.randn(*s, dtype=torch.bfloat16) * 3.2)(B, H, Sk, Dh)),
            "v_cache": _w((lambda *s: torch.randn(*s, dtype=torch.bfloat16) * 1.6)(B, H, Sk, Dh)),
            "wq": _w((lambda *s: torch.randn(*s, dtype=torch.bfloat16) * 3.2)(D, H * Dh) * (D ** -0.5)),
            "wo": _w((lambda *s: torch.randn(*s, dtype=torch.bfloat16) * 1.6)(H * Dh, D) * ((H * Dh) ** -0.5)),
        }
    _A_in = _A_setup()

    def _B_setup():
        B, S, D, F = 1, 1, 512, 1792
        return {
            "x": _w((lambda *s: torch.randn(*s, dtype=torch.bfloat16) * 0.05)(B, S, D)),
            "w_gate": _w((lambda *s: torch.randn(*s, dtype=torch.bfloat16) * 0.8)(D, F)),
            "w_up": _w((lambda *s: torch.randn(*s, dtype=torch.bfloat16) * 0.05)(D, F)),
            "w_down": _w((lambda *s: torch.randn(*s, dtype=torch.bfloat16) * 0.05)(F, D)),
        }
    _B_in = _B_setup()

    return {
        "A_x": _A_in["x"],
        "A_k_cache": _A_in["k_cache"],
        "A_v_cache": _A_in["v_cache"],
        "A_wq": _A_in["wq"],
        "A_wo": _A_in["wo"],
        "B_x": _B_in["x"],
        "B_w_gate": _B_in["w_gate"],
        "B_w_up": _B_in["w_up"],
        "B_w_down": _B_in["w_down"],
    }

def reference(A_x, A_k_cache, A_v_cache, A_wq, A_wo, B_x, B_w_gate, B_w_up, B_w_down):
    def _A_ref(x, k_cache, v_cache, wq, wo):
        B, Sq, D = x.shape
        H, Dh = 32, 128
        q = (lambda x, *d: x.permute(*d))((x @ wq).reshape(B, Sq, H, Dh), 0, 2, 1, 3)
        scores = torch.einsum("bhqd,bhkd->bhqk", q, k_cache) * (Dh ** -0.5)
        p = _softmax(scores, axis=-1)
        o = torch.einsum("bhqk,bhkd->bhqd", p, v_cache)
        return (lambda x, *d: x.permute(*d))(o, 0, 2, 1, 3).reshape(B, Sq, H * Dh) @ wo

    def _B_ref(x, w_gate, w_up, w_down):
        g = torch.einsum("bsd,df->bsf", x, w_gate)
        u = torch.einsum("bsd,df->bsf", x, w_up)
        h = _sigmoid(g) * u
        return torch.einsum("bsf,fd->bsd", h, w_down)

    x = A_x
    xn = _layernorm(x, axis=-1)
    return x + _A_ref(xn, A_k_cache, A_v_cache, A_wq, A_wo) + _B_ref(xn, B_w_gate, B_w_up, B_w_down)
```

```cpp
#include <hip/hip_runtime.h>
#include <hip/hip_bf16.h>

#define EMU_BF16 1
#define DEPTH 3

typedef unsigned v4u __attribute__((ext_vector_type(4)));
typedef float f32x4 __attribute__((ext_vector_type(4)));
typedef float f32x2 __attribute__((ext_vector_type(2)));
typedef __bf16 bf16x2 __attribute__((ext_vector_type(2)));
typedef unsigned long long u64;

constexpr int NWG = 256, WAVES = 8, NMLP = 224;
constexpr size_t WS_ML = 0, WS_YA = 4096, WS_YM = WS_YA + (size_t)NWG * 512 * 4, WS_END = WS_YM + (size_t)NMLP * 512 * 4;

template <int OFF> __device__ __forceinline__ void ld128nt(v4u& dst, unsigned voff, u64 sbase) {
  asm volatile("global_load_dwordx4 %0, %1, %2 offset:%3 nt" : "=v"(dst) : "v"(voff), "s"(sbase), "i"(OFF) : "memory");
}
template <int OFF> __device__ __forceinline__ void ld128(v4u& dst, unsigned voff, u64 sbase) {
  asm volatile("global_load_dwordx4 %0, %1, %2 offset:%3" : "=v"(dst) : "v"(voff), "s"(sbase), "i"(OFF) : "memory");
}
__device__ __forceinline__ void ld16(unsigned& dst, unsigned voff, u64 sbase) {
  asm volatile("global_load_ushort %0, %1, %2" : "=v"(dst) : "v"(voff), "s"(sbase) : "memory");
}
template <int N> __device__ __forceinline__ void waitv(v4u& b) { asm volatile("s_waitcnt vmcnt(%1)" : "+v"(b) : "i"(N) : "memory"); }
template <int N> __device__ __forceinline__ void waitv2(v4u& a, v4u& b) { asm volatile("s_waitcnt vmcnt(%2)" : "+v"(a), "+v"(b) : "i"(N) : "memory"); }
template <int N> __device__ __forceinline__ void waitvx(v4u& a, unsigned& b) { asm volatile("s_waitcnt vmcnt(%2)" : "+v"(a), "+v"(b) : "i"(N) : "memory"); }

#define ACLOB "a0","a1","a2","a3","a4","a5","a6","a7","a8","a9","a10","a11","a12","a13","a14","a15","a16","a17","a18","a19","a20","a21","a22","a23","a24","a25","a26","a27","a28","a29","a30","a31", \
  "a32","a33","a34","a35","a36","a37","a38","a39","a40","a41","a42","a43","a44","a45","a46","a47","a48","a49","a50","a51","a52","a53","a54","a55","a56","a57","a58","a59","a60","a61","a62","a63", \
  "a64","a65","a66","a67","a68","a69","a70","a71","a72","a73","a74","a75","a76","a77","a78","a79","a80","a81","a82","a83","a84","a85","a86","a87","a88","a89","a90","a91","a92","a93","a94","a95", \
  "a96","a97","a98","a99"
template <int SLOT, int OFF> __device__ __forceinline__ void ald(unsigned voff, u64 sbase) {
  asm volatile("global_load_dwordx4 a[%c2:%c3], %0, %1 offset:%c4 nt" :: "v"(voff), "s"(sbase), "i"(4 * SLOT), "i"(4 * SLOT + 3), "i"(OFF) : "memory", ACLOB);
}
template <int SLOT, int N> __device__ __forceinline__ v4u aget(void) {
  unsigned a, b, c, d;
  asm volatile("s_waitcnt vmcnt(%c4)\n\tv_accvgpr_read_b32 %0, a%c5\n\tv_accvgpr_read_b32 %1, a%c6\n\tv_accvgpr_read_b32 %2, a%c7\n\tv_accvgpr_read_b32 %3, a%c8"
               : "=v"(a), "=v"(b), "=v"(c), "=v"(d) : "i"(N), "i"(4 * SLOT), "i"(4 * SLOT + 1), "i"(4 * SLOT + 2), "i"(4 * SLOT + 3) : "memory", ACLOB);
  return v4u{a, b, c, d};
}
template <int SLOT> __device__ __forceinline__ v4u aread(void) {
  unsigned a, b, c, d;
  asm volatile("v_accvgpr_read_b32 %0, a%c4\n\tv_accvgpr_read_b32 %1, a%c5\n\tv_accvgpr_read_b32 %2, a%c6\n\tv_accvgpr_read_b32 %3, a%c7"
               : "=v"(a), "=v"(b), "=v"(c), "=v"(d) : "i"(4 * SLOT), "i"(4 * SLOT + 1), "i"(4 * SLOT + 2), "i"(4 * SLOT + 3) : "memory", ACLOB);
  return v4u{a, b, c, d};
}

__device__ __forceinline__ float dot2(unsigned a, unsigned b, float acc) {
  return __builtin_amdgcn_fdot2_f32_bf16(__builtin_bit_cast(bf16x2, a), __builtin_bit_cast(bf16x2, b), acc, false);
}
__device__ __forceinline__ float lo16(unsigned v) { return __builtin_bit_cast(float, v << 16); }
__device__ __forceinline__ float hi16(unsigned v) { return __builtin_bit_cast(float, v & 0xffff0000u); }
template <int CTRL> __device__ __forceinline__ float dpp(float x) {
  return __builtin_bit_cast(float, __builtin_amdgcn_mov_dpp(__builtin_bit_cast(int, x), CTRL, 0xf, 0xf, true));
}
constexpr int XOR1 = 0xB1, XOR2 = 0x4E, XOR7 = 0x141, XOR8 = 0x128;
__device__ __forceinline__ unsigned cvtpk(float lo, float hi) {
  unsigned r; asm("v_cvt_pk_bf16_f32 %0, %1, %2" : "=v"(r) : "v"(lo), "v"(hi)); return r;
}
__device__ __forceinline__ float rbf(float x) { return lo16(cvtpk(x, x) & 0xffffu); }
__device__ __forceinline__ float pair32(float a, float b) {
  auto s = __builtin_amdgcn_permlane32_swap(__float_as_uint(a), __float_as_uint(b), false, false);
  return __uint_as_float(s[0]) + __uint_as_float(s[1]);
}
__device__ __forceinline__ float pair16(float a, float b) {
  auto s = __builtin_amdgcn_permlane16_swap(__float_as_uint(a), __float_as_uint(b), false, false);
  return __uint_as_float(s[0]) + __uint_as_float(s[1]);
}
__device__ __forceinline__ float wave_sum(float x) {
  x += dpp<XOR1>(x); x += dpp<XOR2>(x); x += dpp<XOR7>(x); x += dpp<XOR8>(x);
  x = pair16(x, x); x = pair32(x, x); return x;
}
__device__ __forceinline__ float wave_max(float x) {
  x = fmaxf(x, dpp<XOR1>(x)); x = fmaxf(x, dpp<XOR2>(x)); x = fmaxf(x, dpp<XOR7>(x)); x = fmaxf(x, dpp<XOR8>(x));
  auto s = __builtin_amdgcn_permlane16_swap(__float_as_uint(x), __float_as_uint(x), false, false);
  x = fmaxf(__uint_as_float(s[0]), __uint_as_float(s[1]));
  auto t = __builtin_amdgcn_permlane32_swap(__float_as_uint(x), __float_as_uint(x), false, false);
  return fmaxf(__uint_as_float(t[0]), __uint_as_float(t[1]));
}
__device__ __forceinline__ float treduce16(float (&x)[16], int c) {
#define TR_STEP(HALF, CTRL, BIT) _Pragma("unroll") for (int h = 0; h < (HALF); ++h) { \
    const float lo_ = x[h], hi_ = x[h + (HALF)]; const float t1_ = lo_ + dpp<CTRL>(lo_), t2_ = hi_ + dpp<CTRL>(hi_); x[h] = (c & (BIT)) ? t2_ : t1_; }
  TR_STEP(8, XOR8, 8) TR_STEP(4, XOR7, 4) TR_STEP(2, XOR2, 2) TR_STEP(1, XOR1, 1)
#undef TR_STEP
  return x[0];
}
template <int T> __device__ __forceinline__ float bcast(float p) {
  return __builtin_bit_cast(float, __builtin_amdgcn_mov_dpp(__builtin_bit_cast(int, p), 0x150 + T, 0xf, 0xf, true));
}
__device__ __forceinline__ void fma8(float (&acc)[8], float p, const v4u& v) {
#pragma unroll
  for (int k = 0; k < 4; ++k) { acc[2 * k] = fmaf(p, lo16(v[k]), acc[2 * k]); acc[2 * k + 1] = fmaf(p, hi16(v[k]), acc[2 * k + 1]); }
}

struct Stream {
  u64 K, V; unsigned voff; int rot;
  __device__ __forceinline__ u64 base(int round) const { return ((round < 32) ? K : V) + (u64)(unsigned)(((round + rot) & 31) << 15); }
  template <int J> __device__ __forceinline__ void issue(int round0) const {
    ald<(J & 7), (J & 3) * 1024>(voff, base(round0 + (J >> 2)));
  }
};
__device__ __forceinline__ float qk(const v4u& k, const v4u& q) { return dot2(k[3], q[3], dot2(k[2], q[2], dot2(k[1], q[1], dot2(k[0], q[0], 0.f)))); }

template <int U> __device__ __forceinline__ void kstep(const Stream& s, float (&d)[16], const v4u& q, int round0) {
  const v4u k = aget<(U & 7), DEPTH - 1>();
  s.issue<U + DEPTH>(round0);
  d[U] = qk(k, q);
}
template <int U> __device__ __forceinline__ void vstep(const Stream& s, float (&acc)[8], float p, int round0) {
  const v4u v = aget<(U & 7), DEPTH - 1>();
  s.issue<U + DEPTH>(round0);
  fma8(acc, bcast<U>(p), v);
}
#define REP16(M) M(0) M(1) M(2) M(3) M(4) M(5) M(6) M(7) M(8) M(9) M(10) M(11) M(12) M(13) M(14) M(15)
#define REP8(M) M(0) M(1) M(2) M(3) M(4) M(5) M(6) M(7)

__device__ __forceinline__ float fin_score(float d) {
  return rbf(rbf(d) * 0.08838834764831845f);
}

__global__ void __launch_bounds__(WAVES * 64, 2) attn_main(const void* xp, const void* Kp, const void* Vp, const void* wqp, const void* wop,
                                                          const void* wgp, const void* wup, const void* wdp, void* wsp) {
  __shared__ __attribute__((aligned(16))) float lds[8 * 128 + 8 * 16 + 8 * 128 + 8 * 2 + 8 * 64 * 2];
  float* Lq = lds;
  float* Lg = lds + 1024;
  float* Lo = lds + 1024 + 128;
  float* Lml = Lo + 1024;
  unsigned* Lp = (unsigned*)(Lml + 16);
  const int tid = threadIdx.x, lane = tid & 63, w = __builtin_amdgcn_readfirstlane(tid >> 6), r = lane >> 4, c = lane & 15;
  const int rg = lane >> 3, cg = lane & 7;
  const int b = blockIdx.x, xcd = b & 7, i8 = b >> 3;
  const int head = xcd * 4 + (i8 & 3), split = i8 >> 2, chunk = head * 8 + split;
  const bool has_mlp = i8 < 28;
  const int j = xcd * 28 + (has_mlp ? i8 : 27);

  Stream s;
  { const u64 co = ((u64)chunk << 20) + (u64)(w << 12); s.K = (u64)Kp + co; s.V = (u64)Vp + co; s.voff = lane * 16; s.rot = b & 31; }

  v4u x8; unsigned xs; v4u wq[16], wg, wu;
  ld128<0>(x8, lane * 16, (u64)xp);
  ld16(xs, (4 * c + r) * 2, (u64)xp + w * 128);
  {
    const unsigned vo = r * 8192 + c * 16; const u64 sb = (u64)wqp + (u64)w * (64 * 8192) + head * 256;
#define M(T) ld128nt<0>(wq[T], vo, sb + (u64)(T) * (4 * 8192));
    REP16(M)
#undef M
  }
  { const unsigned vo = (4 * c + r) * 3584; const u64 o = (u64)w * (64 * 3584) + j * 16;
    ld128nt<0>(wg, vo, (u64)wgp + o); ld128nt<0>(wu, vo, (u64)wup + o); }
#define M(T) s.issue<T>(0);
  REP8(M)
#undef M
  {
    const unsigned vo = rg * 2048 + cg * 16; const u64 sb = (u64)wop + (u64)head * (128 * 1024) + w * 128;
#define M(T) ald<8 + 2 * T, 0>(vo, sb + (u64)(T) * 16384); ald<9 + 2 * T, 1024>(vo, sb + (u64)(T) * 16384);
    REP8(M)
#undef M
    ald<24, 0>(rg * 1024 + cg * 16, (u64)wdp + (u64)j * 8192 + w * 128);
  }
  waitvx<43>(x8, xs);
  float xn;
  {
    float xv[8];
#pragma unroll
    for (int k = 0; k < 4; ++k) { xv[2 * k] = lo16(x8[k]); xv[2 * k + 1] = hi16(x8[k]); }
    float sum = 0.f;
#pragma unroll
    for (int k = 0; k < 8; ++k) sum += xv[k];
    const float mean = wave_sum(sum) * (1.f / 512.f);
    float sq = 0.f;
#pragma unroll
    for (int k = 0; k < 8; ++k) { const float dlt = xv[k] - mean; sq = fmaf(dlt, dlt, sq); }
    const float var = wave_sum(sq) * (1.f / 512.f);
    xn = rbf(rbf(lo16(xs) - rbf(mean)) / rbf(sqrtf(rbf(rbf(var) + 1e-5f))));
  }
  {
    float a[8] = {0.f, 0.f, 0.f, 0.f, 0.f, 0.f, 0.f, 0.f};
#define M(T) waitv<42 - T>(wq[T]); fma8(a, bcast<T>(xn), wq[T]);
    REP16(M)
#undef M
    const float b0 = pair32(a[0], a[4]), b1 = pair32(a[1], a[5]), b2 = pair32(a[2], a[6]), b3 = pair32(a[3], a[7]);
    const float c0 = pair16(b0, b2), c1 = pair16(b1, b3);
    *(f32x2*)(Lq + w * 128 + 8 * c + 2 * r) = f32x2{c0, c1};
  }
  {
    waitv2<25>(wg, wu);
    float g[16];
#pragma unroll
    for (int k = 0; k < 4; ++k) { g[2 * k] = xn * lo16(wg[k]); g[2 * k + 1] = xn * hi16(wg[k]); g[8 + 2 * k] = xn * lo16(wu[k]); g[8 + 2 * k + 1] = xn * hi16(wu[k]); }
    float t = treduce16(g, c);
    t = pair16(t, t); t = pair32(t, t);
    if (r == 0) Lg[w * 16 + c] = t;
  }
  asm volatile("s_waitcnt lgkmcnt(0)\n\ts_barrier" ::: "memory");
  v4u q;
  {
    f32x4 a0 = *(const f32x4*)(Lq + (2 * r) * 128 + 8 * c), a1 = *(const f32x4*)(Lq + (2 * r) * 128 + 8 * c + 4);
    f32x4 b0 = *(const f32x4*)(Lq + (2 * r + 1) * 128 + 8 * c), b1 = *(const f32x4*)(Lq + (2 * r + 1) * 128 + 8 * c + 4);
    a0 += b0; a1 += b1;
    float v[8] = {a0[0], a0[1], a0[2], a0[3], a1[0], a1[1], a1[2], a1[3]};
#pragma unroll
    for (int k = 0; k < 8; ++k) { v[k] = pair16(v[k], v[k]); v[k] = pair32(v[k], v[k]); }
#pragma unroll
    for (int k = 0; k < 4; ++k) q[k] = cvtpk(v[2 * k], v[2 * k + 1]);
  }
  float sc[8];
  float d[16];
#define M(U) { const v4u k = aget<U, (7 - U) + 17 + (U < DEPTH ? U : DEPTH)>(); if (U < DEPTH) s.issue<8 + U>(0); d[U] = qk(k, q); }
  REP8(M)
#undef M
  kstep<8>(s, d, q, 0); kstep<9>(s, d, q, 0); kstep<10>(s, d, q, 0); kstep<11>(s, d, q, 0);
  kstep<12>(s, d, q, 0); kstep<13>(s, d, q, 0); kstep<14>(s, d, q, 0); kstep<15>(s, d, q, 0);
  sc[7] = fin_score(treduce16(d, c));
  float hval;
  {
    float g = 0.f, u = 0.f;
#pragma unroll
    for (int ww = 0; ww < 8; ++ww) { g += Lg[ww * 16 + rg]; u += Lg[ww * 16 + 8 + rg]; }
    g = rbf(g); u = rbf(u);
    hval = rbf(rbf(1.f / (1.f + __expf(-g))) * u);
    if (!has_mlp) hval = 0.f;
  }
  for (int tile = 1; tile < 8; ++tile) {
#define M(U) kstep<U>(s, d, q, tile * 4);
    REP16(M)
#undef M
#pragma unroll
    for (int k = 0; k < 7; ++k) sc[k] = sc[k + 1];
    sc[7] = fin_score(treduce16(d, c));
  }
  float mw, lw;
  {
    float m = sc[0];
#pragma unroll
    for (int k = 1; k < 8; ++k) m = fmaxf(m, sc[k]);
    mw = wave_max(m);
    float l = 0.f;
#pragma unroll
    for (int k = 0; k < 8; ++k) { sc[k] = __builtin_amdgcn_exp2f((sc[k] - mw) * 1.4426950408889634f); l += sc[k]; }
    lw = wave_sum(l);
    if (lane == 0) *(f32x2*)(Lml + 2 * w) = f32x2{mw, lw};
  }
  float acc[8] = {0.f, 0.f, 0.f, 0.f, 0.f, 0.f, 0.f, 0.f};
  for (int tile = 8; tile < 15; ++tile) {
    const float p = sc[0];
#define M(U) vstep<U>(s, acc, p, tile * 4);
    REP16(M)
#undef M
#pragma unroll
    for (int k = 0; k < 7; ++k) sc[k] = sc[k + 1];
  }
  v4u wo[16], wd;
  {
    const float p = sc[0];
#define M(U) if (U >= DEPTH) s.issue<U>(60);
    REP8(M)
#undef M
#define M(T) { const v4u A = aread<8 + 2 * T>(), B = aread<9 + 2 * T>(); _Pragma("unroll") for (int k = 0; k < 4; ++k) { \
      wo[2 * T][k] = __builtin_amdgcn_perm(B[k], A[k], 0x05040100u); wo[2 * T + 1][k] = __builtin_amdgcn_perm(B[k], A[k], 0x07060302u); } }
    REP8(M)
#undef M
    wd = aread<24>();
#define M(U) { const v4u v = aget<U, 7>(); s.issue<8 + U>(60); fma8(acc, bcast<U>(p), v); }
    REP8(M)
#undef M
#define M(U) { const v4u v = aget<U, 7 - U>(); fma8(acc, bcast<8 + U>(p), v); }
    REP8(M)
#undef M
  }
  {
    const float b0 = pair32(acc[0], acc[4]), b1 = pair32(acc[1], acc[5]), b2 = pair32(acc[2], acc[6]), b3 = pair32(acc[3], acc[7]);
    const float c0 = pair16(b0, b2), c1 = pair16(b1, b3);
    *(f32x2*)(Lo + w * 128 + 8 * c + 2 * r) = f32x2{c0, c1};
  }
  asm volatile("s_waitcnt lgkmcnt(0)\n\ts_barrier" ::: "memory");
  float M_, L_;
  {
    float mm[8], ll[8];
#pragma unroll
    for (int ww = 0; ww < 8; ++ww) { const f32x2 t = *(const f32x2*)(Lml + 2 * ww); mm[ww] = t[0]; ll[ww] = t[1]; }
    M_ = mm[0];
#pragma unroll
    for (int ww = 1; ww < 8; ++ww) M_ = fmaxf(M_, mm[ww]);
    L_ = 0.f;
    float o0 = 0.f, o1 = 0.f;
#pragma unroll
    for (int ww = 0; ww < 8; ++ww) {
      const float f = __builtin_amdgcn_exp2f((mm[ww] - M_) * 1.4426950408889634f);
      const f32x2 t = *(const f32x2*)(Lo + ww * 128 + 2 * lane);
      L_ = fmaf(f, ll[ww], L_); o0 = fmaf(f, t[0], o0); o1 = fmaf(f, t[1], o1);
    }
    const unsigned ph = cvtpk(o0, o1);
    const unsigned pl = cvtpk(o0 - lo16(ph), o1 - hi16(ph));
    Lp[w * 128 + (lane & 7) * 8 + (lane >> 3)] = ph;
    Lp[w * 128 + 64 + (lane & 7) * 8 + (lane >> 3)] = pl;
  }
  asm volatile("s_waitcnt lgkmcnt(0)" ::: "memory");
  {
    const v4u h0 = *(const v4u*)(Lp + w * 128 + rg * 8), h1 = *(const v4u*)(Lp + w * 128 + rg * 8 + 4);
    const v4u l0 = *(const v4u*)(Lp + w * 128 + 64 + rg * 8), l1 = *(const v4u*)(Lp + w * 128 + 64 + rg * 8 + 4);
    float y[8] = {0.f, 0.f, 0.f, 0.f, 0.f, 0.f, 0.f, 0.f}, ym[8];
#pragma unroll
    for (int t = 0; t < 8; ++t) {
      const unsigned ph = t < 4 ? h0[t & 3] : h1[t & 3], pl = t < 4 ? l0[t & 3] : l1[t & 3];
#pragma unroll
      for (int k = 0; k < 4; ++k) {
        y[2 * k] = dot2(wo[2 * t][k], pl, dot2(wo[2 * t][k], ph, y[2 * k]));
        y[2 * k + 1] = dot2(wo[2 * t + 1][k], pl, dot2(wo[2 * t + 1][k], ph, y[2 * k + 1]));
      }
    }
#pragma unroll
    for (int k = 0; k < 4; ++k) { ym[2 * k] = hval * lo16(wd[k]); ym[2 * k + 1] = hval * hi16(wd[k]); }
    float ya, yb;
    {
      const float b0 = pair32(y[0], y[4]), b1 = pair32(y[1], y[5]), b2 = pair32(y[2], y[6]), b3 = pair32(y[3], y[7]);
      const float c0 = pair16(b0, b2), c1 = pair16(b1, b3);
      const float t1 = c0 + dpp<XOR8>(c0), t2 = c1 + dpp<XOR8>(c1);
      ya = (lane & 8) ? t2 : t1;
    }
    {
      const float b0 = pair32(ym[0], ym[4]), b1 = pair32(ym[1], ym[5]), b2 = pair32(ym[2], ym[6]), b3 = pair32(ym[3], ym[7]);
      const float c0 = pair16(b0, b2), c1 = pair16(b1, b3);
      const float t1 = c0 + dpp<XOR8>(c0), t2 = c1 + dpp<XOR8>(c1);
      yb = (lane & 8) ? t2 : t1;
    }
    const int col = 64 * w + 8 * cg + rg;
    float* ws = (float*)wsp;
    ws[WS_YA / 4 + chunk * 512 + col] = ya;
    if (has_mlp) ws[WS_YM / 4 + j * 512 + col] = yb;
    if (tid == 0) *(f32x2*)(ws + WS_ML / 4 + 2 * chunk) = f32x2{M_, L_};
  }
}

__global__ void __launch_bounds__(256) attn_final(const void* xp, const void* wsp, void* outp) {
  __shared__ float wt[256];
  __shared__ __attribute__((aligned(16))) float red[2][64][16];
  const int tid = threadIdx.x, g = blockIdx.x;
  const float* ws = (const float*)wsp;
  const int p = tid >> 2, q4 = tid & 3;
  f32x4 ya[4], ym[4];
#pragma unroll
  for (int i = 0; i < 4; ++i) ya[i] = *(const f32x4*)(ws + WS_YA / 4 + (p + 64 * i) * 512 + 16 * g + 4 * q4);
#pragma unroll
  for (int i = 0; i < 4; ++i) { const int row = p + 64 * i; ym[i] = row < NMLP ? *(const f32x4*)(ws + WS_YM / 4 + row * 512 + 16 * g + 4 * q4) : f32x4{0.f, 0.f, 0.f, 0.f}; }
  {
    const f32x4* ml = (const f32x4*)(ws + WS_ML / 4 + (tid >> 3) * 16);
    f32x4 v[4];
#pragma unroll
    for (int i = 0; i < 4; ++i) v[i] = ml[i];
    float M = v[0][0];
#pragma unroll
    for (int i = 0; i < 4; ++i) M = fmaxf(M, fmaxf(v[i][0], v[i][2]));
    float L = 0.f, mine = 0.f;
#pragma unroll
    for (int i = 0; i < 4; ++i) {
      const float f0 = __builtin_amdgcn_exp2f((v[i][0] - M) * 1.4426950408889634f), f1 = __builtin_amdgcn_exp2f((v[i][2] - M) * 1.4426950408889634f);
      L = fmaf(f0, v[i][1], L); L = fmaf(f1, v[i][3], L);
      if ((tid & 7) == 2 * i) mine = f0;
      if ((tid & 7) == 2 * i + 1) mine = f1;
    }
    wt[tid] = mine / L;
  }
  __syncthreads();
  f32x4 a = {0.f, 0.f, 0.f, 0.f}, m = {0.f, 0.f, 0.f, 0.f};
#pragma unroll
  for (int i = 0; i < 4; ++i) { a += ya[i] * wt[p + 64 * i]; m += ym[i]; }
  *(f32x4*)&red[0][p][4 * q4] = a;
  *(f32x4*)&red[1][p][4 * q4] = m;
  __syncthreads();
  if (tid < 16) {
    float sa = 0.f, sm = 0.f;
    for (int i = 0; i < 64; ++i) { sa += red[0][i][tid]; sm += red[1][i][tid]; }
    const float xv = lo16(((const unsigned short*)xp)[16 * g + tid]);
    const float o = rbf(rbf(xv + rbf(sa)) + rbf(sm));
    ((unsigned short*)outp)[16 * g + tid] = (unsigned short)(cvtpk(o, o) & 0xffffu);
  }
}

extern "C" void kernel_launch(void* const* d_in, const int* in_sizes, int n_in, void* d_out, int out_size, void* d_ws, size_t ws_size,
                              hipStream_t stream) {
  attn_main<<<NWG, WAVES * 64, 0, stream>>>(d_in[0], d_in[1], d_in[2], d_in[3], d_in[4], d_in[6], d_in[7], d_in[8], d_ws);
  attn_final<<<32, 256, 0, stream>>>(d_in[0], d_ws, d_out);
}
```

```cpp
#include <hip/hip_runtime.h>
#include <hip/hip_bf16.h>

#define WPOL ""
#define DEPTH 3
#define P0 16
#define KA 4
#define PRE_BAR 1

typedef unsigned v4u __attribute__((ext_vector_type(4)));
typedef unsigned v2u __attribute__((ext_vector_type(2)));
typedef float f32x4 __attribute__((ext_vector_type(4)));
typedef float f32x2 __attribute__((ext_vector_type(2)));
typedef __bf16 bf16x2 __attribute__((ext_vector_type(2)));
typedef __bf16 bf16x8 __attribute__((ext_vector_type(8)));
typedef unsigned long long u64;
#define GAS __attribute__((address_space(1)))
#define LAS __attribute__((address_space(3)))
#define RLX_AGENT __ATOMIC_RELAXED, __HIP_MEMORY_SCOPE_AGENT
#define STAMP(k)

constexpr int NWG = 256, WAVES = 8;
constexpr unsigned WS_EPOCH = 0, WS_FLAG = 256, WS_ML = 4096, WS_Y = 65536;
constexpr unsigned SENT = 0xffffffffu;
constexpr unsigned SPIN_CAP = 1u << 20;

template <int OFF> __device__ __forceinline__ void ld128w(v4u& dst, unsigned voff, u64 sbase) {
  asm volatile("global_load_dwordx4 %0, %1, %2 offset:%3 " WPOL : "=v"(dst) : "v"(voff), "s"(sbase), "i"(OFF) : "memory");
}
__device__ __forceinline__ void ld128(v4u& dst, unsigned voff, u64 sbase) {
  asm volatile("global_load_dwordx4 %0, %1, %2" : "=v"(dst) : "v"(voff), "s"(sbase) : "memory");
}
__device__ __forceinline__ void ld16(unsigned& dst, unsigned voff, u64 sbase) {
  asm volatile("global_load_ushort %0, %1, %2" : "=v"(dst) : "v"(voff), "s"(sbase) : "memory");
}
template <int N> __device__ __forceinline__ void waitv2(v4u& a, v4u& b) { asm volatile("s_waitcnt vmcnt(%2)" : "+v"(a), "+v"(b) : "i"(N) : "memory"); }
template <int N> __device__ __forceinline__ void waitvx(v4u& a, unsigned& b) { asm volatile("s_waitcnt vmcnt(%2)" : "+v"(a), "+v"(b) : "i"(N) : "memory"); }

#define ACLOB "a0","a1","a2","a3","a4","a5","a6","a7","a8","a9","a10","a11","a12","a13","a14","a15","a16","a17","a18","a19","a20","a21","a22","a23","a24","a25","a26","a27","a28","a29","a30","a31", \
  "a32","a33","a34","a35","a36","a37","a38","a39","a40","a41","a42","a43","a44","a45","a46","a47","a48","a49","a50","a51","a52","a53","a54","a55","a56","a57","a58","a59","a60","a61","a62","a63", \
  "a64","a65","a66","a67","a68","a69","a70","a71","a72","a73","a74","a75","a76","a77","a78","a79","a80","a81","a82","a83","a84","a85","a86","a87","a88","a89","a90","a91","a92","a93","a94","a95", \
  "a96","a97","a98","a99","a100","a101","a102","a103","a104","a105","a106","a107","a108","a109","a110","a111","a112","a113","a114","a115"
template <int SLOT, int OFF> __device__ __forceinline__ void ald(unsigned voff, u64 sbase) {
  asm volatile("global_load_dwordx4 a[%c2:%c3], %0, %1 offset:%c4 nt" :: "v"(voff), "s"(sbase), "i"(4 * SLOT), "i"(4 * SLOT + 3), "i"(OFF) : "memory", ACLOB);
}
template <int SLOT, int OFF> __device__ __forceinline__ void aldw(unsigned voff, u64 sbase) {
  asm volatile("global_load_dwordx4 a[%c2:%c3], %0, %1 offset:%c4 " WPOL :: "v"(voff), "s"(sbase), "i"(4 * SLOT), "i"(4 * SLOT + 3), "i"(OFF) : "memory", ACLOB);
}
__device__ __forceinline__ void ald_gu(unsigned voff, u64 gbase, u64 ubase, int idx) {
  const u64 mask = 0xffull << (8 * idx);
  asm volatile("s_mov_b64 exec, %3\n\tglobal_load_dwordx4 a[100:103], %0, %1 " WPOL "\n\tglobal_load_dwordx4 a[104:107], %0, %2 " WPOL "\n\ts_mov_b64 exec, -1"
               :: "v"(voff), "s"(gbase), "s"(ubase), "s"(mask) : "memory", ACLOB);
}
template <int REG> __device__ __forceinline__ void ald64sc1(unsigned voff, u64 sbase) {
  asm volatile("global_load_dwordx2 a[%c2:%c3], %0, %1 sc1" :: "v"(voff), "s"(sbase), "i"(REG), "i"(REG + 1) : "memory", ACLOB);
}
template <int RSLOT, int N> __device__ __forceinline__ v4u aget(void) {
  unsigned a, b, c, d;
  asm volatile("s_waitcnt vmcnt(%c4)\n\tv_accvgpr_read_b32 %0, a%c5\n\tv_accvgpr_read_b32 %1, a%c6\n\tv_accvgpr_read_b32 %2, a%c7\n\tv_accvgpr_read_b32 %3, a%c8"
               : "=v"(a), "=v"(b), "=v"(c), "=v"(d) : "i"(N), "i"(4 * RSLOT), "i"(4 * RSLOT + 1), "i"(4 * RSLOT + 2), "i"(4 * RSLOT + 3) : "memory", ACLOB);
  return v4u{a, b, c, d};
}
template <int RSLOT, int N, int WSLOT, int OFF> __device__ __forceinline__ v4u astep(unsigned voff, u64 sbase) {
  unsigned a, b, c, d;
  asm volatile("s_waitcnt vmcnt(%c6)\n\tglobal_load_dwordx4 a[%c11:%c12], %4, %5 offset:%c13 nt\n\t"
               "v_accvgpr_read_b32 %0, a%c7\n\tv_accvgpr_read_b32 %1, a%c8\n\tv_accvgpr_read_b32 %2, a%c9\n\tv_accvgpr_read_b32 %3, a%c10"
               : "=&v"(a), "=&v"(b), "=&v"(c), "=&v"(d)
               : "v"(voff), "s"(sbase), "i"(N), "i"(4 * RSLOT), "i"(4 * RSLOT + 1), "i"(4 * RSLOT + 2), "i"(4 * RSLOT + 3), "i"(4 * WSLOT), "i"(4 * WSLOT + 3), "i"(OFF)
               : "memory", ACLOB);
  return v4u{a, b, c, d};
}
template <int RSLOT> __device__ __forceinline__ v4u aread(void) {
  unsigned a, b, c, d;
  asm volatile("v_accvgpr_read_b32 %0, a%c4\n\tv_accvgpr_read_b32 %1, a%c5\n\tv_accvgpr_read_b32 %2, a%c6\n\tv_accvgpr_read_b32 %3, a%c7\n\ts_nop 1"
               : "=v"(a), "=v"(b), "=v"(c), "=v"(d) : "i"(4 * RSLOT), "i"(4 * RSLOT + 1), "i"(4 * RSLOT + 2), "i"(4 * RSLOT + 3) : "memory", ACLOB);
  return v4u{a, b, c, d};
}
__device__ __forceinline__ void st128sc1(unsigned voff, u64 sbase, const v4u& v) {
  asm volatile("global_store_dwordx4 %0, %1, %2 sc1\n\ts_nop 1" :: "v"(voff), "v"(v), "s"(sbase) : "memory");
}

__device__ __forceinline__ void st64sc1(unsigned voff, u64 sbase, unsigned lo, unsigned hi) {
  const v2u v = {lo, hi};
  asm volatile("global_store_dwordx2 %0, %1, %2 sc1" :: "v"(voff), "v"(v), "s"(sbase) : "memory");
}
__device__ __forceinline__ void st16(unsigned voff, u64 sbase, unsigned v) {
  asm volatile("global_store_short %0, %1, %2" :: "v"(voff), "v"(v), "s"(sbase) : "memory");
}
__device__ __forceinline__ v2u ld64sc1_wait(unsigned voff, u64 sbase) {
  v2u v;
  asm volatile("global_load_dwordx2 %0, %1, %2 sc1\n\ts_waitcnt vmcnt(0)" : "=&v"(v) : "v"(voff), "s"(sbase) : "memory");
  return v;
}

__device__ __forceinline__ float dot2(unsigned a, unsigned b, float acc) {
  return __builtin_amdgcn_fdot2_f32_bf16(__builtin_bit_cast(bf16x2, a), __builtin_bit_cast(bf16x2, b), acc, false);
}
__device__ __forceinline__ float lo16(unsigned v) { return __builtin_bit_cast(float, v << 16); }
__device__ __forceinline__ float hi16(unsigned v) { return __builtin_bit_cast(float, v & 0xffff0000u); }
template <int CTRL> __device__ __forceinline__ float dpp(float x) {
  return __builtin_bit_cast(float, __builtin_amdgcn_mov_dpp(__builtin_bit_cast(int, x), CTRL, 0xf, 0xf, true));
}
template <int CTRL> __device__ __forceinline__ unsigned dppu(unsigned x) { return (unsigned)__builtin_amdgcn_mov_dpp((int)x, CTRL, 0xf, 0xf, true); }
constexpr int XOR1 = 0xB1, XOR2 = 0x4E, XOR7 = 0x141, XOR8 = 0x128, ROR4 = 0x124;
__device__ __forceinline__ unsigned cvtpk(float lo, float hi) {
  const bf16x2 v = {(__bf16)lo, (__bf16)hi};
  return __builtin_bit_cast(unsigned, v);
}
__device__ __forceinline__ float rbf(float x) { return lo16(cvtpk(x, x)); }
__device__ __forceinline__ float pair32(float a, float b) {
  auto s = __builtin_amdgcn_permlane32_swap(__float_as_uint(a), __float_as_uint(b), false, false);
  return __uint_as_float(s[0]) + __uint_as_float(s[1]);
}
__device__ __forceinline__ float pair16(float a, float b) {
  auto s = __builtin_amdgcn_permlane16_swap(__float_as_uint(a), __float_as_uint(b), false, false);
  return __uint_as_float(s[0]) + __uint_as_float(s[1]);
}
__device__ __forceinline__ float wave_sum(float x) {
  x += dpp<XOR1>(x); x += dpp<XOR2>(x); x += dpp<XOR7>(x); x += dpp<XOR8>(x);
  x = pair16(x, x); x = pair32(x, x); return x;
}
__device__ __forceinline__ float wave_max(float x) {
  x = fmaxf(x, dpp<XOR1>(x)); x = fmaxf(x, dpp<XOR2>(x)); x = fmaxf(x, dpp<XOR7>(x)); x = fmaxf(x, dpp<XOR8>(x));
  auto s = __builtin_amdgcn_permlane16_swap(__float_as_uint(x), __float_as_uint(x), false, false);
  x = fmaxf(__uint_as_float(s[0]), __uint_as_float(s[1]));
  auto t = __builtin_amdgcn_permlane32_swap(__float_as_uint(x), __float_as_uint(x), false, false);
  return fmaxf(__uint_as_float(t[0]), __uint_as_float(t[1]));
}
__device__ __forceinline__ float treduce16(float (&x)[16], int c) {
#define TR_STEP(HALF, CTRL, BIT) _Pragma("unroll") for (int h = 0; h < (HALF); ++h) { \
    const float lo_ = x[h], hi_ = x[h + (HALF)]; const float t1_ = lo_ + dpp<CTRL>(lo_), t2_ = hi_ + dpp<CTRL>(hi_); x[h] = (c & (BIT)) ? t2_ : t1_; }
  TR_STEP(8, XOR8, 8) TR_STEP(4, XOR7, 4) TR_STEP(2, XOR2, 2) TR_STEP(1, XOR1, 1)
#undef TR_STEP
  return x[0];
}
template <int T> __device__ __forceinline__ float bcast(float p) { return dpp<0x150 + T>(p); }
__device__ __forceinline__ void fma8(float (&acc)[8], float p, const v4u& v) {
#pragma unroll
  for (int k = 0; k < 4; ++k) { acc[2 * k] = fmaf(p, lo16(v[k]), acc[2 * k]); acc[2 * k + 1] = fmaf(p, hi16(v[k]), acc[2 * k + 1]); }
}
__device__ __forceinline__ unsigned plo(unsigned hi_row, unsigned lo_row) { return __builtin_amdgcn_perm(hi_row, lo_row, 0x05040100u); }
__device__ __forceinline__ unsigned phi(unsigned hi_row, unsigned lo_row) { return __builtin_amdgcn_perm(hi_row, lo_row, 0x07060302u); }

struct Stream {
  u64 K, V; unsigned voff; int rot;
  __device__ __forceinline__ u64 base(int round) const { return ((round < 32) ? K : V) + (u64)(unsigned)(((round + rot) & 31) << 15); }
  template <int J, int SLOT> __device__ __forceinline__ void issue(int round0) const { ald<SLOT, (J & 3) * 1024>(voff, base(round0 + (J >> 2))); }
  template <int U, int EXTRA> __device__ __forceinline__ v4u step(int round0) const {
    return astep<(U & 7), DEPTH - 1 + EXTRA, ((U + DEPTH) & 7), ((U + DEPTH) & 3) * 1024>(voff, base(round0 + ((U + DEPTH) >> 2)));
  }
};
__device__ __forceinline__ float qk(const v4u& k, const v4u& q) { return dot2(k[3], q[3], dot2(k[2], q[2], dot2(k[1], q[1], dot2(k[0], q[0], 0.f)))); }
#define REP16(M) M(0) M(1) M(2) M(3) M(4) M(5) M(6) M(7) M(8) M(9) M(10) M(11) M(12) M(13) M(14) M(15)
#define REP15(M) M(1) M(2) M(3) M(4) M(5) M(6) M(7) M(8) M(9) M(10) M(11) M(12) M(13) M(14) M(15)
#define REP8(M) M(0) M(1) M(2) M(3) M(4) M(5) M(6) M(7)
#define BARRIER() asm volatile("s_waitcnt lgkmcnt(0)\n\ts_barrier" ::: "memory")

__device__ __forceinline__ float fin_score(float d) { return rbf(rbf(d) * 0.08838834764831845f); }
constexpr float LOG2E = 1.4426950408889634f;

__global__ void __launch_bounds__(WAVES * 64, 2) fused_decode(const void* xp, const void* Kp, const void* Vp, const void* wqp, const void* wop,
                                                             const void* wgp, const void* wup, const void* wdp, void* wsp, void* outp) {
  __shared__ __attribute__((aligned(16))) float lds[8 * 128 + 8 * 16 + 8 * 68 + 8 * 16 + 4];
  float* Lq = lds;
  float* Lg = lds + 1024;
  unsigned* Lo = (unsigned*)(lds + 1024 + 128);
  float* Lr = lds + 1024 + 128 + 8 * 68;
  volatile LAS unsigned* Lcnt = (volatile LAS unsigned*)(Lr + 128);
  STAMP(0)
  const int tid = threadIdx.x, lane = tid & 63, w = __builtin_amdgcn_readfirstlane(tid >> 6), r = lane >> 4, c = lane & 15;
  const int b = blockIdx.x, xcd = b & 7, i8 = b >> 3;
  const int head = xcd * 4 + (i8 & 3), split = i8 >> 2, chunk = head * 8 + split;
  const bool has_mlp = i8 < 28;
  const int j = xcd * 28 + (has_mlp ? i8 : 27);
  const u64 ws = (u64)wsp;
  const u64 mlb = ws + WS_ML + head * 512;

  v4u x8; unsigned xs; v4u wq[16];
  ld128(x8, lane * 16, (u64)xp);
  ld16(xs, (4 * c + r) * 2, (u64)xp + w * 128);
  {
    const unsigned vo = r * 8192 + c * 16; const u64 sb = (u64)wqp + (u64)w * (64 * 8192) + head * 256;
#define M(T) ld128w<0>(wq[T], vo, sb + (u64)(T) * (4 * 8192));
    REP16(M)
#undef M
  }
  Stream s;
  { const u64 co = ((u64)chunk << 20) + (u64)(w << 12); s.K = (u64)Kp + co; s.V = (u64)Vp + co; s.voff = lane * 16; s.rot = b & 31; }
  asm volatile("s_barrier" ::: "memory");
#define M(T) if (T < KA) s.issue<T, T>(0);
  REP16(M)
#undef M
  waitvx<16 + KA>(x8, xs);
  float xn;
  {
    float xv[8];
#pragma unroll
    for (int k = 0; k < 4; ++k) { xv[2 * k] = lo16(x8[k]); xv[2 * k + 1] = hi16(x8[k]); }
    float sum = 0.f;
#pragma unroll
    for (int k = 0; k < 8; ++k) sum += xv[k];
    const float mean = wave_sum(sum) * (1.f / 512.f);
    float sq = 0.f;
#pragma unroll
    for (int k = 0; k < 8; ++k) { const float dlt = xv[k] - mean; sq = fmaf(dlt, dlt, sq); }
    const float var = wave_sum(sq) * (1.f / 512.f);
    xn = rbf(rbf(lo16(xs) - rbf(mean)) / rbf(sqrtf(rbf(rbf(var) + 1e-5f))));
  }
  STAMP(1)
  {
    const unsigned xb = __float_as_uint(xn);
    const unsigned xpair = phi(dppu<XOR1>(xb), xb);
    float a[8] = {0.f, 0.f, 0.f, 0.f, 0.f, 0.f, 0.f, 0.f};
#define KISS(T) (KA + 2 * (T) < P0 ? KA + 2 * (T) : P0)
#define M(T) { waitv2<14 - 2 * T + KISS(T)>(wq[2 * T], wq[2 * T + 1]); \
      if (KA + 2 * T < P0) s.issue<(KA + 2 * T) & 15, (KA + 2 * T) & 15>(0); if (KA + 2 * T + 1 < P0) s.issue<(KA + 2 * T + 1) & 15, (KA + 2 * T + 1) & 15>(0); \
      const unsigned xx = dppu<0x150 + 2 * T>(xpair); \
      _Pragma("unroll") for (int k = 0; k < 4; ++k) { a[2 * k] = dot2(plo(wq[2 * T + 1][k], wq[2 * T][k]), xx, a[2 * k]); a[2 * k + 1] = dot2(phi(wq[2 * T + 1][k], wq[2 * T][k]), xx, a[2 * k + 1]); } \
      __builtin_amdgcn_sched_barrier(0); }
    REP8(M)
#undef M
    static_assert(KA + 16 >= P0 && (KA & 1) == 0, "all P0 loads issued by the end of the q loop");
    const float b0 = pair32(a[0], a[4]), b1 = pair32(a[1], a[5]), b2 = pair32(a[2], a[6]), b3 = pair32(a[3], a[7]);
    const float c0 = pair16(b0, b2), c1 = pair16(b1, b3);
    *(f32x2*)(Lq + w * 128 + 8 * c + 2 * r) = f32x2{c0, c1};
  }
  if (tid == 0) *Lcnt = 0u;
  BARRIER();
  v4u q;
  {
    f32x4 a0 = *(const f32x4*)(Lq + (2 * r) * 128 + 8 * c), a1 = *(const f32x4*)(Lq + (2 * r) * 128 + 8 * c + 4);
    f32x4 b0 = *(const f32x4*)(Lq + (2 * r + 1) * 128 + 8 * c), b1 = *(const f32x4*)(Lq + (2 * r + 1) * 128 + 8 * c + 4);
    a0 += b0; a1 += b1;
    float v[8] = {a0[0], a0[1], a0[2], a0[3], a1[0], a1[1], a1[2], a1[3]};
#pragma unroll
    for (int k = 0; k < 8; ++k) { v[k] = pair16(v[k], v[k]); v[k] = pair32(v[k], v[k]); }
#pragma unroll
    for (int k = 0; k < 4; ++k) q[k] = cvtpk(v[2 * k], v[2 * k + 1]);
  }
  STAMP(2)
  float sc[8];
  float d[16];
  static_assert(P0 == 16, "P0");
  const unsigned gu_vo = (4 * c + r) * 3584;
  const u64 g_sb = (u64)wgp + (u64)w * (64 * 3584) + j * 16, u_sb = (u64)wup + (u64)w * (64 * 3584) + j * 16;
  ald_gu(gu_vo, g_sb, u_sb, 0);
#define M(U) { const v4u k = aget<U, (15 - U) + 2 + (U < DEPTH ? U : DEPTH)>(); if (U < DEPTH) s.issue<16 + U, (U & 7)>(0); d[U] = qk(k, q); }
  REP16(M)
#undef M
  sc[7] = fin_score(treduce16(d, c));
  STAMP(5)
  unsigned tag_lo = 0, tag_hi = 0;
  if (!has_mlp && w == 0) {
    const v4u sv = {SENT, SENT, SENT, SENT};
    for (int i = 0; i < 16; ++i) st128sc1(lane * 16 + i * 1024, ws + WS_Y + head * 16384, sv);
    if (lane < 32) st128sc1(lane * 16, mlb, sv);
  }
  const int mg = lane >> 4, mn = lane & 15, cg = lane & 7, rowsel = (lane >> 3) & 1;
  const unsigned wo_vo = (64 * rowsel + 8 * mg) * 1024 + cg * 16;
  const u64 wo_sb = (u64)wop + (u64)head * (128 * 1024) + w * 128;
  for (int tile = 1; tile < 8; ++tile) {
    d[0] = qk(s.step<0, 0>(tile * 4), q);
    switch (tile) {
      case 1: ald64sc1<108>(0u, ws + WS_EPOCH); aldw<8, 0>(wo_vo, wo_sb); aldw<9, 1024>(wo_vo, wo_sb); break;
      case 2: aldw<10, 2048>(wo_vo, wo_sb); aldw<11, 3072>(wo_vo, wo_sb); aldw<12, 0>(wo_vo, wo_sb + 4096); break;
      case 3: aldw<13, 1024>(wo_vo, wo_sb + 4096); aldw<14, 2048>(wo_vo, wo_sb + 4096); aldw<15, 3072>(wo_vo, wo_sb + 4096); break;
      case 4: aldw<16, 0>(wo_vo, wo_sb + 32768); aldw<17, 1024>(wo_vo, wo_sb + 32768); aldw<18, 2048>(wo_vo, wo_sb + 32768); break;
      case 5: aldw<19, 3072>(wo_vo, wo_sb + 32768); aldw<20, 0>(wo_vo, wo_sb + 36864); aldw<21, 1024>(wo_vo, wo_sb + 36864); break;
      case 6: aldw<22, 2048>(wo_vo, wo_sb + 36864); aldw<23, 3072>(wo_vo, wo_sb + 36864);
              aldw<24, 0>((lane >> 3) * 1024 + cg * 16, (u64)wdp + (u64)j * 8192 + w * 128); break;
      default: ald64sc1<110>((lane & 31) * 8, ws + WS_FLAG); ald64sc1<112>(0u, ws + WS_FLAG); ald64sc1<114>(0u, ws + WS_FLAG); break;
    }
#define M(U) d[U] = qk(s.step<U, (U <= DEPTH ? 3 : (U > 8 && U <= 8 + DEPTH) ? 2 : 0)>(tile * 4), q); if (U == 8) ald_gu(gu_vo, g_sb, u_sb, tile);
    REP15(M)
#undef M
#pragma unroll
    for (int k = 0; k < 7; ++k) sc[k] = sc[k + 1];
    sc[7] = fin_score(treduce16(d, c));
    if (tile == 1) {
      const v4u e = aread<27>();
      const u64 tag = (((u64)(unsigned)__builtin_amdgcn_readfirstlane(e[1]) << 32) | (u64)(unsigned)__builtin_amdgcn_readfirstlane(e[0])) + 1ull;
      tag_lo = (unsigned)tag; tag_hi = (unsigned)(tag >> 32);
      if (!has_mlp && w == 0 && lane == 0) st64sc1(0u, ws + WS_FLAG + head * 8, tag_lo, tag_hi);
      STAMP(6)
    }
  }
  {
    const v4u wg = aread<25>(), wu = aread<26>();
    float g[16];
#pragma unroll
    for (int k = 0; k < 4; ++k) { g[2 * k] = xn * lo16(wg[k]); g[2 * k + 1] = xn * hi16(wg[k]); g[8 + 2 * k] = xn * lo16(wu[k]); g[8 + 2 * k + 1] = xn * hi16(wu[k]); }
    float t = treduce16(g, c);
    t = pair16(t, t); t = pair32(t, t);
    if (r == 0) Lg[w * 16 + c] = t;
    asm volatile("s_waitcnt lgkmcnt(0)" ::: "memory");
    if (lane == 0) __hip_atomic_fetch_add((LAS unsigned*)Lcnt, 1u, __ATOMIC_RELAXED, __HIP_MEMORY_SCOPE_WORKGROUP);
  }
  STAMP(3)
  float mw;
  {
    float m = sc[0];
#pragma unroll
    for (int k = 1; k < 8; ++k) m = fmaxf(m, sc[k]);
    mw = wave_max(m);
    float l = 0.f;
#pragma unroll
    for (int k = 0; k < 8; ++k) { sc[k] = __builtin_amdgcn_exp2f((sc[k] - mw) * LOG2E); l += sc[k]; }
    const float lw = wave_sum(l);
    const v4u e = aread<27>();
    if (!__all(e[2] == tag_lo && e[3] == tag_hi)) {
      for (unsigned spins = 0; spins < SPIN_CAP; ++spins) {
        __builtin_amdgcn_s_sleep(2);
        const v2u f = ld64sc1_wait((lane & 31) * 8, ws + WS_FLAG);
        if (__all(f[0] == tag_lo && f[1] == tag_hi)) break;
      }
    }
    if (lane == 0) st64sc1(0u, mlb + (split * 8 + w) * 8, __float_as_uint(mw) & 0xfffffffeu, __float_as_uint(lw) & 0xfffffffeu);
  }
  float acc[8] = {0.f, 0.f, 0.f, 0.f, 0.f, 0.f, 0.f, 0.f};
  for (int tile = 8; tile < 15; ++tile) {
    const float p = sc[0];
    if (tile == 14) ald64sc1<112>(lane * 8, mlb);
#define M(U) fma8(acc, bcast<U>(p), s.step<U, 0>(tile * 4));
    REP16(M)
#undef M
#pragma unroll
    for (int k = 0; k < 7; ++k) sc[k] = sc[k + 1];
  }
  v4u bf[16];
  float scale, ymlp[4];
  {
    const float p = sc[0];
#define M(U) if (U >= DEPTH) s.issue<U, U>(60);
    REP8(M)
#undef M
    {
      const v4u e = aread<28>();
      unsigned mb = e[0], lb = e[1];
      if (!__all(mb != SENT && lb != SENT)) {
        for (unsigned spins = 0; spins < SPIN_CAP; ++spins) {
          __builtin_amdgcn_s_sleep(2);
          const v2u t = ld64sc1_wait(lane * 8, mlb);
          mb = t[0]; lb = t[1];
          if (__all(mb != SENT && lb != SENT)) break;
        }
      }
      const float mi = __uint_as_float(mb), li = __uint_as_float(lb);
      const float M = wave_max(mi);
      const float L = wave_sum(li * __builtin_amdgcn_exp2f((mi - M) * LOG2E));
      scale = __builtin_amdgcn_exp2f((mw - M) * LOG2E) / L;
    }
#define M(T) { const v4u A = aread<8 + 2 * T>(), B = aread<9 + 2 * T>(); _Pragma("unroll") for (int k = 0; k < 4; ++k) { \
      bf[(T >> 2) * 8 + 2 * k][T & 3] = plo(B[k], A[k]); bf[(T >> 2) * 8 + 2 * k + 1][T & 3] = phi(B[k], A[k]); } }
    REP8(M)
#undef M
    {
      for (unsigned spins = 0; *Lcnt != 8u && spins < SPIN_CAP; ++spins) __builtin_amdgcn_s_sleep(1);
      asm volatile("" ::: "memory");
      const int f = lane >> 3;
      float g = 0.f, u = 0.f;
#pragma unroll
      for (int ww = 0; ww < 8; ++ww) { g += Lg[ww * 16 + f]; u += Lg[ww * 16 + 8 + f]; }
      g = rbf(g); u = rbf(u);
      float hval = rbf(rbf(1.f / (1.f + __expf(-g))) * u);
      if (!has_mlp) hval = 0.f;
      const v4u wd = aread<24>();
      float ym[8];
#pragma unroll
      for (int k = 0; k < 4; ++k) { ym[2 * k] = hval * lo16(wd[k]); ym[2 * k + 1] = hval * hi16(wd[k]); }
#pragma unroll
      for (int k = 0; k < 4; ++k) { float t = pair32(ym[k], ym[k + 4]); t = pair16(t, t); ymlp[k] = t + dpp<XOR8>(t); asm volatile("" : "+v"(ymlp[k])); }
    }
#define M(U) fma8(acc, bcast<U>(p), aget<U, 7>()); s.issue<8 + U, U>(60);
    REP8(M)
#undef M
#define M(U) fma8(acc, bcast<8 + U>(p), aget<U, 7 - U>());
    REP8(M)
#undef M
  }
  STAMP(4)
  {
    const float b0 = pair32(acc[0], acc[4]), b1 = pair32(acc[1], acc[5]), b2 = pair32(acc[2], acc[6]), b3 = pair32(acc[3], acc[7]);
    const float c0 = pair16(b0, b2), c1 = pair16(b1, b3);
    Lo[w * 68 + 4 * c + r] = cvtpk(c0 * scale, c1 * scale);
  }
  BARRIER();
  {
    const unsigned* ap = Lo + (mn & 7) * 68 + 32 * (mn >> 3) + 4 * mg;
    const v4u a0 = *(const v4u*)ap, a1 = *(const v4u*)(ap + 16);
    float y[8];
#pragma unroll
    for (int cc = 0; cc < 8; ++cc) {
      f32x4 dd = {0.f, 0.f, 0.f, 0.f};
      dd = __builtin_amdgcn_mfma_f32_16x16x32_bf16(__builtin_bit_cast(bf16x8, a0), __builtin_bit_cast(bf16x8, bf[cc]), dd, 0, 0, 0);
      dd = __builtin_amdgcn_mfma_f32_16x16x32_bf16(__builtin_bit_cast(bf16x8, a1), __builtin_bit_cast(bf16x8, bf[8 + cc]), dd, 0, 0, 0);
      const float t = (dd[0] + dd[1]) + (dd[2] + dd[3]);
      y[cc] = __builtin_bit_cast(float, __builtin_amdgcn_update_dpp(__builtin_bit_cast(int, t), __builtin_bit_cast(int, t), XOR8, 0xc, 0xf, false));
    }
    v4u o;
#pragma unroll
    for (int k = 0; k < 4; ++k) { float t = pair32(y[k], y[k + 4]); t = pair16(t, t); o[k] = __float_as_uint(t + ymlp[k]) & 0xfffffffeu; }
    if ((lane & 24) == 0) st128sc1((4 * w + (cg >> 1)) * 16384 + chunk * 64 + (cg & 1) * 32 + (lane >> 5) * 16, ws + WS_Y, o);
  }
  if (!has_mlp) {
    unsigned xv;
    ld16(xv, (lane & 15) * 2, (u64)xp + head * 32);
    const u64 yb = ws + WS_Y + head * 16384 + w * 2048;
    v4u y0, y1;
    for (unsigned spins = 0; spins < SPIN_CAP; ++spins) {
      asm volatile("global_load_dwordx4 %0, %3, %4 sc1\n\tglobal_load_dwordx4 %1, %3, %4 offset:1024 sc1\n\ts_waitcnt vmcnt(0)"
                   : "=&v"(y0), "=&v"(y1), "+v"(xv) : "v"(lane * 16), "s"(yb) : "memory");
      bool ok = true;
#pragma unroll
      for (int k = 0; k < 4; ++k) ok = ok && (y0[k] != SENT) && (y1[k] != SENT);
      if (__all(ok)) break;
    }
    float v[4];
#pragma unroll
    for (int k = 0; k < 4; ++k) v[k] = __uint_as_float(y0[k]) + __uint_as_float(y1[k]);
    float t = pair16(pair32(v[0], v[2]), pair32(v[1], v[3]));
    t += dpp<XOR8>(t); t += dpp<ROR4>(t);
    if ((lane & 12) == 0) Lr[w * 16 + 4 * (lane & 3) + (lane >> 4)] = t;
    BARRIER();
    if (w == 0 && lane < 16) {
      float sum = 0.f;
#pragma unroll
      for (int ww = 0; ww < 8; ++ww) sum += Lr[ww * 16 + lane];
      const float o = lo16(xv) + sum;
      st16(lane * 2, (u64)outp + head * 32, cvtpk(o, o));
      if (head == 0 && lane == 0) st64sc1(0u, ws + WS_EPOCH, tag_lo, tag_hi);
    }
  }
}

extern "C" void kernel_launch(void* const* d_in, const int* in_sizes, int n_in, void* d_out, int out_size, void* d_ws, size_t ws_size,
                              hipStream_t stream) {
  fused_decode<<<NWG, WAVES * 64, 0, stream>>>(d_in[0], d_in[1], d_in[2], d_in[3], d_in[4], d_in[6], d_in[7], d_in[8], d_ws, d_out);
}
```

```cpp
#include <hip/hip_runtime.h>
#include <hip/hip_bf16.h>

#define WPOL ""
#define KPOL "nt"
#define VPOL "nt"
#define DEPTH 3
#define P0 16
#define KA 4
#define PRE_BAR 1

typedef unsigned v4u __attribute__((ext_vector_type(4)));
typedef unsigned v2u __attribute__((ext_vector_type(2)));
typedef float f32x4 __attribute__((ext_vector_type(4)));
typedef float f32x2 __attribute__((ext_vector_type(2)));
typedef __bf16 bf16x2 __attribute__((ext_vector_type(2)));
typedef __bf16 bf16x8 __attribute__((ext_vector_type(8)));
typedef unsigned long long u64;
#define GAS __attribute__((address_space(1)))
#define LAS __attribute__((address_space(3)))
#define RLX_AGENT __ATOMIC_RELAXED, __HIP_MEMORY_SCOPE_AGENT
#define STAMP(k)

constexpr int NWG = 256, WAVES = 8;
constexpr unsigned WS_EPOCH = 0, WS_FLAG = 256, WS_ML = 4096, WS_Y = 65536;
constexpr unsigned SENT = 0xffffffffu;
constexpr unsigned SPIN_CAP = 1u << 20;

template <int OFF> __device__ __forceinline__ void ld128w(v4u& dst, unsigned voff, u64 sbase) {
  asm volatile("global_load_dwordx4 %0, %1, %2 offset:%3 " WPOL : "=v"(dst) : "v"(voff), "s"(sbase), "i"(OFF) : "memory");
}
__device__ __forceinline__ void ld128(v4u& dst, unsigned voff, u64 sbase) {
  asm volatile("global_load_dwordx4 %0, %1, %2" : "=v"(dst) : "v"(voff), "s"(sbase) : "memory");
}
__device__ __forceinline__ void ld16(unsigned& dst, unsigned voff, u64 sbase) {
  asm volatile("global_load_ushort %0, %1, %2" : "=v"(dst) : "v"(voff), "s"(sbase) : "memory");
}
template <int N> __device__ __forceinline__ void waitv2(v4u& a, v4u& b) { asm volatile("s_waitcnt vmcnt(%2)" : "+v"(a), "+v"(b) : "i"(N) : "memory"); }
template <int N> __device__ __forceinline__ void waitvx(v4u& a, unsigned& b) { asm volatile("s_waitcnt vmcnt(%2)" : "+v"(a), "+v"(b) : "i"(N) : "memory"); }

#define ACLOB "a0","a1","a2","a3","a4","a5","a6","a7","a8","a9","a10","a11","a12","a13","a14","a15","a16","a17","a18","a19","a20","a21","a22","a23","a24","a25","a26","a27","a28","a29","a30","a31", \
  "a32","a33","a34","a35","a36","a37","a38","a39","a40","a41","a42","a43","a44","a45","a46","a47","a48","a49","a50","a51","a52","a53","a54","a55","a56","a57","a58","a59","a60","a61","a62","a63", \
  "a64","a65","a66","a67","a68","a69","a70","a71","a72","a73","a74","a75","a76","a77","a78","a79","a80","a81","a82","a83","a84","a85","a86","a87","a88","a89","a90","a91","a92","a93","a94","a95", \
  "a96","a97","a98","a99","a100","a101","a102","a103","a104","a105","a106","a107","a108","a109","a110","a111","a112","a113","a114","a115"
template <int PH, int SLOT, int OFF> __device__ __forceinline__ void ald(unsigned voff, u64 sbase) {
  if constexpr (PH == 0) asm volatile("global_load_dwordx4 a[%c2:%c3], %0, %1 offset:%c4 " KPOL :: "v"(voff), "s"(sbase), "i"(4 * SLOT), "i"(4 * SLOT + 3), "i"(OFF) : "memory", ACLOB);
  else asm volatile("global_load_dwordx4 a[%c2:%c3], %0, %1 offset:%c4 " VPOL :: "v"(voff), "s"(sbase), "i"(4 * SLOT), "i"(4 * SLOT + 3), "i"(OFF) : "memory", ACLOB);
}
template <int SLOT, int OFF> __device__ __forceinline__ void aldw(unsigned voff, u64 sbase) {
  asm volatile("global_load_dwordx4 a[%c2:%c3], %0, %1 offset:%c4 " WPOL :: "v"(voff), "s"(sbase), "i"(4 * SLOT), "i"(4 * SLOT + 3), "i"(OFF) : "memory", ACLOB);
}
__device__ __forceinline__ void ald_gu(unsigned voff, u64 gbase, u64 ubase, int idx) {
  const u64 mask = 0xffull << (8 * idx);
  asm volatile("s_mov_b64 exec, %3\n\tglobal_load_dwordx4 a[100:103], %0, %1 " WPOL "\n\tglobal_load_dwordx4 a[104:107], %0, %2 " WPOL "\n\ts_mov_b64 exec, -1"
               :: "v"(voff), "s"(gbase), "s"(ubase), "s"(mask) : "memory", ACLOB);
}
template <int REG> __device__ __forceinline__ void ald64sc1(unsigned voff, u64 sbase) {
  asm volatile("global_load_dwordx2 a[%c2:%c3], %0, %1 sc1" :: "v"(voff), "s"(sbase), "i"(REG), "i"(REG + 1) : "memory", ACLOB);
}
template <int RSLOT, int N> __device__ __forceinline__ v4u aget(void) {
  unsigned a, b, c, d;
  asm volatile("s_waitcnt vmcnt(%c4)\n\tv_accvgpr_read_b32 %0, a%c5\n\tv_accvgpr_read_b32 %1, a%c6\n\tv_accvgpr_read_b32 %2, a%c7\n\tv_accvgpr_read_b32 %3, a%c8"
               : "=v"(a), "=v"(b), "=v"(c), "=v"(d) : "i"(N), "i"(4 * RSLOT), "i"(4 * RSLOT + 1), "i"(4 * RSLOT + 2), "i"(4 * RSLOT + 3) : "memory", ACLOB);
  return v4u{a, b, c, d};
}
#define ASTEP(POL) asm volatile("s_waitcnt vmcnt(%c6)\n\tglobal_load_dwordx4 a[%c11:%c12], %4, %5 offset:%c13 " POL "\n\t" \
               "v_accvgpr_read_b32 %0, a%c7\n\tv_accvgpr_read_b32 %1, a%c8\n\tv_accvgpr_read_b32 %2, a%c9\n\tv_accvgpr_read_b32 %3, a%c10" \
               : "=&v"(a), "=&v"(b), "=&v"(c), "=&v"(d) \
               : "v"(voff), "s"(sbase), "i"(N), "i"(4 * RSLOT), "i"(4 * RSLOT + 1), "i"(4 * RSLOT + 2), "i"(4 * RSLOT + 3), "i"(4 * WSLOT), "i"(4 * WSLOT + 3), "i"(OFF) \
               : "memory", ACLOB)
template <int PH, int RSLOT, int N, int WSLOT, int OFF> __device__ __forceinline__ v4u astep(unsigned voff, u64 sbase) {
  unsigned a, b, c, d;
  if constexpr (PH == 0) ASTEP(KPOL); else ASTEP(VPOL);
  return v4u{a, b, c, d};
}
template <int RSLOT> __device__ __forceinline__ v4u aread(void) {
  unsigned a, b, c, d;
  asm volatile("v_accvgpr_read_b32 %0, a%c4\n\tv_accvgpr_read_b32 %1, a%c5\n\tv_accvgpr_read_b32 %2, a%c6\n\tv_accvgpr_read_b32 %3, a%c7\n\ts_nop 1"
               : "=v"(a), "=v"(b), "=v"(c), "=v"(d) : "i"(4 * RSLOT), "i"(4 * RSLOT + 1), "i"(4 * RSLOT + 2), "i"(4 * RSLOT + 3) : "memory", ACLOB);
  return v4u{a, b, c, d};
}
__device__ __forceinline__ void st128sc1(unsigned voff, u64 sbase, const v4u& v) {
  asm volatile("global_store_dwordx4 %0, %1, %2 sc1\n\ts_nop 1" :: "v"(voff), "v"(v), "s"(sbase) : "memory");
}

__device__ __forceinline__ void st64sc1(unsigned voff, u64 sbase, unsigned lo, unsigned hi) {
  const v2u v = {lo, hi};
  asm volatile("global_store_dwordx2 %0, %1, %2 sc1" :: "v"(voff), "v"(v), "s"(sbase) : "memory");
}
__device__ __forceinline__ void st16(unsigned voff, u64 sbase, unsigned v) {
  asm volatile("global_store_short %0, %1, %2" :: "v"(voff), "v"(v), "s"(sbase) : "memory");
}
__device__ __forceinline__ v2u ld64sc1_wait(unsigned voff, u64 sbase) {
  v2u v;
  asm volatile("global_load_dwordx2 %0, %1, %2 sc1\n\ts_waitcnt vmcnt(0)" : "=&v"(v) : "v"(voff), "s"(sbase) : "memory");
  return v;
}

__device__ __forceinline__ float dot2(unsigned a, unsigned b, float acc) {
  return __builtin_amdgcn_fdot2_f32_bf16(__builtin_bit_cast(bf16x2, a), __builtin_bit_cast(bf16x2, b), acc, false);
}
__device__ __forceinline__ float lo16(unsigned v) { return __builtin_bit_cast(float, v << 16); }
__device__ __forceinline__ float hi16(unsigned v) { return __builtin_bit_cast(float, v & 0xffff0000u); }
template <int CTRL> __device__ __forceinline__ float dpp(float x) {
  return __builtin_bit_cast(float, __builtin_amdgcn_mov_dpp(__builtin_bit_cast(int, x), CTRL, 0xf, 0xf, true));
}
template <int CTRL> __device__ __forceinline__ unsigned dppu(unsigned x) { return (unsigned)__builtin_amdgcn_mov_dpp((int)x, CTRL, 0xf, 0xf, true); }
constexpr int XOR1 = 0xB1, XOR2 = 0x4E, XOR7 = 0x141, XOR8 = 0x128, ROR4 = 0x124;
__device__ __forceinline__ unsigned cvtpk(float lo, float hi) {
  const bf16x2 v = {(__bf16)lo, (__bf16)hi};
  return __builtin_bit_cast(unsigned, v);
}
__device__ __forceinline__ float rbf(float x) { return lo16(cvtpk(x, x)); }
__device__ __forceinline__ float pair32(float a, float b) {
  auto s = __builtin_amdgcn_permlane32_swap(__float_as_uint(a), __float_as_uint(b), false, false);
  return __uint_as_float(s[0]) + __uint_as_float(s[1]);
}
__device__ __forceinline__ float pair16(float a, float b) {
  auto s = __builtin_amdgcn_permlane16_swap(__float_as_uint(a), __float_as_uint(b), false, false);
  return __uint_as_float(s[0]) + __uint_as_float(s[1]);
}
__device__ __forceinline__ float wave_sum(float x) {
  x += dpp<XOR1>(x); x += dpp<XOR2>(x); x += dpp<XOR7>(x); x += dpp<XOR8>(x);
  x = pair16(x, x); x = pair32(x, x); return x;
}
__device__ __forceinline__ float wave_max(float x) {
  x = fmaxf(x, dpp<XOR1>(x)); x = fmaxf(x, dpp<XOR2>(x)); x = fmaxf(x, dpp<XOR7>(x)); x = fmaxf(x, dpp<XOR8>(x));
  auto s = __builtin_amdgcn_permlane16_swap(__float_as_uint(x), __float_as_uint(x), false, false);
  x = fmaxf(__uint_as_float(s[0]), __uint_as_float(s[1]));
  auto t = __builtin_amdgcn_permlane32_swap(__float_as_uint(x), __float_as_uint(x), false, false);
  return fmaxf(__uint_as_float(t[0]), __uint_as_float(t[1]));
}
__device__ __forceinline__ float treduce16(float (&x)[16], int c) {
#define TR_STEP(HALF, CTRL, BIT) _Pragma("unroll") for (int h = 0; h < (HALF); ++h) { \
    const float lo_ = x[h], hi_ = x[h + (HALF)]; const float t1_ = lo_ + dpp<CTRL>(lo_), t2_ = hi_ + dpp<CTRL>(hi_); x[h] = (c & (BIT)) ? t2_ : t1_; }
  TR_STEP(8, XOR8, 8) TR_STEP(4, XOR7, 4) TR_STEP(2, XOR2, 2) TR_STEP(1, XOR1, 1)
#undef TR_STEP
  return x[0];
}
template <int T> __device__ __forceinline__ float bcast(float p) { return dpp<0x150 + T>(p); }
__device__ __forceinline__ void fma8(float (&acc)[8], float p, const v4u& v) {
#pragma unroll
  for (int k = 0; k < 4; ++k) { acc[2 * k] = fmaf(p, lo16(v[k]), acc[2 * k]); acc[2 * k + 1] = fmaf(p, hi16(v[k]), acc[2 * k + 1]); }
}
__device__ __forceinline__ unsigned plo(unsigned hi_row, unsigned lo_row) { return __builtin_amdgcn_perm(hi_row, lo_row, 0x05040100u); }
__device__ __forceinline__ unsigned phi(unsigned hi_row, unsigned lo_row) { return __builtin_amdgcn_perm(hi_row, lo_row, 0x07060302u); }

struct Stream {
  u64 K, V; unsigned voff; int rot;
  __device__ __forceinline__ u64 base(int round) const { return ((round < 32) ? K : V) + (u64)(unsigned)(((round + rot) & 31) << 15); }
  template <int J, int SLOT, int PH = 0> __device__ __forceinline__ void issue(int round0) const { ald<PH, SLOT, (J & 3) * 1024>(voff, base(round0 + (J >> 2))); }
  template <int U, int EXTRA, int PH = 0> __device__ __forceinline__ v4u step(int round0) const {
    return astep<PH, (U & 7), DEPTH - 1 + EXTRA, ((U + DEPTH) & 7), ((U + DEPTH) & 3) * 1024>(voff, base(round0 + ((U + DEPTH) >> 2)));
  }
};
__device__ __forceinline__ float qk(const v4u& k, const v4u& q) { return dot2(k[3], q[3], dot2(k[2], q[2], dot2(k[1], q[1], dot2(k[0], q[0], 0.f)))); }
#define REP16(M) M(0) M(1) M(2) M(3) M(4) M(5) M(6) M(7) M(8) M(9) M(10) M(11) M(12) M(13) M(14) M(15)
#define REP15(M) M(1) M(2) M(3) M(4) M(5) M(6) M(7) M(8) M(9) M(10) M(11) M(12) M(13) M(14) M(15)
#define REP8(M) M(0) M(1) M(2) M(3) M(4) M(5) M(6) M(7)
#define BARRIER() asm volatile("s_waitcnt lgkmcnt(0)\n\ts_barrier" ::: "memory")

__device__ __forceinline__ float fin_score(float d) { return rbf(rbf(d) * 0.08838834764831845f); }
constexpr float LOG2E = 1.4426950408889634f;

__global__ void __launch_bounds__(WAVES * 64, 2) fused_decode(const void* xp, const void* Kp, const void* Vp, const void* wqp, const void* wop,
                                                             const void* wgp, const void* wup, const void* wdp, void* wsp, void* outp) {
  __shared__ __attribute__((aligned(16))) float lds[8 * 128 + 8 * 16 + 8 * 68 + 8 * 16 + 4];
  float* Lq = lds;
  float* Lg = lds + 1024;
  unsigned* Lo = (unsigned*)(lds + 1024 + 128);
  float* Lr = lds + 1024 + 128 + 8 * 68;
  volatile LAS unsigned* Lcnt = (volatile LAS unsigned*)(Lr + 128);
  STAMP(0)
  const int tid = threadIdx.x, lane = tid & 63, w = __builtin_amdgcn_readfirstlane(tid >> 6), r = lane >> 4, c = lane & 15;
  const int b = blockIdx.x, xcd = b & 7, i8 = b >> 3;
  const int head = xcd * 4 + (i8 & 3), split = i8 >> 2, chunk = head * 8 + split;
  const bool has_mlp = i8 < 28;
  const int j = xcd * 28 + (has_mlp ? i8 : 27);
  const u64 ws = (u64)wsp;
  const u64 mlb = ws + WS_ML + head * 512;

  v4u x8; unsigned xs; v4u wq[16];
  ld128(x8, lane * 16, (u64)xp);
  ld16(xs, (4 * c + r) * 2, (u64)xp + w * 128);
  {
    const unsigned vo = r * 8192 + c * 16; const u64 sb = (u64)wqp + (u64)w * (64 * 8192) + head * 256;
#define M(T) ld128w<0>(wq[T], vo, sb + (u64)(T) * (4 * 8192));
    REP16(M)
#undef M
  }
  Stream s;
  { const u64 co = ((u64)chunk << 20) + (u64)(w << 12); s.K = (u64)Kp + co; s.V = (u64)Vp + co; s.voff = lane * 16; s.rot = b & 31; }
  asm volatile("s_barrier" ::: "memory");
#define M(T) if (T < KA) s.issue<T, T>(0);
  REP16(M)
#undef M
  waitvx<16 + KA>(x8, xs);
  float xn;
  {
    float xv[8];
#pragma unroll
    for (int k = 0; k < 4; ++k) { xv[2 * k] = lo16(x8[k]); xv[2 * k + 1] = hi16(x8[k]); }
    float sum = 0.f;
#pragma unroll
    for (int k = 0; k < 8; ++k) sum += xv[k];
    const float mean = wave_sum(sum) * (1.f / 512.f);
    float sq = 0.f;
#pragma unroll
    for (int k = 0; k < 8; ++k) { const float dlt = xv[k] - mean; sq = fmaf(dlt, dlt, sq); }
    const float var = wave_sum(sq) * (1.f / 512.f);
    xn = rbf(rbf(lo16(xs) - rbf(mean)) / rbf(sqrtf(rbf(rbf(var) + 1e-5f))));
  }
  STAMP(1)
  {
    const unsigned xb = __float_as_uint(xn);
    const unsigned xpair = phi(dppu<XOR1>(xb), xb);
    float a[8] = {0.f, 0.f, 0.f, 0.f, 0.f, 0.f, 0.f, 0.f};
#define KISS(T) (KA + 2 * (T) < P0 ? KA + 2 * (T) : P0)
#define M(T) { waitv2<14 - 2 * T + KISS(T)>(wq[2 * T], wq[2 * T + 1]); \
      if (KA + 2 * T < P0) s.issue<(KA + 2 * T) & 15, (KA + 2 * T) & 15>(0); if (KA + 2 * T + 1 < P0) s.issue<(KA + 2 * T + 1) & 15, (KA + 2 * T + 1) & 15>(0); \
      const unsigned xx = dppu<0x150 + 2 * T>(xpair); \
      _Pragma("unroll") for (int k = 0; k < 4; ++k) { a[2 * k] = dot2(plo(wq[2 * T + 1][k], wq[2 * T][k]), xx, a[2 * k]); a[2 * k + 1] = dot2(phi(wq[2 * T + 1][k], wq[2 * T][k]), xx, a[2 * k + 1]); } \
      __builtin_amdgcn_sched_barrier(0); }
    REP8(M)
#undef M
    static_assert(KA + 16 >= P0 && (KA & 1) == 0, "all P0 loads issued by the end of the q loop");
    const float b0 = pair32(a[0], a[4]), b1 = pair32(a[1], a[5]), b2 = pair32(a[2], a[6]), b3 = pair32(a[3], a[7]);
    const float c0 = pair16(b0, b2), c1 = pair16(b1, b3);
    *(f32x2*)(Lq + w * 128 + 8 * c + 2 * r) = f32x2{c0, c1};
  }
  if (tid == 0) *Lcnt = 0u;
  BARRIER();
  v4u q;
  {
    f32x4 a0 = *(const f32x4*)(Lq + (2 * r) * 128 + 8 * c), a1 = *(const f32x4*)(Lq + (2 * r) * 128 + 8 * c + 4);
    f32x4 b0 = *(const f32x4*)(Lq + (2 * r + 1) * 128 + 8 * c), b1 = *(const f32x4*)(Lq + (2 * r + 1) * 128 + 8 * c + 4);
    a0 += b0; a1 += b1;
    float v[8] = {a0[0], a0[1], a0[2], a0[3], a1[0], a1[1], a1[2], a1[3]};
#pragma unroll
    for (int k = 0; k < 8; ++k) { v[k] = pair16(v[k], v[k]); v[k] = pair32(v[k], v[k]); }
#pragma unroll
    for (int k = 0; k < 4; ++k) q[k] = cvtpk(v[2 * k], v[2 * k + 1]);
  }
  STAMP(2)
  float sc[8];
  float d[16];
  static_assert(P0 == 16, "P0");
  const unsigned gu_vo = (4 * c + r) * 3584;
  const u64 g_sb = (u64)wgp + (u64)w * (64 * 3584) + j * 16, u_sb = (u64)wup + (u64)w * (64 * 3584) + j * 16;
  ald_gu(gu_vo, g_sb, u_sb, 0);
#define M(U) { const v4u k = aget<U, (15 - U) + 2 + (U < DEPTH ? U : DEPTH)>(); if (U < DEPTH) s.issue<16 + U, (U & 7)>(0); d[U] = qk(k, q); }
  REP16(M)
#undef M
  sc[7] = fin_score(treduce16(d, c));
  STAMP(5)
  unsigned tag_lo = 0, tag_hi = 0;
  if (!has_mlp && w == 0) {
    const v4u sv = {SENT, SENT, SENT, SENT};
    for (int i = 0; i < 16; ++i) st128sc1(lane * 16 + i * 1024, ws + WS_Y + head * 16384, sv);
    if (lane < 32) st128sc1(lane * 16, mlb, sv);
  }
  const int mg = lane >> 4, mn = lane & 15, cg = lane & 7, rowsel = (lane >> 3) & 1;
  const unsigned wo_vo = (64 * rowsel + 8 * mg) * 1024 + cg * 16;
  const u64 wo_sb = (u64)wop + (u64)head * (128 * 1024) + w * 128;
  for (int tile = 1; tile < 8; ++tile) {
    d[0] = qk(s.step<0, 0>(tile * 4), q);
    switch (tile) {
      case 1: ald64sc1<108>(0u, ws + WS_EPOCH); aldw<8, 0>(wo_vo, wo_sb); aldw<9, 1024>(wo_vo, wo_sb); break;
      case 2: aldw<10, 2048>(wo_vo, wo_sb); aldw<11, 3072>(wo_vo, wo_sb); aldw<12, 0>(wo_vo, wo_sb + 4096); break;
      case 3: aldw<13, 1024>(wo_vo, wo_sb + 4096); aldw<14, 2048>(wo_vo, wo_sb + 4096); aldw<15, 3072>(wo_vo, wo_sb + 4096); break;
      case 4: aldw<16, 0>(wo_vo, wo_sb + 32768); aldw<17, 1024>(wo_vo, wo_sb + 32768); aldw<18, 2048>(wo_vo, wo_sb + 32768); break;
      case 5: aldw<19, 3072>(wo_vo, wo_sb + 32768); aldw<20, 0>(wo_vo, wo_sb + 36864); aldw<21, 1024>(wo_vo, wo_sb + 36864); break;
      case 6: aldw<22, 2048>(wo_vo, wo_sb + 36864); aldw<23, 3072>(wo_vo, wo_sb + 36864);
              aldw<24, 0>((lane >> 3) * 1024 + cg * 16, (u64)wdp + (u64)j * 8192 + w * 128); break;
      default: ald64sc1<110>((lane & 31) * 8, ws + WS_FLAG); ald64sc1<112>(0u, ws + WS_FLAG); ald64sc1<114>(0u, ws + WS_FLAG); break;
    }
#define M(U) d[U] = qk(s.step<U, (U <= DEPTH ? 3 : (U > 8 && U <= 8 + DEPTH) ? 2 : 0)>(tile * 4), q); if (U == 8) ald_gu(gu_vo, g_sb, u_sb, tile);
    REP15(M)
#undef M
#pragma unroll
    for (int k = 0; k < 7; ++k) sc[k] = sc[k + 1];
    sc[7] = fin_score(treduce16(d, c));
    if (tile == 1) {
      const v4u e = aread<27>();
      const u64 tag = (((u64)(unsigned)__builtin_amdgcn_readfirstlane(e[1]) << 32) | (u64)(unsigned)__builtin_amdgcn_readfirstlane(e[0])) + 1ull;
      tag_lo = (unsigned)tag; tag_hi = (unsigned)(tag >> 32);
      if (!has_mlp && w == 0 && lane == 0) st64sc1(0u, ws + WS_FLAG + head * 8, tag_lo, tag_hi);
      STAMP(6)
    }
  }
  {
    const v4u wg = aread<25>(), wu = aread<26>();
    float g[16];
#pragma unroll
    for (int k = 0; k < 4; ++k) { g[2 * k] = xn * lo16(wg[k]); g[2 * k + 1] = xn * hi16(wg[k]); g[8 + 2 * k] = xn * lo16(wu[k]); g[8 + 2 * k + 1] = xn * hi16(wu[k]); }
    float t = treduce16(g, c);
    t = pair16(t, t); t = pair32(t, t);
    if (r == 0) Lg[w * 16 + c] = t;
    asm volatile("s_waitcnt lgkmcnt(0)" ::: "memory");
    if (lane == 0) __hip_atomic_fetch_add((LAS unsigned*)Lcnt, 1u, __ATOMIC_RELAXED, __HIP_MEMORY_SCOPE_WORKGROUP);
  }
  STAMP(3)
  float mw;
  {
    float m = sc[0];
#pragma unroll
    for (int k = 1; k < 8; ++k) m = fmaxf(m, sc[k]);
    mw = wave_max(m);
    float l = 0.f;
#pragma unroll
    for (int k = 0; k < 8; ++k) { sc[k] = __builtin_amdgcn_exp2f((sc[k] - mw) * LOG2E); l += sc[k]; }
    const float lw = wave_sum(l);
    const v4u e = aread<27>();
    if (!__all(e[2] == tag_lo && e[3] == tag_hi)) {
      for (unsigned spins = 0; spins < SPIN_CAP; ++spins) {
        __builtin_amdgcn_s_sleep(2);
        const v2u f = ld64sc1_wait((lane & 31) * 8, ws + WS_FLAG);
        if (__all(f[0] == tag_lo && f[1] == tag_hi)) break;
      }
    }
    if (lane == 0) st64sc1(0u, mlb + (split * 8 + w) * 8, __float_as_uint(mw) & 0xfffffffeu, __float_as_uint(lw) & 0xfffffffeu);
  }
  float acc[8] = {0.f, 0.f, 0.f, 0.f, 0.f, 0.f, 0.f, 0.f};
  for (int tile = 8; tile < 15; ++tile) {
    const float p = sc[0];
    if (tile == 14) ald64sc1<112>(lane * 8, mlb);
#define M(U) fma8(acc, bcast<U>(p), s.step<U, 0, 1>(tile * 4));
    REP16(M)
#undef M
#pragma unroll
    for (int k = 0; k < 7; ++k) sc[k] = sc[k + 1];
  }
  v4u bf[16];
  float scale, ymlp[4];
  {
    const float p = sc[0];
#define M(U) if (U >= DEPTH) s.issue<U, U, 1>(60);
    REP8(M)
#undef M
    {
      const v4u e = aread<28>();
      unsigned mb = e[0], lb = e[1];
      if (!__all(mb != SENT && lb != SENT)) {
        for (unsigned spins = 0; spins < SPIN_CAP; ++spins) {
          __builtin_amdgcn_s_sleep(2);
          const v2u t = ld64sc1_wait(lane * 8, mlb);
          mb = t[0]; lb = t[1];
          if (__all(mb != SENT && lb != SENT)) break;
        }
      }
      const float mi = __uint_as_float(mb), li = __uint_as_float(lb);
      const float M = wave_max(mi);
      const float L = wave_sum(li * __builtin_amdgcn_exp2f((mi - M) * LOG2E));
      scale = __builtin_amdgcn_exp2f((mw - M) * LOG2E) / L;
    }
#define M(T) { const v4u A = aread<8 + 2 * T>(), B = aread<9 + 2 * T>(); _Pragma("unroll") for (int k = 0; k < 4; ++k) { \
      bf[(T >> 2) * 8 + 2 * k][T & 3] = plo(B[k], A[k]); bf[(T >> 2) * 8 + 2 * k + 1][T & 3] = phi(B[k], A[k]); } }
    REP8(M)
#undef M
    {
      for (unsigned spins = 0; *Lcnt != 8u && spins < SPIN_CAP; ++spins) __builtin_amdgcn_s_sleep(1);
      asm volatile("" ::: "memory");
      const int f = lane >> 3;
      float g = 0.f, u = 0.f;
#pragma unroll
      for (int ww = 0; ww < 8; ++ww) { g += Lg[ww * 16 + f]; u += Lg[ww * 16 + 8 + f]; }
      g = rbf(g); u = rbf(u);
      float hval = rbf(rbf(1.f / (1.f + __expf(-g))) * u);
      if (!has_mlp) hval = 0.f;
      const v4u wd = aread<24>();
      float ym[8];
#pragma unroll
      for (int k = 0; k < 4; ++k) { ym[2 * k] = hval * lo16(wd[k]); ym[2 * k + 1] = hval * hi16(wd[k]); }
#pragma unroll
      for (int k = 0; k < 4; ++k) { float t = pair32(ym[k], ym[k + 4]); t = pair16(t, t); ymlp[k] = t + dpp<XOR8>(t); asm volatile("" : "+v"(ymlp[k])); }
    }
#define M(U) fma8(acc, bcast<U>(p), aget<U, 7>()); s.issue<8 + U, U, 1>(60);
    REP8(M)
#undef M
#define M(U) fma8(acc, bcast<8 + U>(p), aget<U, 7 - U>());
    REP8(M)
#undef M
  }
  STAMP(4)
  {
    const float b0 = pair32(acc[0], acc[4]), b1 = pair32(acc[1], acc[5]), b2 = pair32(acc[2], acc[6]), b3 = pair32(acc[3], acc[7]);
    const float c0 = pair16(b0, b2), c1 = pair16(b1, b3);
    Lo[w * 68 + 4 * c + r] = cvtpk(c0 * scale, c1 * scale);
  }
  BARRIER();
  {
    const unsigned* ap = Lo + (mn & 7) * 68 + 32 * (mn >> 3) + 4 * mg;
    const v4u a0 = *(const v4u*)ap, a1 = *(const v4u*)(ap + 16);
    float y[8];
#pragma unroll
    for (int cc = 0; cc < 8; ++cc) {
      f32x4 dd = {0.f, 0.f, 0.f, 0.f};
      dd = __builtin_amdgcn_mfma_f32_16x16x32_bf16(__builtin_bit_cast(bf16x8, a0), __builtin_bit_cast(bf16x8, bf[cc]), dd, 0, 0, 0);
      dd = __builtin_amdgcn_mfma_f32_16x16x32_bf16(__builtin_bit_cast(bf16x8, a1), __builtin_bit_cast(bf16x8, bf[8 + cc]), dd, 0, 0, 0);
      const float t = (dd[0] + dd[1]) + (dd[2] + dd[3]);
      y[cc] = __builtin_bit_cast(float, __builtin_amdgcn_update_dpp(__builtin_bit_cast(int, t), __builtin_bit_cast(int, t), XOR8, 0xc, 0xf, false));
    }
    v4u o;
#pragma unroll
    for (int k = 0; k < 4; ++k) { float t = pair32(y[k], y[k + 4]); t = pair16(t, t); o[k] = __float_as_uint(t + ymlp[k]) & 0xfffffffeu; }
    if ((lane & 24) == 0) st128sc1((4 * w + (cg >> 1)) * 16384 + chunk * 64 + (cg & 1) * 32 + (lane >> 5) * 16, ws + WS_Y, o);
  }
  if (!has_mlp) {
    unsigned xv;
    ld16(xv, (lane & 15) * 2, (u64)xp + head * 32);
    const u64 yb = ws + WS_Y + head * 16384 + w * 2048;
    v4u y0, y1;
    for (unsigned spins = 0; spins < SPIN_CAP; ++spins) {
      asm volatile("global_load_dwordx4 %0, %3, %4 sc1\n\tglobal_load_dwordx4 %1, %3, %4 offset:1024 sc1\n\ts_waitcnt vmcnt(0)"
                   : "=&v"(y0), "=&v"(y1), "+v"(xv) : "v"(lane * 16), "s"(yb) : "memory");
      bool ok = true;
#pragma unroll
      for (int k = 0; k < 4; ++k) ok = ok && (y0[k] != SENT) && (y1[k] != SENT);
      if (__all(ok)) break;
    }
    float v[4];
#pragma unroll
    for (int k = 0; k < 4; ++k) v[k] = __uint_as_float(y0[k]) + __uint_as_float(y1[k]);
    float t = pair16(pair32(v[0], v[2]), pair32(v[1], v[3]));
    t += dpp<XOR8>(t); t += dpp<ROR4>(t);
    if ((lane & 12) == 0) Lr[w * 16 + 4 * (lane & 3) + (lane >> 4)] = t;
    BARRIER();
    if (w == 0 && lane < 16) {
      float sum = 0.f;
#pragma unroll
      for (int ww = 0; ww < 8; ++ww) sum += Lr[ww * 16 + lane];
      const float o = lo16(xv) + sum;
      st16(lane * 2, (u64)outp + head * 32, cvtpk(o, o));
      if (head == 0 && lane == 0) st64sc1(0u, ws + WS_EPOCH, tag_lo, tag_hi);
    }
  }
}

extern "C" void kernel_launch(void* const* d_in, const int* in_sizes, int n_in, void* d_out, int out_size, void* d_ws, size_t ws_size,
                              hipStream_t stream) {
  fused_decode<<<NWG, WAVES * 64, 0, stream>>>(d_in[0], d_in[1], d_in[2], d_in[3], d_in[4], d_in[6], d_in[7], d_in[8], d_ws, d_out);
}
```

```cpp
#include <hip/hip_runtime.h>
#include <hip/hip_bf16.h>

#define WPOL ""
#define KVPOL "nt"
#define PRIO 2
#define FB 1
#define FB_T1 40
#define FB_T2 100
#define FB_HALF 1
#define TICKET 3
#define ABL 0
#define POLL_SLEEP 0
#define BASEMODE 0

typedef unsigned v4u __attribute__((ext_vector_type(4)));
typedef unsigned v2u __attribute__((ext_vector_type(2)));
typedef float f32x4 __attribute__((ext_vector_type(4)));
typedef float f32x2 __attribute__((ext_vector_type(2)));
typedef __bf16 bf16x2 __attribute__((ext_vector_type(2)));
typedef __bf16 bf16x8 __attribute__((ext_vector_type(8)));
typedef unsigned long long u64;
#define LAS __attribute__((address_space(3)))
#define STAMP(k)

constexpr int NWG = 256, WAVES = 8;
#define W0V 3
constexpr int W0 = W0V;
constexpr int DM = 5;
constexpr unsigned WS_EPOCH = 0, WS_FLAG = 256, WS_ML = 4096, WS_CNT = 32768, WS_Y = 65536;
constexpr unsigned SENT = 0xffffffffu;
constexpr unsigned SPIN_CAP = 1u << 20;

__device__ __forceinline__ void ld128w(v4u& dst, unsigned voff, u64 sbase) {
  asm volatile("global_load_dwordx4 %0, %1, %2 " WPOL : "=v"(dst) : "v"(voff), "s"(sbase) : "memory");
}
__device__ __forceinline__ void ld16(unsigned& dst, unsigned voff, u64 sbase) {
  asm volatile("global_load_ushort %0, %1, %2" : "=v"(dst) : "v"(voff), "s"(sbase) : "memory");
}

#define ACLOB "a0","a1","a2","a3","a4","a5","a6","a7","a8","a9","a10","a11","a12","a13","a14","a15","a16","a17","a18","a19","a20","a21","a22","a23","a24","a25","a26","a27","a28","a29","a30","a31", \
  "a32","a33","a34","a35","a36","a37","a38","a39","a40","a41","a42","a43","a44","a45","a46","a47","a48","a49","a50","a51","a52","a53","a54","a55","a56","a57","a58","a59","a60","a61","a62","a63", \
  "a64","a65","a66","a67","a68","a69","a70","a71","a72","a73","a74","a75","a76","a77","a78","a79","a80","a81","a82","a83","a84","a85","a86","a87","a88","a89","a90","a91","a92","a93","a94","a95", \
  "a96","a97","a98","a99","a100","a101","a102","a103","a104","a105","a106","a107","a108","a109","a110","a111","a112","a113","a114","a115"
template <int SLOT, int OFF> __device__ __forceinline__ void ald(unsigned voff, u64 sbase) {
  asm volatile("global_load_dwordx4 a[%c2:%c3], %0, %1 offset:%c4 " KVPOL :: "v"(voff), "s"(sbase), "i"(4 * SLOT), "i"(4 * SLOT + 3), "i"(OFF) : "memory", ACLOB);
}
template <int SLOT, int OFF> __device__ __forceinline__ void aldw(unsigned voff, u64 sbase) {
  asm volatile("global_load_dwordx4 a[%c2:%c3], %0, %1 offset:%c4 " WPOL :: "v"(voff), "s"(sbase), "i"(4 * SLOT), "i"(4 * SLOT + 3), "i"(OFF) : "memory", ACLOB);
}
__device__ __forceinline__ void ald_gu(unsigned voff, u64 gbase, u64 ubase, int idx) {
  const u64 mask = 0xffull << (8 * idx);
  asm volatile("s_mov_b64 exec, %3\n\tglobal_load_dwordx4 a[100:103], %0, %1 " WPOL "\n\tglobal_load_dwordx4 a[104:107], %0, %2 " WPOL "\n\ts_mov_b64 exec, -1"
               :: "v"(voff), "s"(gbase), "s"(ubase), "s"(mask) : "memory", ACLOB);
}
template <int REG> __device__ __forceinline__ void ald64sc1(unsigned voff, u64 sbase) {
  asm volatile("global_load_dwordx2 a[%c2:%c3], %0, %1 sc1" :: "v"(voff), "s"(sbase), "i"(REG), "i"(REG + 1) : "memory", ACLOB);
}
__device__ __forceinline__ void aticket(unsigned zero, u64 sbase) {
  asm volatile("s_mov_b64 exec, 1\n\tglobal_load_dword a115, %0, %1\n\ts_mov_b64 exec, -1" :: "v"(zero), "s"(sbase) : "memory", ACLOB);
}
template <int RSLOT, int N> __device__ __forceinline__ v4u aget(void) {
  unsigned a, b, c, d;
  asm volatile("s_waitcnt vmcnt(%c4)\n\tv_accvgpr_read_b32 %0, a%c5\n\tv_accvgpr_read_b32 %1, a%c6\n\tv_accvgpr_read_b32 %2, a%c7\n\tv_accvgpr_read_b32 %3, a%c8"
               : "=v"(a), "=v"(b), "=v"(c), "=v"(d) : "i"(N), "i"(4 * RSLOT), "i"(4 * RSLOT + 1), "i"(4 * RSLOT + 2), "i"(4 * RSLOT + 3) : "memory", ACLOB);
  return v4u{a, b, c, d};
}
template <int RSLOT, int N5, int N4, int N3, int WSLOT, int OFF> __device__ __forceinline__ v4u astep(unsigned voff, u64 sbase, int mode) {
  unsigned a, b, c, d;
  asm volatile("s_waitcnt vmcnt(%c7)\n\t"
               "s_cmp_gt_i32 %6, %c17\n\ts_cbranch_scc1 1f\n\t"
               "s_waitcnt vmcnt(%c8)\n\t"
               "s_cmp_gt_i32 %6, %c18\n\ts_cbranch_scc1 1f\n\t"
               "s_waitcnt vmcnt(%c9)\n"
               "1:\n\tglobal_load_dwordx4 a[%c14:%c15], %4, %5 offset:%c16 " KVPOL "\n\t"
               "v_accvgpr_read_b32 %0, a%c10\n\tv_accvgpr_read_b32 %1, a%c11\n\tv_accvgpr_read_b32 %2, a%c12\n\tv_accvgpr_read_b32 %3, a%c13"
               : "=&v"(a), "=&v"(b), "=&v"(c), "=&v"(d)
               : "v"(voff), "s"(sbase), "s"(mode), "i"(N5), "i"(N4), "i"(N3),
                 "i"(4 * RSLOT), "i"(4 * RSLOT + 1), "i"(4 * RSLOT + 2), "i"(4 * RSLOT + 3), "i"(4 * WSLOT), "i"(4 * WSLOT + 3), "i"(OFF),
                 "i"(FB_T2), "i"(FB_T1)
               : "memory", "scc", ACLOB);
  return v4u{a, b, c, d};
}
template <int N, int WSLOT, int OFF> __device__ __forceinline__ void apre(unsigned voff, u64 sbase) {
  asm volatile("s_waitcnt vmcnt(%c2)\n\tglobal_load_dwordx4 a[%c3:%c4], %0, %1 offset:%c5 " KVPOL
               :: "v"(voff), "s"(sbase), "i"(N), "i"(4 * WSLOT), "i"(4 * WSLOT + 3), "i"(OFF) : "memory", ACLOB);
}
template <int RSLOT> __device__ __forceinline__ v4u aread(void) {
  unsigned a, b, c, d;
  asm volatile("v_accvgpr_read_b32 %0, a%c4\n\tv_accvgpr_read_b32 %1, a%c5\n\tv_accvgpr_read_b32 %2, a%c6\n\tv_accvgpr_read_b32 %3, a%c7\n\ts_nop 1"
               : "=v"(a), "=v"(b), "=v"(c), "=v"(d) : "i"(4 * RSLOT), "i"(4 * RSLOT + 1), "i"(4 * RSLOT + 2), "i"(4 * RSLOT + 3) : "memory", ACLOB);
  return v4u{a, b, c, d};
}
__device__ __forceinline__ unsigned aread_ticket(void) {
  unsigned a;
  asm volatile("v_accvgpr_read_b32 %0, a115\n\ts_nop 1" : "=v"(a) :: "memory", ACLOB);
  return a;
}
__device__ __forceinline__ void st128sc1(unsigned voff, u64 sbase, const v4u& v) {
  asm volatile("global_store_dwordx4 %0, %1, %2 sc1\n\ts_nop 1" :: "v"(voff), "v"(v), "s"(sbase) : "memory");
}
__device__ __forceinline__ void st64sc1(unsigned voff, u64 sbase, unsigned lo, unsigned hi) {
  const v2u v = {lo, hi};
  asm volatile("global_store_dwordx2 %0, %1, %2 sc1" :: "v"(voff), "v"(v), "s"(sbase) : "memory");
}
__device__ __forceinline__ void st16(unsigned voff, u64 sbase, unsigned v) {
  asm volatile("global_store_short %0, %1, %2" :: "v"(voff), "v"(v), "s"(sbase) : "memory");
}
__device__ __forceinline__ v2u ld64sc1_wait(unsigned voff, u64 sbase) {
  v2u v;
  asm volatile("global_load_dwordx2 %0, %1, %2 sc1\n\ts_waitcnt vmcnt(0)" : "=&v"(v) : "v"(voff), "s"(sbase) : "memory");
  return v;
}

__device__ __forceinline__ float dot2(unsigned a, unsigned b, float acc) {
  return __builtin_amdgcn_fdot2_f32_bf16(__builtin_bit_cast(bf16x2, a), __builtin_bit_cast(bf16x2, b), acc, false);
}
__device__ __forceinline__ float lo16(unsigned v) { return __builtin_bit_cast(float, v << 16); }
__device__ __forceinline__ float hi16(unsigned v) { return __builtin_bit_cast(float, v & 0xffff0000u); }
template <int CTRL> __device__ __forceinline__ float dpp(float x) {
  return __builtin_bit_cast(float, __builtin_amdgcn_mov_dpp(__builtin_bit_cast(int, x), CTRL, 0xf, 0xf, true));
}
template <int CTRL> __device__ __forceinline__ unsigned dppu(unsigned x) { return (unsigned)__builtin_amdgcn_mov_dpp((int)x, CTRL, 0xf, 0xf, true); }
constexpr int XOR1 = 0xB1, XOR2 = 0x4E, XOR7 = 0x141, XOR8 = 0x128, ROR4 = 0x124;
__device__ __forceinline__ unsigned cvtpk(float lo, float hi) {
  const bf16x2 v = {(__bf16)lo, (__bf16)hi};
  return __builtin_bit_cast(unsigned, v);
}
__device__ __forceinline__ float rbf(float x) { return lo16(cvtpk(x, x)); }
__device__ __forceinline__ float pair32(float a, float b) {
  auto s = __builtin_amdgcn_permlane32_swap(__float_as_uint(a), __float_as_uint(b), false, false);
  return __uint_as_float(s[0]) + __uint_as_float(s[1]);
}
__device__ __forceinline__ float pair16(float a, float b) {
  auto s = __builtin_amdgcn_permlane16_swap(__float_as_uint(a), __float_as_uint(b), false, false);
  return __uint_as_float(s[0]) + __uint_as_float(s[1]);
}
__device__ __forceinline__ float wave_sum(float x) {
  x += dpp<XOR1>(x); x += dpp<XOR2>(x); x += dpp<XOR7>(x); x += dpp<XOR8>(x);
  x = pair16(x, x); x = pair32(x, x); return x;
}
__device__ __forceinline__ float wave_max(float x) {
  x = fmaxf(x, dpp<XOR1>(x)); x = fmaxf(x, dpp<XOR2>(x)); x = fmaxf(x, dpp<XOR7>(x)); x = fmaxf(x, dpp<XOR8>(x));
  auto s = __builtin_amdgcn_permlane16_swap(__float_as_uint(x), __float_as_uint(x), false, false);
  x = fmaxf(__uint_as_float(s[0]), __uint_as_float(s[1]));
  auto t = __builtin_amdgcn_permlane32_swap(__float_as_uint(x), __float_as_uint(x), false, false);
  return fmaxf(__uint_as_float(t[0]), __uint_as_float(t[1]));
}
__device__ __forceinline__ float treduce16(float (&x)[16], int c) {
#define TR_STEP(HALF, CTRL, BIT) _Pragma("unroll") for (int h = 0; h < (HALF); ++h) { \
    const float lo_ = x[h], hi_ = x[h + (HALF)]; const float t1_ = lo_ + dpp<CTRL>(lo_), t2_ = hi_ + dpp<CTRL>(hi_); x[h] = (c & (BIT)) ? t2_ : t1_; }
  TR_STEP(8, XOR8, 8) TR_STEP(4, XOR7, 4) TR_STEP(2, XOR2, 2) TR_STEP(1, XOR1, 1)
#undef TR_STEP
  return x[0];
}
template <int T> __device__ __forceinline__ float bcast(float p) { return dpp<0x150 + T>(p); }
__device__ __forceinline__ void fma8(float (&acc)[8], float p, const v4u& v) {
#pragma unroll
  for (int k = 0; k < 4; ++k) { acc[2 * k] = fmaf(p, lo16(v[k]), acc[2 * k]); acc[2 * k + 1] = fmaf(p, hi16(v[k]), acc[2 * k + 1]); }
}
__device__ __forceinline__ unsigned plo(unsigned hi_row, unsigned lo_row) { return __builtin_amdgcn_perm(hi_row, lo_row, 0x05040100u); }
__device__ __forceinline__ unsigned phi(unsigned hi_row, unsigned lo_row) { return __builtin_amdgcn_perm(hi_row, lo_row, 0x07060302u); }

constexpr int younger(int U, int X, int NA, int NB) { return (U + DM - 1 - X) + ((U >= 1 && X <= DM) ? NA : 0) + ((U >= 9 && X <= 8 + DM) ? NB : 0); }
struct Stream {
  u64 K, V; unsigned voff; int rot;
  __device__ __forceinline__ u64 base(int round) const { return ((round < 32) ? K : V) + (u64)(unsigned)(((round + rot) & 31) << 15); }
  template <int J, int SLOT> __device__ __forceinline__ void issue(int round0) const { ald<SLOT, (J & 3) * 1024>(voff, base(round0 + (J >> 2))); }
  template <int U, int NA, int NB> __device__ __forceinline__ v4u step(int round0, int mode) const {
    return astep<(U & 7), younger(U, U, NA, NB), younger(U, U + 1, NA, NB), younger(U, U + 2, NA, NB), ((U + DM) & 7), ((U + DM) & 3) * 1024>(
        voff, base(round0 + ((U + DM) >> 2)), mode);
  }
};
__device__ __forceinline__ float qk(const v4u& k, const v4u& q) { return dot2(k[3], q[3], dot2(k[2], q[2], dot2(k[1], q[1], dot2(k[0], q[0], 0.f)))); }
#define REP16(M) M(0) M(1) M(2) M(3) M(4) M(5) M(6) M(7) M(8) M(9) M(10) M(11) M(12) M(13) M(14) M(15)
#define REP15(M) M(1) M(2) M(3) M(4) M(5) M(6) M(7) M(8) M(9) M(10) M(11) M(12) M(13) M(14) M(15)
#define REP8(M) M(0) M(1) M(2) M(3) M(4) M(5) M(6) M(7)
#define BARRIER() asm volatile("s_waitcnt lgkmcnt(0)\n\ts_barrier" ::: "memory")

constexpr int WQ0 = 4, QPOST = W0 + 8 - WQ0, QGET = 10, XSTEP = 8;
static_assert(QGET > QPOST && QGET < 16 - W0 && XSTEP + WQ0 >= 8 && XSTEP + W0 < 16, "tile-0 schedule");
constexpr int t0_ext(int m) { return m < 0 ? 2 * WQ0 : (m + WQ0 < 8 ? 2 : (m == XSTEP ? 4 : 0)); }
constexpr int t0_wait(int n) { int c = W0 - 1; for (int m = (n - W0 < -1 ? -1 : n - W0); m <= n - 1; ++m) c += t0_ext(m); return c; }

__device__ __forceinline__ float fin_score(float d) { return rbf(rbf(d) * 0.08838834764831845f); }
constexpr float LOG2E = 1.4426950408889634f;

__global__ void __launch_bounds__(WAVES * 64, 2) fused_decode(const void* xp, const void* Kp, const void* Vp, const void* wqp, const void* wop,
                                                             const void* wgp, const void* wup, const void* wdp, void* wsp, void* outp) {
  __shared__ __attribute__((aligned(16))) float lds[8 * 128 + 8 * 16 + 8 * 68 + 8 * 16 + 4];
  float* Lq = lds;
  float* Lg = lds + 1024;
  unsigned* Lo = (unsigned*)(lds + 1024 + 128);
  float* Lr = lds + 1024 + 128 + 8 * 68;
  volatile LAS unsigned* Lcnt = (volatile LAS unsigned*)(Lr + 128);
  STAMP(0)
  const u64 tstart = __builtin_amdgcn_s_memrealtime();
  const int tid = threadIdx.x, lane = tid & 63, w = __builtin_amdgcn_readfirstlane(tid >> 6), r = lane >> 4, c = lane & 15;
  const int b = blockIdx.x, xcd = b & 7, i8 = b >> 3;
  const int head = xcd * 4 + (i8 & 3), split = i8 >> 2, chunk = head * 8 + split;
  const bool has_mlp = i8 < 28;
  const int j = xcd * 28 + (has_mlp ? i8 : 27);
  const u64 ws = (u64)wsp;
  const u64 mlb = ws + WS_ML + head * 512;

  Stream s;
  { const u64 co = ((u64)chunk << 20) + (u64)(w << 12); s.K = (u64)Kp + co; s.V = (u64)Vp + co; s.voff = lane * 16; s.rot = b & 31; }
  v4u x8; unsigned xs; v4u wq[16];
  const unsigned wq_vo = r * 8192 + c * 16; const u64 wq_sb = (u64)wqp + (u64)w * (64 * 8192) + head * 256;
#define WQ_PAIR(P) { ld128w(wq[2 * (P)], wq_vo, wq_sb + (u64)(2 * (P)) * (4 * 8192)); ld128w(wq[2 * (P) + 1], wq_vo, wq_sb + (u64)(2 * (P) + 1) * (4 * 8192)); }
  ld128w(x8, lane * 16, (u64)xp);
  ld16(xs, (4 * c + r) * 2, (u64)xp + w * 128);
  s.issue<0, 0>(0); s.issue<1, 1>(0); s.issue<2, 2>(0); if (W0 > 3) s.issue<3, 3>(0); if (W0 > 4) s.issue<4, 4>(0);
  WQ_PAIR(0) WQ_PAIR(1) WQ_PAIR(2) WQ_PAIR(3)
  if (tid == 0) { Lcnt[0] = 0u; Lcnt[1] = 0u; }
  BARRIER();
  const int mg = lane >> 4, mn = lane & 15, cg = lane & 7, rowsel = (lane >> 3) & 1;
  const unsigned gu_vo = (4 * c + r) * 3584;
  const u64 g_sb = (u64)wgp + (u64)w * (64 * 3584) + j * 16, u_sb = (u64)wup + (u64)w * (64 * 3584) + j * 16;
  const u64 wo_sb = (u64)wop + (u64)head * (128 * 1024) + w * 128;
  float xn; unsigned xpair;
  float qa[8] = {0.f, 0.f, 0.f, 0.f, 0.f, 0.f, 0.f, 0.f};
  v4u q;
  float sc[8];
  float d[16];
#define PRE(N) apre<t0_wait(N), ((N) + W0 < 16 ? (N) + W0 : ((N) + W0) & 7), (((N) + W0) & 3) * 1024>(s.voff, s.base(((N) + W0) >> 2));
#define QP(P) { asm volatile("" : "+v"(wq[2 * (P)]), "+v"(wq[2 * (P) + 1])); const unsigned xx = dppu<0x150 + 2 * (P)>(xpair); \
      _Pragma("unroll") for (int k = 0; k < 4; ++k) { qa[2 * k] = dot2(plo(wq[2 * (P) + 1][k], wq[2 * (P)][k]), xx, qa[2 * k]); \
        qa[2 * k + 1] = dot2(phi(wq[2 * (P) + 1][k], wq[2 * (P)][k]), xx, qa[2 * k + 1]); } }
#define KQ(U) d[U] = qk(aread<U>(), q);
#define M(N) PRE(N) \
    if ((N) + WQ0 < 8) WQ_PAIR(((N) + WQ0) & 7) \
    if ((N) == XSTEP) { ald_gu(gu_vo, g_sb, u_sb, 0); ald64sc1<108>(0u, ws + WS_EPOCH); \
                        aldw<24, 0>((lane >> 3) * 1024 + cg * 16, (u64)wdp + (u64)j * 8192 + w * 128); } \
    if ((N) == 0) { \
        \
      asm volatile("" : "+v"(x8), "+v"(xs)); \
      float xv[8]; \
      _Pragma("unroll") for (int k = 0; k < 4; ++k) { xv[2 * k] = lo16(x8[k]); xv[2 * k + 1] = hi16(x8[k]); } \
      float sum = 0.f; \
      _Pragma("unroll") for (int k = 0; k < 8; ++k) sum += xv[k]; \
      const float mean = wave_sum(sum) * (1.f / 512.f); \
      float sq = 0.f; \
      _Pragma("unroll") for (int k = 0; k < 8; ++k) { const float dlt = xv[k] - mean; sq = fmaf(dlt, dlt, sq); } \
      const float var = wave_sum(sq) * (1.f / 512.f); \
      xn = rbf(rbf(lo16(xs) - rbf(mean)) / rbf(sqrtf(rbf(rbf(var) + 1e-5f)))); \
      const unsigned xb = __float_as_uint(xn); \
      xpair = phi(dppu<XOR1>(xb), xb);     \
      STAMP(1) \
    } \
    if ((N) == W0) { QP(0) QP(1) QP(2) QP(3) } \
    if ((N) > W0 && (N) - W0 - 1 + WQ0 < 8) QP(((N) - W0 - 1 + WQ0) & 7) \
    if ((N) == QPOST) { \
      const float b0 = pair32(qa[0], qa[4]), b1 = pair32(qa[1], qa[5]), b2 = pair32(qa[2], qa[6]), b3 = pair32(qa[3], qa[7]); \
      const float c0 = pair16(b0, b2), c1 = pair16(b1, b3); \
      *(f32x2*)(Lq + w * 128 + 8 * c + 2 * r) = f32x2{c0, c1};     \
      asm volatile("s_waitcnt lgkmcnt(0)" ::: "memory"); \
      if (lane == 0) __hip_atomic_fetch_add((LAS unsigned*)(Lcnt + 1), 1u, __ATOMIC_RELAXED, __HIP_MEMORY_SCOPE_WORKGROUP); \
    } \
    if ((N) == QGET) { \
      for (unsigned spins = 0; Lcnt[1] != 8u && spins < SPIN_CAP; ++spins) __builtin_amdgcn_s_sleep(1); \
      asm volatile("" ::: "memory"); \
      f32x4 a0 = *(const f32x4*)(Lq + (2 * r) * 128 + 8 * c), a1 = *(const f32x4*)(Lq + (2 * r) * 128 + 8 * c + 4); \
      f32x4 b0 = *(const f32x4*)(Lq + (2 * r + 1) * 128 + 8 * c), b1 = *(const f32x4*)(Lq + (2 * r + 1) * 128 + 8 * c + 4); \
      a0 += b0; a1 += b1; \
      float v[8] = {a0[0], a0[1], a0[2], a0[3], a1[0], a1[1], a1[2], a1[3]}; \
      _Pragma("unroll") for (int k = 0; k < 8; ++k) { v[k] = pair16(v[k], v[k]); v[k] = pair32(v[k], v[k]); } \
      _Pragma("unroll") for (int k = 0; k < 4; ++k) q[k] = cvtpk(v[2 * k], v[2 * k + 1]); \
      STAMP(2) \
      KQ(0) KQ(1) KQ(2) KQ(3) \
    } \
    if ((N) == QGET + 1) { KQ(4) KQ(5) KQ(6) KQ(7) KQ(8) KQ(9) KQ(10) } \
    if ((N) > QGET) KQ(N) \
    __builtin_amdgcn_sched_barrier(0);
  REP16(M)
#undef M
  sc[7] = fin_score(treduce16(d, c));
  if (W0 < 5) { PRE(16) } if (W0 < 4) { PRE(17) }
  STAMP(5)
  unsigned tag_lo, tag_hi;
  {
    const v4u e = aread<27>();
    const u64 tag = (((u64)(unsigned)__builtin_amdgcn_readfirstlane(e[1]) << 32) | (u64)(unsigned)__builtin_amdgcn_readfirstlane(e[0])) + 1ull;
    tag_lo = (unsigned)tag; tag_hi = (unsigned)(tag >> 32);
    asm volatile("v_accvgpr_write_b32 a114, 1" ::: ACLOB);
  }
  const bool clearer = !has_mlp && w == 0;
  if (clearer && !(ABL & 4)) {
    const v4u sv = {SENT, SENT, SENT, SENT};
    for (int i = 0; i < 16; ++i) st128sc1(lane * 16 + i * 1024, ws + WS_Y + head * 16384, sv);
    if (lane < 32) st128sc1(lane * 16, mlb, sv);
    if (lane == 0) st64sc1(0u, ws + WS_CNT + head * 128, 0u, 0u);
  }
  const unsigned wo_vo = (64 * rowsel + 8 * mg) * 1024 + cg * 16;
  const u64 cnt_sb = ws + WS_CNT + ((i8 + 4 * w) & 31) * 128;
  constexpr int MODE0 = BASEMODE == 0 ? -1000 : (BASEMODE == 1 ? FB_T1 + 1 : FB_T2 + 1);
  int mode = MODE0;
#define SCH_T0 600
#define SCH_K 472
#define SCH_V 440
#define FEEDBACK(TILE) if (FB) { const int now_ = (int)(unsigned)(__builtin_amdgcn_s_memrealtime() - tstart); \
    mode = now_ - ((TILE) <= 8 ? SCH_T0 + (TILE) * SCH_K : SCH_T0 + 8 * SCH_K + ((TILE) - 8) * SCH_V); }
#define SETPRIO(TILE) if (PRIO == 2) { if (((w >> 2) ^ (TILE)) & 1) __builtin_amdgcn_s_setprio(1); else __builtin_amdgcn_s_setprio(0); }
  for (int tile = 1; tile < 8; ++tile) {
    SETPRIO(tile)
    if (FB_HALF) mode = MODE0;
    d[0] = qk(s.step<0, 3, 2>(tile * 4, mode), q);
    switch (tile) {
      case 1: aldw<8, 0>(wo_vo, wo_sb); aldw<9, 1024>(wo_vo, wo_sb); aldw<10, 2048>(wo_vo, wo_sb); break;
      case 2: aldw<11, 3072>(wo_vo, wo_sb); aldw<12, 0>(wo_vo, wo_sb + 4096); aldw<13, 1024>(wo_vo, wo_sb + 4096); break;
      case 3: aldw<14, 2048>(wo_vo, wo_sb + 4096); aldw<15, 3072>(wo_vo, wo_sb + 4096); ald64sc1<110>((lane & 31) * 8, ws + WS_FLAG); break;
      case 4: aldw<16, 0>(wo_vo, wo_sb + 32768); aldw<17, 1024>(wo_vo, wo_sb + 32768); aticket(0u, cnt_sb); break;
      case 5: aldw<18, 2048>(wo_vo, wo_sb + 32768); aldw<19, 3072>(wo_vo, wo_sb + 32768); aticket(0u, cnt_sb); break;
      case 6: aldw<20, 0>(wo_vo, wo_sb + 36864); aldw<21, 1024>(wo_vo, wo_sb + 36864); aticket(0u, cnt_sb); break;
      default: aldw<22, 2048>(wo_vo, wo_sb + 36864); aldw<23, 3072>(wo_vo, wo_sb + 36864); aticket(0u, cnt_sb); break;
    }
#define M(U) d[U] = qk(s.step<U, 3, 2>(tile * 4, mode), q); \
    if (U == 7) { \
      { FEEDBACK(tile) } \
        \
      if (tile == 1 && clearer && lane == 0) st64sc1(0u, ws + WS_FLAG + head * 8, tag_lo, tag_hi); \
    } \
    if (U == 8) ald_gu(gu_vo, g_sb, u_sb, tile);
    REP15(M)
#undef M
#pragma unroll
    for (int k = 0; k < 7; ++k) sc[k] = sc[k + 1];
    sc[7] = fin_score(treduce16(d, c));
    if (tile == 1) { STAMP(6) }
    if (tile == 3) {
      const v4u e = aread<27>();
      if (!__all(e[2] == tag_lo && e[3] == tag_hi)) {
        for (unsigned spins = 0; spins < SPIN_CAP; ++spins) {
          __builtin_amdgcn_s_sleep(2);
          const v2u f = ld64sc1_wait((lane & 31) * 8, ws + WS_FLAG);
          if (__all(f[0] == tag_lo && f[1] == tag_hi)) break;
        }
      }
    }
  }
  STAMP(3)
  float mw;
  {
    float m = sc[0];
#pragma unroll
    for (int k = 1; k < 8; ++k) m = fmaxf(m, sc[k]);
    mw = wave_max(m);
    float l = 0.f;
#pragma unroll
    for (int k = 0; k < 8; ++k) { sc[k] = __builtin_amdgcn_exp2f((sc[k] - mw) * LOG2E); l += sc[k]; }
    const float lw = wave_sum(l);
    if (!(ABL & 1) && lane == 0) st64sc1(0u, mlb + (split * 8 + w) * 8, __float_as_uint(mw) & 0xfffffffeu, __float_as_uint(lw) & 0xfffffffeu);
  }
  float acc[8] = {0.f, 0.f, 0.f, 0.f, 0.f, 0.f, 0.f, 0.f};
  for (int tile = 8; tile < 15; ++tile) {
    SETPRIO(tile)
    if (FB_HALF) mode = MODE0;
    const float p = sc[0];
#define M(U) fma8(acc, bcast<U>(p), s.step<U, 1, 0>(tile * 4, mode)); \
    if (U == 0) { aticket(0u, cnt_sb); if (!(ABL & 1) && tile == 14) ald64sc1<112>(lane * 8, mlb); } \
    if (U == 2 && tile == 8) { \
      const v4u wg = aread<25>(), wu = aread<26>(); \
      float g[16]; \
      _Pragma("unroll") for (int k = 0; k < 4; ++k) { g[2 * k] = xn * lo16(wg[k]); g[2 * k + 1] = xn * hi16(wg[k]); g[8 + 2 * k] = xn * lo16(wu[k]); g[8 + 2 * k + 1] = xn * hi16(wu[k]); } \
      float t = treduce16(g, c); \
      t = pair16(t, t); t = pair32(t, t); \
      if (r == 0) Lg[w * 16 + c] = t; \
      asm volatile("s_waitcnt lgkmcnt(0)" ::: "memory"); \
      if (lane == 0) __hip_atomic_fetch_add((LAS unsigned*)Lcnt, 1u, __ATOMIC_RELAXED, __HIP_MEMORY_SCOPE_WORKGROUP); \
    } \
    if (U == 7) { FEEDBACK(tile) }
    REP16(M)
#undef M
#pragma unroll
    for (int k = 0; k < 7; ++k) sc[k] = sc[k + 1];
  }
  v4u bf[16];
  float scale, ymlp[4];
  {
    const float p = sc[0];
#define M(U) if (U >= DM) s.issue<U, U>(60);
    REP8(M)
#undef M
    {
      const v4u e = aread<28>();
      unsigned mb = e[0], lb = e[1];
      if (!(ABL & 1) && !__all(mb != SENT && lb != SENT)) {
        for (unsigned spins = 0; spins < SPIN_CAP; ++spins) {
          __builtin_amdgcn_s_sleep(2);
          const v2u t = ld64sc1_wait(lane * 8, mlb);
          mb = t[0]; lb = t[1];
          if (__all(mb != SENT && lb != SENT)) break;
        }
      }
      const float mi = __uint_as_float(mb), li = __uint_as_float(lb);
      const float M = wave_max(mi);
      const float L = wave_sum(li * __builtin_amdgcn_exp2f((mi - M) * LOG2E));
      scale = __builtin_amdgcn_exp2f((mw - M) * LOG2E) / L;
    }
#define M(T) { const v4u A = aread<8 + 2 * T>(), B = aread<9 + 2 * T>(); _Pragma("unroll") for (int k = 0; k < 4; ++k) { \
      bf[(T >> 2) * 8 + 2 * k][T & 3] = plo(B[k], A[k]); bf[(T >> 2) * 8 + 2 * k + 1][T & 3] = phi(B[k], A[k]); } }
    REP8(M)
#undef M
    {
      for (unsigned spins = 0; Lcnt[0] != 8u && spins < SPIN_CAP; ++spins) __builtin_amdgcn_s_sleep(1);
      asm volatile("" ::: "memory");
      const int f = lane >> 3;
      float g = 0.f, u = 0.f;
#pragma unroll
      for (int ww = 0; ww < 8; ++ww) { g += Lg[ww * 16 + f]; u += Lg[ww * 16 + 8 + f]; }
      g = rbf(g); u = rbf(u);
      float hval = rbf(rbf(1.f / (1.f + __expf(-g))) * u);
      if (!has_mlp) hval = 0.f;
      const v4u wd = aread<24>();
      float ym[8];
#pragma unroll
      for (int k = 0; k < 4; ++k) { ym[2 * k] = hval * lo16(wd[k]); ym[2 * k + 1] = hval * hi16(wd[k]); }
#pragma unroll
      for (int k = 0; k < 4; ++k) { float t = pair32(ym[k], ym[k + 4]); t = pair16(t, t); ymlp[k] = t + dpp<XOR8>(t); asm volatile("" : "+v"(ymlp[k])); }
    }
#define M(U) fma8(acc, bcast<U>(p), aget<U, 7>()); s.issue<8 + U, U>(60);
    REP8(M)
#undef M
#define M(U) fma8(acc, bcast<8 + U>(p), aget<U, 7 - U>());
    REP8(M)
#undef M
  }
  STAMP(4)
  {
    const float b0 = pair32(acc[0], acc[4]), b1 = pair32(acc[1], acc[5]), b2 = pair32(acc[2], acc[6]), b3 = pair32(acc[3], acc[7]);
    const float c0 = pair16(b0, b2), c1 = pair16(b1, b3);
    Lo[w * 68 + 4 * c + r] = cvtpk(c0 * scale, c1 * scale);
  }
  BARRIER();
  {
    const unsigned* ap = Lo + (mn & 7) * 68 + 32 * (mn >> 3) + 4 * mg;
    const v4u a0 = *(const v4u*)ap, a1 = *(const v4u*)(ap + 16);
    float y[8];
#pragma unroll
    for (int cc = 0; cc < 8; ++cc) {
      f32x4 dd = {0.f, 0.f, 0.f, 0.f};
      dd = __builtin_amdgcn_mfma_f32_16x16x32_bf16(__builtin_bit_cast(bf16x8, a0), __builtin_bit_cast(bf16x8, bf[cc]), dd, 0, 0, 0);
      dd = __builtin_amdgcn_mfma_f32_16x16x32_bf16(__builtin_bit_cast(bf16x8, a1), __builtin_bit_cast(bf16x8, bf[8 + cc]), dd, 0, 0, 0);
      const float t = (dd[0] + dd[1]) + (dd[2] + dd[3]);
      y[cc] = __builtin_bit_cast(float, __builtin_amdgcn_update_dpp(__builtin_bit_cast(int, t), __builtin_bit_cast(int, t), XOR8, 0xc, 0xf, false));
    }
    v4u o;
#pragma unroll
    for (int k = 0; k < 4; ++k) { float t = pair32(y[k], y[k + 4]); t = pair16(t, t); o[k] = __float_as_uint(t + ymlp[k]) & 0xfffffffeu; }
    if (!(ABL & 8) && (lane & 24) == 0) st128sc1((4 * w + (cg >> 1)) * 16384 + chunk * 64 + (cg & 1) * 32 + (lane >> 5) * 16, ws + WS_Y, o);
  }
  if (!has_mlp) {
    unsigned xv;
    ld16(xv, (lane & 15) * 2, (u64)xp + head * 32);
    const u64 yb = ws + WS_Y + head * 16384 + w * 2048;
    v4u y0, y1;
    for (unsigned spins = 0; spins < SPIN_CAP; ++spins) {
      asm volatile("global_load_dwordx4 %0, %3, %4 sc1\n\tglobal_load_dwordx4 %1, %3, %4 offset:1024 sc1\n\ts_waitcnt vmcnt(0)"
                   : "=&v"(y0), "=&v"(y1), "+v"(xv) : "v"(lane * 16), "s"(yb) : "memory");
      bool ok = true;
#pragma unroll
      for (int k = 0; k < 4; ++k) ok = ok && (y0[k] != SENT) && (y1[k] != SENT);
      if (__all(ok)) break;
      if (POLL_SLEEP) __builtin_amdgcn_s_sleep(POLL_SLEEP);
    }
    float v[4];
#pragma unroll
    for (int k = 0; k < 4; ++k) v[k] = __uint_as_float(y0[k]) + __uint_as_float(y1[k]);
    float t = pair16(pair32(v[0], v[2]), pair32(v[1], v[3]));
    t += dpp<XOR8>(t); t += dpp<ROR4>(t);
    if ((lane & 12) == 0) Lr[w * 16 + 4 * (lane & 3) + (lane >> 4)] = t;
    BARRIER();
    if (w == 0 && lane < 16) {
      float sum = 0.f;
#pragma unroll
      for (int ww = 0; ww < 8; ++ww) sum += Lr[ww * 16 + lane];
      const float o = lo16(xv) + sum;
      st16(lane * 2, (u64)outp + head * 32, cvtpk(o, o));
      if (head == 0 && lane == 0) st64sc1(0u, ws + WS_EPOCH, tag_lo, tag_hi);
    }
  }
}

extern "C" void kernel_launch(void* const* d_in, const int* in_sizes, int n_in, void* d_out, int out_size, void* d_ws, size_t ws_size,
                              hipStream_t stream) {
  fused_decode<<<NWG, WAVES * 64, 0, stream>>>(d_in[0], d_in[1], d_in[2], d_in[3], d_in[4], d_in[6], d_in[7], d_in[8], d_ws, d_out);
}
```

```cpp
#include <hip/hip_runtime.h>
#include <hip/hip_bf16.h>

#define WPOL ""
#define KVPOL "nt"
#define PRIO 2
#define FB 1
#define FB_T1 40
#define FB_T2 100
#define FB_HALF 1
#define TICKET 3
#define ABL 0
#define POLL_SLEEP 0
#define BASEMODE 0

typedef unsigned v4u __attribute__((ext_vector_type(4)));
typedef unsigned v2u __attribute__((ext_vector_type(2)));
typedef float f32x4 __attribute__((ext_vector_type(4)));
typedef float f32x2 __attribute__((ext_vector_type(2)));
typedef __bf16 bf16x2 __attribute__((ext_vector_type(2)));
typedef __bf16 bf16x8 __attribute__((ext_vector_type(8)));
typedef unsigned long long u64;
#define LAS __attribute__((address_space(3)))
#define STAMP(k)

constexpr int NWG = 256, WAVES = 8;
#define W0V 3
constexpr int W0 = W0V;
constexpr int DM = 5;
constexpr unsigned WS_EPOCH = 0, WS_FLAG = 256, WS_ML = 4096, WS_CNT = 32768, WS_Y = 65536;
constexpr unsigned SENT = 0xffffffffu;
constexpr unsigned SPIN_CAP = 1u << 20;

__device__ __forceinline__ void ld128w(v4u& dst, unsigned voff, u64 sbase) {
  asm volatile("global_load_dwordx4 %0, %1, %2 " WPOL : "=v"(dst) : "v"(voff), "s"(sbase) : "memory");
}
__device__ __forceinline__ void ld16(unsigned& dst, unsigned voff, u64 sbase) {
  asm volatile("global_load_ushort %0, %1, %2" : "=v"(dst) : "v"(voff), "s"(sbase) : "memory");
}

#define ACLOB "a0","a1","a2","a3","a4","a5","a6","a7","a8","a9","a10","a11","a12","a13","a14","a15","a16","a17","a18","a19","a20","a21","a22","a23","a24","a25","a26","a27","a28","a29","a30","a31", \
  "a32","a33","a34","a35","a36","a37","a38","a39","a40","a41","a42","a43","a44","a45","a46","a47","a48","a49","a50","a51","a52","a53","a54","a55","a56","a57","a58","a59","a60","a61","a62","a63", \
  "a64","a65","a66","a67","a68","a69","a70","a71","a72","a73","a74","a75","a76","a77","a78","a79","a80","a81","a82","a83","a84","a85","a86","a87","a88","a89","a90","a91","a92","a93","a94","a95", \
  "a96","a97","a98","a99","a100","a101","a102","a103","a104","a105","a106","a107","a108","a109","a110","a111","a112","a113","a114","a115"
template <int SLOT, int OFF> __device__ __forceinline__ void ald(unsigned voff, u64 sbase) {
  asm volatile("global_load_dwordx4 a[%c2:%c3], %0, %1 offset:%c4 " KVPOL :: "v"(voff), "s"(sbase), "i"(4 * SLOT), "i"(4 * SLOT + 3), "i"(OFF) : "memory", ACLOB);
}
template <int SLOT, int OFF> __device__ __forceinline__ void aldw(unsigned voff, u64 sbase) {
  asm volatile("global_load_dwordx4 a[%c2:%c3], %0, %1 offset:%c4 " WPOL :: "v"(voff), "s"(sbase), "i"(4 * SLOT), "i"(4 * SLOT + 3), "i"(OFF) : "memory", ACLOB);
}
__device__ __forceinline__ void ald_gu(unsigned voff, u64 gbase, u64 ubase, int idx) {
  const u64 mask = 0xffull << (8 * idx);
  asm volatile("s_mov_b64 exec, %3\n\tglobal_load_dwordx4 a[100:103], %0, %1 " WPOL "\n\tglobal_load_dwordx4 a[104:107], %0, %2 " WPOL "\n\ts_mov_b64 exec, -1"
               :: "v"(voff), "s"(gbase), "s"(ubase), "s"(mask) : "memory", ACLOB);
}
template <int REG> __device__ __forceinline__ void ald64sc1(unsigned voff, u64 sbase) {
  asm volatile("global_load_dwordx2 a[%c2:%c3], %0, %1 sc1" :: "v"(voff), "s"(sbase), "i"(REG), "i"(REG + 1) : "memory", ACLOB);
}
__device__ __forceinline__ void aticket(unsigned zero, u64 sbase) {
  asm volatile("s_mov_b64 exec, 1\n\tglobal_load_dword a115, %0, %1\n\ts_mov_b64 exec, -1" :: "v"(zero), "s"(sbase) : "memory", ACLOB);
}
template <int RSLOT, int N> __device__ __forceinline__ v4u aget(void) {
  unsigned a, b, c, d;
  asm volatile("s_waitcnt vmcnt(%c4)\n\tv_accvgpr_read_b32 %0, a%c5\n\tv_accvgpr_read_b32 %1, a%c6\n\tv_accvgpr_read_b32 %2, a%c7\n\tv_accvgpr_read_b32 %3, a%c8"
               : "=v"(a), "=v"(b), "=v"(c), "=v"(d) : "i"(N), "i"(4 * RSLOT), "i"(4 * RSLOT + 1), "i"(4 * RSLOT + 2), "i"(4 * RSLOT + 3) : "memory", ACLOB);
  return v4u{a, b, c, d};
}
template <int RSLOT, int N5, int N4, int N3, int WSLOT, int OFF> __device__ __forceinline__ v4u astep(unsigned voff, u64 sbase, int mode) {
  unsigned a, b, c, d;
  asm volatile("s_waitcnt vmcnt(%c7)\n\t"
               "s_cmp_gt_i32 %6, %c17\n\ts_cbranch_scc1 1f\n\t"
               "s_waitcnt vmcnt(%c8)\n\t"
               "s_cmp_gt_i32 %6, %c18\n\ts_cbranch_scc1 1f\n\t"
               "s_waitcnt vmcnt(%c9)\n"
               "1:\n\tglobal_load_dwordx4 a[%c14:%c15], %4, %5 offset:%c16 " KVPOL "\n\t"
               "v_accvgpr_read_b32 %0, a%c10\n\tv_accvgpr_read_b32 %1, a%c11\n\tv_accvgpr_read_b32 %2, a%c12\n\tv_accvgpr_read_b32 %3, a%c13"
               : "=&v"(a), "=&v"(b), "=&v"(c), "=&v"(d)
               : "v"(voff), "s"(sbase), "s"(mode), "i"(N5), "i"(N4), "i"(N3),
                 "i"(4 * RSLOT), "i"(4 * RSLOT + 1), "i"(4 * RSLOT + 2), "i"(4 * RSLOT + 3), "i"(4 * WSLOT), "i"(4 * WSLOT + 3), "i"(OFF),
                 "i"(FB_T2), "i"(FB_T1)
               : "memory", "scc", ACLOB);
  return v4u{a, b, c, d};
}
template <int N, int WSLOT, int OFF> __device__ __forceinline__ void apre(unsigned voff, u64 sbase) {
  asm volatile("s_waitcnt vmcnt(%c2)\n\tglobal_load_dwordx4 a[%c3:%c4], %0, %1 offset:%c5 " KVPOL
               :: "v"(voff), "s"(sbase), "i"(N), "i"(4 * WSLOT), "i"(4 * WSLOT + 3), "i"(OFF) : "memory", ACLOB);
}
template <int RSLOT> __device__ __forceinline__ v4u aread(void) {
  unsigned a, b, c, d;
  asm volatile("v_accvgpr_read_b32 %0, a%c4\n\tv_accvgpr_read_b32 %1, a%c5\n\tv_accvgpr_read_b32 %2, a%c6\n\tv_accvgpr_read_b32 %3, a%c7\n\ts_nop 1"
               : "=v"(a), "=v"(b), "=v"(c), "=v"(d) : "i"(4 * RSLOT), "i"(4 * RSLOT + 1), "i"(4 * RSLOT + 2), "i"(4 * RSLOT + 3) : "memory", ACLOB);
  return v4u{a, b, c, d};
}
__device__ __forceinline__ unsigned aread_ticket(void) {
  unsigned a;
  asm volatile("v_accvgpr_read_b32 %0, a115\n\ts_nop 1" : "=v"(a) :: "memory", ACLOB);
  return a;
}
__device__ __forceinline__ void st128sc1(unsigned voff, u64 sbase, const v4u& v) {
  asm volatile("global_store_dwordx4 %0, %1, %2 sc1\n\ts_nop 1" :: "v"(voff), "v"(v), "s"(sbase) : "memory");
}
__device__ __forceinline__ void st64sc1(unsigned voff, u64 sbase, unsigned lo, unsigned hi) {
  const v2u v = {lo, hi};
  asm volatile("global_store_dwordx2 %0, %1, %2 sc1" :: "v"(voff), "v"(v), "s"(sbase) : "memory");
}
__device__ __forceinline__ void st16(unsigned voff, u64 sbase, unsigned v) {
  asm volatile("global_store_short %0, %1, %2" :: "v"(voff), "v"(v), "s"(sbase) : "memory");
}
__device__ __forceinline__ v2u ld64sc1_wait(unsigned voff, u64 sbase) {
  v2u v;
  asm volatile("global_load_dwordx2 %0, %1, %2 sc1\n\ts_waitcnt vmcnt(0)" : "=&v"(v) : "v"(voff), "s"(sbase) : "memory");
  return v;
}

__device__ __forceinline__ float dot2(unsigned a, unsigned b, float acc) {
  return __builtin_amdgcn_fdot2_f32_bf16(__builtin_bit_cast(bf16x2, a), __builtin_bit_cast(bf16x2, b), acc, false);
}
__device__ __forceinline__ float lo16(unsigned v) { return __builtin_bit_cast(float, v << 16); }
__device__ __forceinline__ float hi16(unsigned v) { return __builtin_bit_cast(float, v & 0xffff0000u); }
template <int CTRL> __device__ __forceinline__ float dpp(float x) {
  return __builtin_bit_cast(float, __builtin_amdgcn_mov_dpp(__builtin_bit_cast(int, x), CTRL, 0xf, 0xf, true));
}
template <int CTRL> __device__ __forceinline__ unsigned dppu(unsigned x) { return (unsigned)__builtin_amdgcn_mov_dpp((int)x, CTRL, 0xf, 0xf, true); }
constexpr int XOR1 = 0xB1, XOR2 = 0x4E, XOR7 = 0x141, XOR8 = 0x128, ROR4 = 0x124;
__device__ __forceinline__ unsigned cvtpk(float lo, float hi) {
  const bf16x2 v = {(__bf16)lo, (__bf16)hi};
  return __builtin_bit_cast(unsigned, v);
}
__device__ __forceinline__ float rbf(float x) { return lo16(cvtpk(x, x)); }
__device__ __forceinline__ float pair32(float a, float b) {
  auto s = __builtin_amdgcn_permlane32_swap(__float_as_uint(a), __float_as_uint(b), false, false);
  return __uint_as_float(s[0]) + __uint_as_float(s[1]);
}
__device__ __forceinline__ float pair16(float a, float b) {
  auto s = __builtin_amdgcn_permlane16_swap(__float_as_uint(a), __float_as_uint(b), false, false);
  return __uint_as_float(s[0]) + __uint_as_float(s[1]);
}
__device__ __forceinline__ float wave_sum(float x) {
  x += dpp<XOR1>(x); x += dpp<XOR2>(x); x += dpp<XOR7>(x); x += dpp<XOR8>(x);
  x = pair16(x, x); x = pair32(x, x); return x;
}
__device__ __forceinline__ float wave_max(float x) {
  x = fmaxf(x, dpp<XOR1>(x)); x = fmaxf(x, dpp<XOR2>(x)); x = fmaxf(x, dpp<XOR7>(x)); x = fmaxf(x, dpp<XOR8>(x));
  auto s = __builtin_amdgcn_permlane16_swap(__float_as_uint(x), __float_as_uint(x), false, false);
  x = fmaxf(__uint_as_float(s[0]), __uint_as_float(s[1]));
  auto t = __builtin_amdgcn_permlane32_swap(__float_as_uint(x), __float_as_uint(x), false, false);
  return fmaxf(__uint_as_float(t[0]), __uint_as_float(t[1]));
}
__device__ __forceinline__ float treduce16(float (&x)[16], int c) {
#define TR_STEP(HALF, CTRL, BIT) _Pragma("unroll") for (int h = 0; h < (HALF); ++h) { \
    const float lo_ = x[h], hi_ = x[h + (HALF)]; const float t1_ = lo_ + dpp<CTRL>(lo_), t2_ = hi_ + dpp<CTRL>(hi_); x[h] = (c & (BIT)) ? t2_ : t1_; }
  TR_STEP(8, XOR8, 8) TR_STEP(4, XOR7, 4) TR_STEP(2, XOR2, 2) TR_STEP(1, XOR1, 1)
#undef TR_STEP
  return x[0];
}
template <int T> __device__ __forceinline__ float bcast(float p) { return dpp<0x150 + T>(p); }
__device__ __forceinline__ void fma8(float (&acc)[8], float p, const v4u& v) {
#pragma unroll
  for (int k = 0; k < 4; ++k) { acc[2 * k] = fmaf(p, lo16(v[k]), acc[2 * k]); acc[2 * k + 1] = fmaf(p, hi16(v[k]), acc[2 * k + 1]); }
}
__device__ __forceinline__ unsigned plo(unsigned hi_row, unsigned lo_row) { return __builtin_amdgcn_perm(hi_row, lo_row, 0x05040100u); }
__device__ __forceinline__ unsigned phi(unsigned hi_row, unsigned lo_row) { return __builtin_amdgcn_perm(hi_row, lo_row, 0x07060302u); }

constexpr int younger(int U, int X, int NA, int NB) { return (U + DM - 1 - X) + ((U >= 1 && X <= DM) ? NA : 0) + ((U >= 9 && X <= 8 + DM) ? NB : 0); }
struct Stream {
  u64 K, V; unsigned voff; int rot;
  __device__ __forceinline__ u64 base(int round) const { return ((round < 32) ? K : V) + (u64)(unsigned)(((round + rot) & 31) << 15); }
  template <int J, int SLOT> __device__ __forceinline__ void issue(int round0) const { ald<SLOT, (J & 3) * 1024>(voff, base(round0 + (J >> 2))); }
  template <int U, int NA, int NB> __device__ __forceinline__ v4u step(int round0, int mode) const {
    return astep<(U & 7), younger(U, U, NA, NB), younger(U, U + 1, NA, NB), younger(U, U + 2, NA, NB), ((U + DM) & 7), ((U + DM) & 3) * 1024>(
        voff, base(round0 + ((U + DM) >> 2)), mode);
  }
};
__device__ __forceinline__ float qk(const v4u& k, const v4u& q) { return dot2(k[3], q[3], dot2(k[2], q[2], dot2(k[1], q[1], dot2(k[0], q[0], 0.f)))); }
#define REP16(M) M(0) M(1) M(2) M(3) M(4) M(5) M(6) M(7) M(8) M(9) M(10) M(11) M(12) M(13) M(14) M(15)
#define REP15(M) M(1) M(2) M(3) M(4) M(5) M(6) M(7) M(8) M(9) M(10) M(11) M(12) M(13) M(14) M(15)
#define REP8(M) M(0) M(1) M(2) M(3) M(4) M(5) M(6) M(7)
#define BARRIER() asm volatile("s_waitcnt lgkmcnt(0)\n\ts_barrier" ::: "memory")

constexpr int WQ0 = 4, QPOST = W0 + 8 - WQ0, QGET = 10, XSTEP = 8;
static_assert(QGET > QPOST && QGET < 16 - W0 && XSTEP + WQ0 >= 8 && XSTEP + W0 < 16, "tile-0 schedule");
constexpr int t0_ext(int m) { return m < 0 ? 2 * WQ0 : (m + WQ0 < 8 ? 2 : (m == XSTEP ? 4 : 0)); }
constexpr int t0_wait(int n) { int c = W0 - 1; for (int m = (n - W0 < -1 ? -1 : n - W0); m <= n - 1; ++m) c += t0_ext(m); return c; }

__device__ __forceinline__ float fin_score(float d) { return rbf(rbf(d) * 0.08838834764831845f); }
constexpr float LOG2E = 1.4426950408889634f;

__global__ void __launch_bounds__(WAVES * 64, 2) fused_decode(const void* xp, const void* Kp, const void* wqp, const void* Vp, const void* wgp,
                                                             const void* wup, const void* wdp, const void* wop, void* wsp, void* outp) {
  __shared__ __attribute__((aligned(16))) float lds[8 * 128 + 8 * 16 + 8 * 68 + 8 * 16 + 4];
  float* Lq = lds;
  float* Lg = lds + 1024;
  unsigned* Lo = (unsigned*)(lds + 1024 + 128);
  float* Lr = lds + 1024 + 128 + 8 * 68;
  volatile LAS unsigned* Lcnt = (volatile LAS unsigned*)(Lr + 128);
  STAMP(0)
  const int tid = threadIdx.x, lane = tid & 63, w = __builtin_amdgcn_readfirstlane(tid >> 6), r = lane >> 4, c = lane & 15;
  const int b = blockIdx.x, xcd = b & 7, i8 = b >> 3;
  const int head = xcd * 4 + (i8 & 3), split = i8 >> 2, chunk = head * 8 + split;
  const bool has_mlp = i8 < 28;
  const int j = xcd * 28 + (has_mlp ? i8 : 27);
  const u64 ws = (u64)wsp;
  const u64 mlb = ws + WS_ML + head * 512;

  Stream s;
  { const u64 co = ((u64)chunk << 20) + (u64)(w << 12); s.K = (u64)Kp + co; s.V = (u64)Vp + co; s.voff = lane * 16; s.rot = b & 31; }
  v4u x8; unsigned xs; v4u wq[16];
  const unsigned wq_vo = r * 8192 + c * 16; const u64 wq_sb = (u64)wqp + (u64)w * (64 * 8192) + head * 256;
#define WQ_PAIR(P) { ld128w(wq[2 * (P)], wq_vo, wq_sb + (u64)(2 * (P)) * (4 * 8192)); ld128w(wq[2 * (P) + 1], wq_vo, wq_sb + (u64)(2 * (P) + 1) * (4 * 8192)); }
  ld128w(x8, lane * 16, (u64)xp);
  ld16(xs, (4 * c + r) * 2, (u64)xp + w * 128);
  s.issue<0, 0>(0); s.issue<1, 1>(0); s.issue<2, 2>(0);
  __builtin_amdgcn_sched_barrier(0);
  const u64 tstart = __builtin_amdgcn_s_memrealtime();
  if (W0 > 3) s.issue<3, 3>(0); if (W0 > 4) s.issue<4, 4>(0);
  WQ_PAIR(0) WQ_PAIR(1) WQ_PAIR(2) WQ_PAIR(3)
  if (tid == 0) { Lcnt[0] = 0u; Lcnt[1] = 0u; }
  BARRIER();
  const int mg = lane >> 4, mn = lane & 15, cg = lane & 7, rowsel = (lane >> 3) & 1;
  const unsigned gu_vo = (4 * c + r) * 3584;
  const u64 g_sb = (u64)wgp + (u64)w * (64 * 3584) + j * 16, u_sb = (u64)wup + (u64)w * (64 * 3584) + j * 16;
  const u64 wo_sb = (u64)wop + (u64)head * (128 * 1024) + w * 128;
  float xn; unsigned xpair;
  float qa[8] = {0.f, 0.f, 0.f, 0.f, 0.f, 0.f, 0.f, 0.f};
  v4u q;
  float sc[8];
  float d[16];
#define PRE(N) apre<t0_wait(N), ((N) + W0 < 16 ? (N) + W0 : ((N) + W0) & 7), (((N) + W0) & 3) * 1024>(s.voff, s.base(((N) + W0) >> 2));
#define QP(P) { asm volatile("" : "+v"(wq[2 * (P)]), "+v"(wq[2 * (P) + 1])); const unsigned xx = dppu<0x150 + 2 * (P)>(xpair); \
      _Pragma("unroll") for (int k = 0; k < 4; ++k) { qa[2 * k] = dot2(plo(wq[2 * (P) + 1][k], wq[2 * (P)][k]), xx, qa[2 * k]); \
        qa[2 * k + 1] = dot2(phi(wq[2 * (P) + 1][k], wq[2 * (P)][k]), xx, qa[2 * k + 1]); } }
#define KQ(U) d[U] = qk(aread<U>(), q);
#define M(N) PRE(N) \
    if ((N) + WQ0 < 8) WQ_PAIR(((N) + WQ0) & 7) \
    if ((N) == XSTEP) { ald_gu(gu_vo, g_sb, u_sb, 0); ald64sc1<108>(0u, ws + WS_EPOCH); \
                        aldw<24, 0>((lane >> 3) * 1024 + cg * 16, (u64)wdp + (u64)j * 8192 + w * 128); } \
    if ((N) == 0) { \
        \
      asm volatile("" : "+v"(x8), "+v"(xs)); \
      float xv[8]; \
      _Pragma("unroll") for (int k = 0; k < 4; ++k) { xv[2 * k] = lo16(x8[k]); xv[2 * k + 1] = hi16(x8[k]); } \
      float sum = 0.f; \
      _Pragma("unroll") for (int k = 0; k < 8; ++k) sum += xv[k]; \
      const float mean = wave_sum(sum) * (1.f / 512.f); \
      float sq = 0.f; \
      _Pragma("unroll") for (int k = 0; k < 8; ++k) { const float dlt = xv[k] - mean; sq = fmaf(dlt, dlt, sq); } \
      const float var = wave_sum(sq) * (1.f / 512.f); \
      xn = rbf(rbf(lo16(xs) - rbf(mean)) / rbf(sqrtf(rbf(rbf(var) + 1e-5f)))); \
      const unsigned xb = __float_as_uint(xn); \
      xpair = phi(dppu<XOR1>(xb), xb);     \
      STAMP(1) \
    } \
    if ((N) == W0) { QP(0) QP(1) QP(2) QP(3) } \
    if ((N) > W0 && (N) - W0 - 1 + WQ0 < 8) QP(((N) - W0 - 1 + WQ0) & 7) \
    if ((N) == QPOST) { \
      const float b0 = pair32(qa[0], qa[4]), b1 = pair32(qa[1], qa[5]), b2 = pair32(qa[2], qa[6]), b3 = pair32(qa[3], qa[7]); \
      const float c0 = pair16(b0, b2), c1 = pair16(b1, b3); \
      *(f32x2*)(Lq + w * 128 + 8 * c + 2 * r) = f32x2{c0, c1};     \
      asm volatile("s_waitcnt lgkmcnt(0)" ::: "memory"); \
      if (lane == 0) __hip_atomic_fetch_add((LAS unsigned*)(Lcnt + 1), 1u, __ATOMIC_RELAXED, __HIP_MEMORY_SCOPE_WORKGROUP); \
    } \
    if ((N) == QGET) { \
      for (unsigned spins = 0; Lcnt[1] != 8u && spins < SPIN_CAP; ++spins) __builtin_amdgcn_s_sleep(1); \
      asm volatile("" ::: "memory"); \
      f32x4 a0 = *(const f32x4*)(Lq + (2 * r) * 128 + 8 * c), a1 = *(const f32x4*)(Lq + (2 * r) * 128 + 8 * c + 4); \
      f32x4 b0 = *(const f32x4*)(Lq + (2 * r + 1) * 128 + 8 * c), b1 = *(const f32x4*)(Lq + (2 * r + 1) * 128 + 8 * c + 4); \
      a0 += b0; a1 += b1; \
      float v[8] = {a0[0], a0[1], a0[2], a0[3], a1[0], a1[1], a1[2], a1[3]}; \
      _Pragma("unroll") for (int k = 0; k < 8; ++k) { v[k] = pair16(v[k], v[k]); v[k] = pair32(v[k], v[k]); } \
      _Pragma("unroll") for (int k = 0; k < 4; ++k) q[k] = cvtpk(v[2 * k], v[2 * k + 1]); \
      STAMP(2) \
      KQ(0) KQ(1) KQ(2) KQ(3) \
    } \
    if ((N) == QGET + 1) { KQ(4) KQ(5) KQ(6) KQ(7) KQ(8) KQ(9) KQ(10) } \
    if ((N) > QGET) KQ(N) \
    __builtin_amdgcn_sched_barrier(0);
  REP16(M)
#undef M
  sc[7] = fin_score(treduce16(d, c));
  if (W0 < 5) { PRE(16) } if (W0 < 4) { PRE(17) }
  STAMP(5)
  unsigned tag_lo, tag_hi;
  {
    const v4u e = aread<27>();
    const u64 tag = (((u64)(unsigned)__builtin_amdgcn_readfirstlane(e[1]) << 32) | (u64)(unsigned)__builtin_amdgcn_readfirstlane(e[0])) + 1ull;
    tag_lo = (unsigned)tag; tag_hi = (unsigned)(tag >> 32);
    asm volatile("v_accvgpr_write_b32 a114, 1" ::: ACLOB);
  }
  const bool clearer = !has_mlp && w == 0;
  if (clearer && !(ABL & 4)) {
    const v4u sv = {SENT, SENT, SENT, SENT};
    for (int i = 0; i < 16; ++i) st128sc1(lane * 16 + i * 1024, ws + WS_Y + head * 16384, sv);
    if (lane < 32) st128sc1(lane * 16, mlb, sv);
    if (lane == 0) st64sc1(0u, ws + WS_CNT + head * 128, 0u, 0u);
  }
  const unsigned wo_vo = (64 * rowsel + 8 * mg) * 1024 + cg * 16;
  const u64 cnt_sb = ws + WS_CNT + ((i8 + 4 * w) & 31) * 128;
  constexpr int MODE0 = BASEMODE == 0 ? -1000 : (BASEMODE == 1 ? FB_T1 + 1 : FB_T2 + 1);
  int mode = MODE0;
#define SCH_T0 600
#define SCH_K 472
#define SCH_V 440
#define FEEDBACK(TILE) if (FB) { const int now_ = (int)(unsigned)(__builtin_amdgcn_s_memrealtime() - tstart); \
    mode = now_ - ((TILE) <= 8 ? SCH_T0 + (TILE) * SCH_K : SCH_T0 + 8 * SCH_K + ((TILE) - 8) * SCH_V); }
#define SETPRIO(TILE) if (PRIO == 2) { if (((w >> 2) ^ (TILE)) & 1) __builtin_amdgcn_s_setprio(1); else __builtin_amdgcn_s_setprio(0); }
  for (int tile = 1; tile < 8; ++tile) {
    SETPRIO(tile)
    if (FB_HALF) mode = MODE0;
    d[0] = qk(s.step<0, 3, 2>(tile * 4, mode), q);
    switch (tile) {
      case 1: aldw<8, 0>(wo_vo, wo_sb); aldw<9, 1024>(wo_vo, wo_sb); aldw<10, 2048>(wo_vo, wo_sb); break;
      case 2: aldw<11, 3072>(wo_vo, wo_sb); aldw<12, 0>(wo_vo, wo_sb + 4096); aldw<13, 1024>(wo_vo, wo_sb + 4096); break;
      case 3: aldw<14, 2048>(wo_vo, wo_sb + 4096); aldw<15, 3072>(wo_vo, wo_sb + 4096); ald64sc1<110>((lane & 31) * 8, ws + WS_FLAG); break;
      case 4: aldw<16, 0>(wo_vo, wo_sb + 32768); aldw<17, 1024>(wo_vo, wo_sb + 32768); aticket(0u, cnt_sb); break;
      case 5: aldw<18, 2048>(wo_vo, wo_sb + 32768); aldw<19, 3072>(wo_vo, wo_sb + 32768); aticket(0u, cnt_sb); break;
      case 6: aldw<20, 0>(wo_vo, wo_sb + 36864); aldw<21, 1024>(wo_vo, wo_sb + 36864); aticket(0u, cnt_sb); break;
      default: aldw<22, 2048>(wo_vo, wo_sb + 36864); aldw<23, 3072>(wo_vo, wo_sb + 36864); aticket(0u, cnt_sb); break;
    }
#define M(U) d[U] = qk(s.step<U, 3, 2>(tile * 4, mode), q); \
    if (U == 7) { \
      { FEEDBACK(tile) } \
        \
      if (tile == 1 && clearer && lane == 0) st64sc1(0u, ws + WS_FLAG + head * 8, tag_lo, tag_hi); \
    } \
    if (U == 8) ald_gu(gu_vo, g_sb, u_sb, tile);
    REP15(M)
#undef M
#pragma unroll
    for (int k = 0; k < 7; ++k) sc[k] = sc[k + 1];
    sc[7] = fin_score(treduce16(d, c));
    if (tile == 1) { STAMP(6) }
    if (tile == 3) {
      const v4u e = aread<27>();
      if (!__all(e[2] == tag_lo && e[3] == tag_hi)) {
        for (unsigned spins = 0; spins < SPIN_CAP; ++spins) {
          __builtin_amdgcn_s_sleep(2);
          const v2u f = ld64sc1_wait((lane & 31) * 8, ws + WS_FLAG);
          if (__all(f[0] == tag_lo && f[1] == tag_hi)) break;
        }
      }
    }
  }
  STAMP(3)
  float mw;
  {
    float m = sc[0];
#pragma unroll
    for (int k = 1; k < 8; ++k) m = fmaxf(m, sc[k]);
    mw = wave_max(m);
    float l = 0.f;
#pragma unroll
    for (int k = 0; k < 8; ++k) { sc[k] = __builtin_amdgcn_exp2f((sc[k] - mw) * LOG2E); l += sc[k]; }
    const float lw = wave_sum(l);
    if (!(ABL & 1) && lane == 0) st64sc1(0u, mlb + (split * 8 + w) * 8, __float_as_uint(mw) & 0xfffffffeu, __float_as_uint(lw) & 0xfffffffeu);
  }
  float acc[8] = {0.f, 0.f, 0.f, 0.f, 0.f, 0.f, 0.f, 0.f};
  for (int tile = 8; tile < 15; ++tile) {
    SETPRIO(tile)
    if (FB_HALF) mode = MODE0;
    const float p = sc[0];
#define M(U) fma8(acc, bcast<U>(p), s.step<U, 1, 0>(tile * 4, mode)); \
    if (U == 0) { aticket(0u, cnt_sb); if (!(ABL & 1) && tile == 14) ald64sc1<112>(lane * 8, mlb); } \
    if (U == 2 && tile == 8) { \
      const v4u wg = aread<25>(), wu = aread<26>(); \
      float g[16]; \
      _Pragma("unroll") for (int k = 0; k < 4; ++k) { g[2 * k] = xn * lo16(wg[k]); g[2 * k + 1] = xn * hi16(wg[k]); g[8 + 2 * k] = xn * lo16(wu[k]); g[8 + 2 * k + 1] = xn * hi16(wu[k]); } \
      float t = treduce16(g, c); \
      t = pair16(t, t); t = pair32(t, t); \
      if (r == 0) Lg[w * 16 + c] = t; \
      asm volatile("s_waitcnt lgkmcnt(0)" ::: "memory"); \
      if (lane == 0) __hip_atomic_fetch_add((LAS unsigned*)Lcnt, 1u, __ATOMIC_RELAXED, __HIP_MEMORY_SCOPE_WORKGROUP); \
    } \
    if (U == 7) { FEEDBACK(tile) }
    REP16(M)
#undef M
#pragma unroll
    for (int k = 0; k < 7; ++k) sc[k] = sc[k + 1];
  }
  v4u bf[16];
  float scale, ymlp[4];
  {
    const float p = sc[0];
#define M(U) if (U >= DM) s.issue<U, U>(60);
    REP8(M)
#undef M
    {
      const v4u e = aread<28>();
      unsigned mb = e[0], lb = e[1];
      if (!(ABL & 1) && !__all(mb != SENT && lb != SENT)) {
        for (unsigned spins = 0; spins < SPIN_CAP; ++spins) {
          __builtin_amdgcn_s_sleep(2);
          const v2u t = ld64sc1_wait(lane * 8, mlb);
          mb = t[0]; lb = t[1];
          if (__all(mb != SENT && lb != SENT)) break;
        }
      }
      const float mi = __uint_as_float(mb), li = __uint_as_float(lb);
      const float M = wave_max(mi);
      const float L = wave_sum(li * __builtin_amdgcn_exp2f((mi - M) * LOG2E));
      scale = __builtin_amdgcn_exp2f((mw - M) * LOG2E) / L;
    }
#define M(T) { const v4u A = aread<8 + 2 * T>(), B = aread<9 + 2 * T>(); _Pragma("unroll") for (int k = 0; k < 4; ++k) { \
      bf[(T >> 2) * 8 + 2 * k][T & 3] = plo(B[k], A[k]); bf[(T >> 2) * 8 + 2 * k + 1][T & 3] = phi(B[k], A[k]); } }
    REP8(M)
#undef M
    {
      for (unsigned spins = 0; Lcnt[0] != 8u && spins < SPIN_CAP; ++spins) __builtin_amdgcn_s_sleep(1);
      asm volatile("" ::: "memory");
      const int f = lane >> 3;
      float g = 0.f, u = 0.f;
#pragma unroll
      for (int ww = 0; ww < 8; ++ww) { g += Lg[ww * 16 + f]; u += Lg[ww * 16 + 8 + f]; }
      g = rbf(g); u = rbf(u);
      float hval = rbf(rbf(1.f / (1.f + __expf(-g))) * u);
      if (!has_mlp) hval = 0.f;
      const v4u wd = aread<24>();
      float ym[8];
#pragma unroll
      for (int k = 0; k < 4; ++k) { ym[2 * k] = hval * lo16(wd[k]); ym[2 * k + 1] = hval * hi16(wd[k]); }
#pragma unroll
      for (int k = 0; k < 4; ++k) { float t = pair32(ym[k], ym[k + 4]); t = pair16(t, t); ymlp[k] = t + dpp<XOR8>(t); asm volatile("" : "+v"(ymlp[k])); }
    }
#define M(U) fma8(acc, bcast<U>(p), aget<U, 7>()); s.issue<8 + U, U>(60);
    REP8(M)
#undef M
#define M(U) fma8(acc, bcast<8 + U>(p), aget<U, 7 - U>());
    REP8(M)
#undef M
  }
  STAMP(4)
  {
    const float b0 = pair32(acc[0], acc[4]), b1 = pair32(acc[1], acc[5]), b2 = pair32(acc[2], acc[6]), b3 = pair32(acc[3], acc[7]);
    const float c0 = pair16(b0, b2), c1 = pair16(b1, b3);
    Lo[w * 68 + 4 * c + r] = cvtpk(c0 * scale, c1 * scale);
  }
  BARRIER();
  {
    const unsigned* ap = Lo + (mn & 7) * 68 + 32 * (mn >> 3) + 4 * mg;
    const v4u a0 = *(const v4u*)ap, a1 = *(const v4u*)(ap + 16);
    float y[8];
#pragma unroll
    for (int cc = 0; cc < 8; ++cc) {
      f32x4 dd = {0.f, 0.f, 0.f, 0.f};
      dd = __builtin_amdgcn_mfma_f32_16x16x32_bf16(__builtin_bit_cast(bf16x8, a0), __builtin_bit_cast(bf16x8, bf[cc]), dd, 0, 0, 0);
      dd = __builtin_amdgcn_mfma_f32_16x16x32_bf16(__builtin_bit_cast(bf16x8, a1), __builtin_bit_cast(bf16x8, bf[8 + cc]), dd, 0, 0, 0);
      const float t = (dd[0] + dd[1]) + (dd[2] + dd[3]);
      y[cc] = __builtin_bit_cast(float, __builtin_amdgcn_update_dpp(__builtin_bit_cast(int, t), __builtin_bit_cast(int, t), XOR8, 0xc, 0xf, false));
    }
    v4u o;
#pragma unroll
    for (int k = 0; k < 4; ++k) { float t = pair32(y[k], y[k + 4]); t = pair16(t, t); o[k] = __float_as_uint(t + ymlp[k]) & 0xfffffffeu; }
    if (!(ABL & 8) && (lane & 24) == 0) st128sc1((4 * w + (cg >> 1)) * 16384 + chunk * 64 + (cg & 1) * 32 + (lane >> 5) * 16, ws + WS_Y, o);
  }
  if (!has_mlp) {
    unsigned xv;
    ld16(xv, (lane & 15) * 2, (u64)xp + head * 32);
    const u64 yb = ws + WS_Y + head * 16384 + w * 2048;
    v4u y0, y1;
    for (unsigned spins = 0; spins < SPIN_CAP; ++spins) {
      asm volatile("global_load_dwordx4 %0, %3, %4 sc1\n\tglobal_load_dwordx4 %1, %3, %4 offset:1024 sc1\n\ts_waitcnt vmcnt(0)"
                   : "=&v"(y0), "=&v"(y1), "+v"(xv) : "v"(lane * 16), "s"(yb) : "memory");
      bool ok = true;
#pragma unroll
      for (int k = 0; k < 4; ++k) ok = ok && (y0[k] != SENT) && (y1[k] != SENT);
      if (__all(ok)) break;
      if (POLL_SLEEP) __builtin_amdgcn_s_sleep(POLL_SLEEP);
    }
    float v[4];
#pragma unroll
    for (int k = 0; k < 4; ++k) v[k] = __uint_as_float(y0[k]) + __uint_as_float(y1[k]);
    float t = pair16(pair32(v[0], v[2]), pair32(v[1], v[3]));
    t += dpp<XOR8>(t); t += dpp<ROR4>(t);
    if ((lane & 12) == 0) Lr[w * 16 + 4 * (lane & 3) + (lane >> 4)] = t;
    BARRIER();
    if (w == 0 && lane < 16) {
      float sum = 0.f;
#pragma unroll
      for (int ww = 0; ww < 8; ++ww) sum += Lr[ww * 16 + lane];
      const float o = lo16(xv) + sum;
      st16(lane * 2, (u64)outp + head * 32, cvtpk(o, o));
      if (head == 0 && lane == 0) st64sc1(0u, ws + WS_EPOCH, tag_lo, tag_hi);
    }
  }
}

extern "C" void kernel_launch(void* const* d_in, const int* in_sizes, int n_in, void* d_out, int out_size, void* d_ws, size_t ws_size,
                              hipStream_t stream) {
  fused_decode<<<NWG, WAVES * 64, 0, stream>>>(d_in[0], d_in[1], d_in[3], d_in[2], d_in[6], d_in[7], d_in[8], d_in[4], d_ws, d_out);
}
```

```cpp
#include <hip/hip_runtime.h>
#include <hip/hip_bf16.h>

#define WPOL ""
#define KVPOL "nt"
#define PRIO 2
#define FB 1
#define FB_T0 -100
#define FB_T1 0
#define FB_T2 80
#define FB_HALF 1
#define TICKET 3
#define ABL 0
#define POLL_SLEEP 0
#define SCH_T0 600
#define SCH_K 458
#define SCH_V 432
#define ANCH 1
#define ANCH_EVERY 4
#define ANCH_OFF 50
#define LASTL 0
#define BASEMODE 0

typedef unsigned v4u __attribute__((ext_vector_type(4)));
typedef unsigned v2u __attribute__((ext_vector_type(2)));
typedef float f32x4 __attribute__((ext_vector_type(4)));
typedef float f32x2 __attribute__((ext_vector_type(2)));
typedef __bf16 bf16x2 __attribute__((ext_vector_type(2)));
typedef __bf16 bf16x8 __attribute__((ext_vector_type(8)));
typedef unsigned long long u64;
#define LAS __attribute__((address_space(3)))
#define STAMP(k)

constexpr int NWG = 256, WAVES = 8;
#define W0V 3
constexpr int W0 = W0V;
constexpr int DM = 5;
constexpr unsigned WS_EPOCH = 0, WS_FLAG = 256, WS_ML = 4096, WS_CNT = 32768, WS_Y = 65536;
constexpr unsigned SENT = 0xffffffffu;
constexpr unsigned SPIN_CAP = 1u << 20;

__device__ __forceinline__ void ld128w(v4u& dst, unsigned voff, u64 sbase) {
  asm volatile("global_load_dwordx4 %0, %1, %2 " WPOL : "=v"(dst) : "v"(voff), "s"(sbase) : "memory");
}
__device__ __forceinline__ void ld16(unsigned& dst, unsigned voff, u64 sbase) {
  asm volatile("global_load_ushort %0, %1, %2" : "=v"(dst) : "v"(voff), "s"(sbase) : "memory");
}

#define ACLOB "a0","a1","a2","a3","a4","a5","a6","a7","a8","a9","a10","a11","a12","a13","a14","a15","a16","a17","a18","a19","a20","a21","a22","a23","a24","a25","a26","a27","a28","a29","a30","a31", \
  "a32","a33","a34","a35","a36","a37","a38","a39","a40","a41","a42","a43","a44","a45","a46","a47","a48","a49","a50","a51","a52","a53","a54","a55","a56","a57","a58","a59","a60","a61","a62","a63", \
  "a64","a65","a66","a67","a68","a69","a70","a71","a72","a73","a74","a75","a76","a77","a78","a79","a80","a81","a82","a83","a84","a85","a86","a87","a88","a89","a90","a91","a92","a93","a94","a95", \
  "a96","a97","a98","a99","a100","a101","a102","a103","a104","a105","a106","a107","a108","a109","a110","a111","a112","a113","a114","a115"
template <int SLOT, int OFF> __device__ __forceinline__ void ald(unsigned voff, u64 sbase) {
  asm volatile("global_load_dwordx4 a[%c2:%c3], %0, %1 offset:%c4 " KVPOL :: "v"(voff), "s"(sbase), "i"(4 * SLOT), "i"(4 * SLOT + 3), "i"(OFF) : "memory", ACLOB);
}
template <int SLOT, int OFF> __device__ __forceinline__ void aldw(unsigned voff, u64 sbase) {
  asm volatile("global_load_dwordx4 a[%c2:%c3], %0, %1 offset:%c4 " WPOL :: "v"(voff), "s"(sbase), "i"(4 * SLOT), "i"(4 * SLOT + 3), "i"(OFF) : "memory", ACLOB);
}
__device__ __forceinline__ void ald_gu(unsigned voff, u64 gbase, u64 ubase, int idx) {
  const u64 mask = 0xffull << (8 * idx);
  asm volatile("s_mov_b64 exec, %3\n\tglobal_load_dwordx4 a[100:103], %0, %1 " WPOL "\n\tglobal_load_dwordx4 a[104:107], %0, %2 " WPOL "\n\ts_mov_b64 exec, -1"
               :: "v"(voff), "s"(gbase), "s"(ubase), "s"(mask) : "memory", ACLOB);
}
template <int REG> __device__ __forceinline__ void ald64sc1(unsigned voff, u64 sbase) {
  asm volatile("global_load_dwordx2 a[%c2:%c3], %0, %1 sc1" :: "v"(voff), "s"(sbase), "i"(REG), "i"(REG + 1) : "memory", ACLOB);
}
__device__ __forceinline__ void aanchor(unsigned voff, u64 sbase) {
  asm volatile("global_load_dword a115, %0, %1 sc1" :: "v"(voff), "s"(sbase) : "memory", ACLOB);
}
__device__ __forceinline__ void adummy(unsigned zero, u64 sbase) {
  asm volatile("s_mov_b64 exec, 1\n\tglobal_load_dword a114, %0, %1\n\ts_mov_b64 exec, -1" :: "v"(zero), "s"(sbase) : "memory", ACLOB);
}
__device__ __forceinline__ void st32sc1(unsigned voff, u64 sbase, unsigned v) {
  asm volatile("global_store_dword %0, %1, %2 sc1" :: "v"(voff), "v"(v), "s"(sbase) : "memory");
}
template <int RSLOT, int N> __device__ __forceinline__ v4u aget(void) {
  unsigned a, b, c, d;
  asm volatile("s_waitcnt vmcnt(%c4)\n\tv_accvgpr_read_b32 %0, a%c5\n\tv_accvgpr_read_b32 %1, a%c6\n\tv_accvgpr_read_b32 %2, a%c7\n\tv_accvgpr_read_b32 %3, a%c8"
               : "=v"(a), "=v"(b), "=v"(c), "=v"(d) : "i"(N), "i"(4 * RSLOT), "i"(4 * RSLOT + 1), "i"(4 * RSLOT + 2), "i"(4 * RSLOT + 3) : "memory", ACLOB);
  return v4u{a, b, c, d};
}
template <int RSLOT, int N5, int N4, int N3, int N2, int WSLOT, int OFF> __device__ __forceinline__ v4u astep(unsigned voff, u64 sbase, int mode) {
  unsigned a, b, c, d;
  asm volatile("s_waitcnt vmcnt(%c7)\n\t"
               "s_cmp_gt_i32 %6, %c17\n\ts_cbranch_scc1 1f\n\t"
               "s_waitcnt vmcnt(%c8)\n\t"
               "s_cmp_gt_i32 %6, %c18\n\ts_cbranch_scc1 1f\n\t"
               "s_waitcnt vmcnt(%c9)\n\t"
               "s_cmp_gt_i32 %6, %c19\n\ts_cbranch_scc1 1f\n\t"
               "s_waitcnt vmcnt(%c20)\n"
               "1:\n\tglobal_load_dwordx4 a[%c14:%c15], %4, %5 offset:%c16 " KVPOL "\n\t"
               "v_accvgpr_read_b32 %0, a%c10\n\tv_accvgpr_read_b32 %1, a%c11\n\tv_accvgpr_read_b32 %2, a%c12\n\tv_accvgpr_read_b32 %3, a%c13"
               : "=&v"(a), "=&v"(b), "=&v"(c), "=&v"(d)
               : "v"(voff), "s"(sbase), "s"(mode), "i"(N5), "i"(N4), "i"(N3),
                 "i"(4 * RSLOT), "i"(4 * RSLOT + 1), "i"(4 * RSLOT + 2), "i"(4 * RSLOT + 3), "i"(4 * WSLOT), "i"(4 * WSLOT + 3), "i"(OFF),
                 "i"(FB_T2), "i"(FB_T1), "i"(FB_T0), "i"(N2)
               : "memory", "scc", ACLOB);
  return v4u{a, b, c, d};
}
template <int N, int WSLOT, int OFF> __device__ __forceinline__ void apre(unsigned voff, u64 sbase) {
  asm volatile("s_waitcnt vmcnt(%c2)\n\tglobal_load_dwordx4 a[%c3:%c4], %0, %1 offset:%c5 " KVPOL
               :: "v"(voff), "s"(sbase), "i"(N), "i"(4 * WSLOT), "i"(4 * WSLOT + 3), "i"(OFF) : "memory", ACLOB);
}
template <int RSLOT> __device__ __forceinline__ v4u aread(void) {
  unsigned a, b, c, d;
  asm volatile("v_accvgpr_read_b32 %0, a%c4\n\tv_accvgpr_read_b32 %1, a%c5\n\tv_accvgpr_read_b32 %2, a%c6\n\tv_accvgpr_read_b32 %3, a%c7\n\ts_nop 1"
               : "=v"(a), "=v"(b), "=v"(c), "=v"(d) : "i"(4 * RSLOT), "i"(4 * RSLOT + 1), "i"(4 * RSLOT + 2), "i"(4 * RSLOT + 3) : "memory", ACLOB);
  return v4u{a, b, c, d};
}
__device__ __forceinline__ unsigned aread_ticket(void) {
  unsigned a;
  asm volatile("v_accvgpr_read_b32 %0, a115\n\ts_nop 1" : "=v"(a) :: "memory", ACLOB);
  return a;
}
__device__ __forceinline__ void st128sc1(unsigned voff, u64 sbase, const v4u& v) {
  asm volatile("global_store_dwordx4 %0, %1, %2 sc1\n\ts_nop 1" :: "v"(voff), "v"(v), "s"(sbase) : "memory");
}
__device__ __forceinline__ void st64sc1(unsigned voff, u64 sbase, unsigned lo, unsigned hi) {
  const v2u v = {lo, hi};
  asm volatile("global_store_dwordx2 %0, %1, %2 sc1" :: "v"(voff), "v"(v), "s"(sbase) : "memory");
}
__device__ __forceinline__ void st16(unsigned voff, u64 sbase, unsigned v) {
  asm volatile("global_store_short %0, %1, %2" :: "v"(voff), "v"(v), "s"(sbase) : "memory");
}
__device__ __forceinline__ v2u ld64sc1_wait(unsigned voff, u64 sbase) {
  v2u v;
  asm volatile("global_load_dwordx2 %0, %1, %2 sc1\n\ts_waitcnt vmcnt(0)" : "=&v"(v) : "v"(voff), "s"(sbase) : "memory");
  return v;
}

__device__ __forceinline__ float dot2(unsigned a, unsigned b, float acc) {
  return __builtin_amdgcn_fdot2_f32_bf16(__builtin_bit_cast(bf16x2, a), __builtin_bit_cast(bf16x2, b), acc, false);
}
__device__ __forceinline__ float lo16(unsigned v) { return __builtin_bit_cast(float, v << 16); }
__device__ __forceinline__ float hi16(unsigned v) { return __builtin_bit_cast(float, v & 0xffff0000u); }
template <int CTRL> __device__ __forceinline__ float dpp(float x) {
  return __builtin_bit_cast(float, __builtin_amdgcn_mov_dpp(__builtin_bit_cast(int, x), CTRL, 0xf, 0xf, true));
}
template <int CTRL> __device__ __forceinline__ unsigned dppu(unsigned x) { return (unsigned)__builtin_amdgcn_mov_dpp((int)x, CTRL, 0xf, 0xf, true); }
constexpr int XOR1 = 0xB1, XOR2 = 0x4E, XOR7 = 0x141, XOR8 = 0x128, ROR4 = 0x124;
__device__ __forceinline__ unsigned cvtpk(float lo, float hi) {
  const bf16x2 v = {(__bf16)lo, (__bf16)hi};
  return __builtin_bit_cast(unsigned, v);
}
__device__ __forceinline__ float rbf(float x) { return lo16(cvtpk(x, x)); }
__device__ __forceinline__ float pair32(float a, float b) {
  auto s = __builtin_amdgcn_permlane32_swap(__float_as_uint(a), __float_as_uint(b), false, false);
  return __uint_as_float(s[0]) + __uint_as_float(s[1]);
}
__device__ __forceinline__ float pair16(float a, float b) {
  auto s = __builtin_amdgcn_permlane16_swap(__float_as_uint(a), __float_as_uint(b), false, false);
  return __uint_as_float(s[0]) + __uint_as_float(s[1]);
}
__device__ __forceinline__ float wave_sum(float x) {
  x += dpp<XOR1>(x); x += dpp<XOR2>(x); x += dpp<XOR7>(x); x += dpp<XOR8>(x);
  x = pair16(x, x); x = pair32(x, x); return x;
}
__device__ __forceinline__ float wave_max(float x) {
  x = fmaxf(x, dpp<XOR1>(x)); x = fmaxf(x, dpp<XOR2>(x)); x = fmaxf(x, dpp<XOR7>(x)); x = fmaxf(x, dpp<XOR8>(x));
  auto s = __builtin_amdgcn_permlane16_swap(__float_as_uint(x), __float_as_uint(x), false, false);
  x = fmaxf(__uint_as_float(s[0]), __uint_as_float(s[1]));
  auto t = __builtin_amdgcn_permlane32_swap(__float_as_uint(x), __float_as_uint(x), false, false);
  return fmaxf(__uint_as_float(t[0]), __uint_as_float(t[1]));
}
__device__ __forceinline__ float treduce16(float (&x)[16], int c) {
#define TR_STEP(HALF, CTRL, BIT) _Pragma("unroll") for (int h = 0; h < (HALF); ++h) { \
    const float lo_ = x[h], hi_ = x[h + (HALF)]; const float t1_ = lo_ + dpp<CTRL>(lo_), t2_ = hi_ + dpp<CTRL>(hi_); x[h] = (c & (BIT)) ? t2_ : t1_; }
  TR_STEP(8, XOR8, 8) TR_STEP(4, XOR7, 4) TR_STEP(2, XOR2, 2) TR_STEP(1, XOR1, 1)
#undef TR_STEP
  return x[0];
}
template <int T> __device__ __forceinline__ float bcast(float p) { return dpp<0x150 + T>(p); }
__device__ __forceinline__ void fma8(float (&acc)[8], float p, const v4u& v) {
#pragma unroll
  for (int k = 0; k < 4; ++k) { acc[2 * k] = fmaf(p, lo16(v[k]), acc[2 * k]); acc[2 * k + 1] = fmaf(p, hi16(v[k]), acc[2 * k + 1]); }
}
__device__ __forceinline__ unsigned plo(unsigned hi_row, unsigned lo_row) { return __builtin_amdgcn_perm(hi_row, lo_row, 0x05040100u); }
__device__ __forceinline__ unsigned phi(unsigned hi_row, unsigned lo_row) { return __builtin_amdgcn_perm(hi_row, lo_row, 0x07060302u); }

constexpr int younger(int U, int X, int NA, int NB) { return (U + DM - 1 - X) + ((U >= 1 && X <= DM) ? NA : 0) + ((U >= 9 && X <= 8 + DM) ? NB : 0); }
struct Stream {
  u64 K, V; unsigned voff; int rot;
  __device__ __forceinline__ u64 base(int round) const { return ((round < 32) ? K : V) + (u64)(unsigned)(((round + rot) & 31) << 15); }
  template <int J, int SLOT> __device__ __forceinline__ void issue(int round0) const { ald<SLOT, (J & 3) * 1024>(voff, base(round0 + (J >> 2))); }
  template <int U, int NA, int NB> __device__ __forceinline__ v4u step(int round0, int mode) const {
    return astep<(U & 7), younger(U, U, NA, NB), younger(U, U + 1, NA, NB), younger(U, U + 2, NA, NB), younger(U, U + 3, NA, NB), ((U + DM) & 7), ((U + DM) & 3) * 1024>(
        voff, base(round0 + ((U + DM) >> 2)), mode);
  }
};
__device__ __forceinline__ float qk(const v4u& k, const v4u& q) { return dot2(k[3], q[3], dot2(k[2], q[2], dot2(k[1], q[1], dot2(k[0], q[0], 0.f)))); }
#define REP16(M) M(0) M(1) M(2) M(3) M(4) M(5) M(6) M(7) M(8) M(9) M(10) M(11) M(12) M(13) M(14) M(15)
#define REP15(M) M(1) M(2) M(3) M(4) M(5) M(6) M(7) M(8) M(9) M(10) M(11) M(12) M(13) M(14) M(15)
#define REP8(M) M(0) M(1) M(2) M(3) M(4) M(5) M(6) M(7)
#define BARRIER() asm volatile("s_waitcnt lgkmcnt(0)\n\ts_barrier" ::: "memory")

constexpr int WQ0 = 4, QPOST = W0 + 8 - WQ0, QGET = 10, XSTEP = 8;
static_assert(QGET > QPOST && QGET < 16 - W0 && XSTEP + WQ0 >= 8 && XSTEP + W0 < 16, "tile-0 schedule");
constexpr int t0_ext(int m) { return m < 0 ? 2 * WQ0 : (m + WQ0 < 8 ? 2 : (m == XSTEP ? 4 : 0)); }
constexpr int t0_wait(int n) { int c = W0 - 1; for (int m = (n - W0 < -1 ? -1 : n - W0); m <= n - 1; ++m) c += t0_ext(m); return c; }

__device__ __forceinline__ float fin_score(float d) { return rbf(rbf(d) * 0.08838834764831845f); }
constexpr float LOG2E = 1.4426950408889634f;

__global__ void __launch_bounds__(WAVES * 64, 2) fused_decode(const void* xp, const void* Kp, const void* wqp, const void* Vp, const void* wgp,
                                                             const void* wup, const void* wdp, const void* wop, void* wsp, void* outp) {
  __shared__ __attribute__((aligned(16))) float lds[8 * 128 + 8 * 16 + 8 * 68 + 8 * 16 + 4];
  float* Lq = lds;
  float* Lg = lds + 1024;
  unsigned* Lo = (unsigned*)(lds + 1024 + 128);
  float* Lr = lds + 1024 + 128 + 8 * 68;
  volatile LAS unsigned* Lcnt = (volatile LAS unsigned*)(Lr + 128);
  STAMP(0)
  const int tid = threadIdx.x, lane = tid & 63, w = __builtin_amdgcn_readfirstlane(tid >> 6), r = lane >> 4, c = lane & 15;
  const int b = blockIdx.x, xcd = b & 7, i8 = b >> 3;
  const int head = xcd * 4 + (i8 & 3), split = i8 >> 2, chunk = head * 8 + split;
  const bool has_mlp = i8 < 28;
  const int j = xcd * 28 + (has_mlp ? i8 : 27);
  const u64 ws = (u64)wsp;
  const u64 mlb = ws + WS_ML + head * 512;

  Stream s;
  { const u64 co = ((u64)chunk << 20) + (u64)(w << 12); s.K = (u64)Kp + co; s.V = (u64)Vp + co; s.voff = lane * 16; s.rot = b & 31; }
  v4u x8; unsigned xs; v4u wq[16];
  const unsigned wq_vo = r * 8192 + c * 16; const u64 wq_sb = (u64)wqp + (u64)w * (64 * 8192) + head * 256;
#define WQ_PAIR(P) { ld128w(wq[2 * (P)], wq_vo, wq_sb + (u64)(2 * (P)) * (4 * 8192)); ld128w(wq[2 * (P) + 1], wq_vo, wq_sb + (u64)(2 * (P) + 1) * (4 * 8192)); }
  ld128w(x8, lane * 16, (u64)xp);
  ld16(xs, (4 * c + r) * 2, (u64)xp + w * 128);
  s.issue<0, 0>(0); s.issue<1, 1>(0); s.issue<2, 2>(0);
  __builtin_amdgcn_sched_barrier(0);
  const u64 tstart = __builtin_amdgcn_s_memrealtime();
  if (W0 > 3) s.issue<3, 3>(0); if (W0 > 4) s.issue<4, 4>(0);
  WQ_PAIR(0) WQ_PAIR(1) WQ_PAIR(2) WQ_PAIR(3)
  if (tid == 0) { Lcnt[0] = 0u; Lcnt[1] = 0u; Lcnt[2] = (unsigned)tstart + SCH_T0; }
  BARRIER();
  const int mg = lane >> 4, mn = lane & 15, cg = lane & 7, rowsel = (lane >> 3) & 1;
  const unsigned gu_vo = (4 * c + r) * 3584;
  const u64 g_sb = (u64)wgp + (u64)w * (64 * 3584) + j * 16, u_sb = (u64)wup + (u64)w * (64 * 3584) + j * 16;
  const u64 wo_sb = (u64)wop + (u64)head * (128 * 1024) + w * 128;
  float xn; unsigned xpair;
  float qa[8] = {0.f, 0.f, 0.f, 0.f, 0.f, 0.f, 0.f, 0.f};
  v4u q;
  float sc[8];
  float d[16];
#define PRE(N) apre<t0_wait(N), ((N) + W0 < 16 ? (N) + W0 : ((N) + W0) & 7), (((N) + W0) & 3) * 1024>(s.voff, s.base(((N) + W0) >> 2));
#define QP(P) { asm volatile("" : "+v"(wq[2 * (P)]), "+v"(wq[2 * (P) + 1])); const unsigned xx = dppu<0x150 + 2 * (P)>(xpair); \
      _Pragma("unroll") for (int k = 0; k < 4; ++k) { qa[2 * k] = dot2(plo(wq[2 * (P) + 1][k], wq[2 * (P)][k]), xx, qa[2 * k]); \
        qa[2 * k + 1] = dot2(phi(wq[2 * (P) + 1][k], wq[2 * (P)][k]), xx, qa[2 * k + 1]); } }
#define KQ(U) d[U] = qk(aread<U>(), q);
#define M(N) PRE(N) \
    if ((N) + WQ0 < 8) WQ_PAIR(((N) + WQ0) & 7) \
    if ((N) == XSTEP) { ald_gu(gu_vo, g_sb, u_sb, 0); ald64sc1<108>(0u, ws + WS_EPOCH); \
                        aldw<24, 0>((lane >> 3) * 1024 + cg * 16, (u64)wdp + (u64)j * 8192 + w * 128); } \
    if ((N) == 0) { \
        \
      asm volatile("" : "+v"(x8), "+v"(xs)); \
      float xv[8]; \
      _Pragma("unroll") for (int k = 0; k < 4; ++k) { xv[2 * k] = lo16(x8[k]); xv[2 * k + 1] = hi16(x8[k]); } \
      float sum = 0.f; \
      _Pragma("unroll") for (int k = 0; k < 8; ++k) sum += xv[k]; \
      const float mean = wave_sum(sum) * (1.f / 512.f); \
      float sq = 0.f; \
      _Pragma("unroll") for (int k = 0; k < 8; ++k) { const float dlt = xv[k] - mean; sq = fmaf(dlt, dlt, sq); } \
      const float var = wave_sum(sq) * (1.f / 512.f); \
      xn = rbf(rbf(lo16(xs) - rbf(mean)) / rbf(sqrtf(rbf(rbf(var) + 1e-5f)))); \
      const unsigned xb = __float_as_uint(xn); \
      xpair = phi(dppu<XOR1>(xb), xb);     \
      STAMP(1) \
    } \
    if ((N) == W0) { QP(0) QP(1) QP(2) QP(3) } \
    if ((N) > W0 && (N) - W0 - 1 + WQ0 < 8) QP(((N) - W0 - 1 + WQ0) & 7) \
    if ((N) == QPOST) { \
      const float b0 = pair32(qa[0], qa[4]), b1 = pair32(qa[1], qa[5]), b2 = pair32(qa[2], qa[6]), b3 = pair32(qa[3], qa[7]); \
      const float c0 = pair16(b0, b2), c1 = pair16(b1, b3); \
      *(f32x2*)(Lq + w * 128 + 8 * c + 2 * r) = f32x2{c0, c1};     \
      asm volatile("s_waitcnt lgkmcnt(0)" ::: "memory"); \
      if (lane == 0) __hip_atomic_fetch_add((LAS unsigned*)(Lcnt + 1), 1u, __ATOMIC_RELAXED, __HIP_MEMORY_SCOPE_WORKGROUP); \
    } \
    if ((N) == QGET) { \
      for (unsigned spins = 0; Lcnt[1] != 8u && spins < SPIN_CAP; ++spins) __builtin_amdgcn_s_sleep(1); \
      asm volatile("" ::: "memory"); \
      f32x4 a0 = *(const f32x4*)(Lq + (2 * r) * 128 + 8 * c), a1 = *(const f32x4*)(Lq + (2 * r) * 128 + 8 * c + 4); \
      f32x4 b0 = *(const f32x4*)(Lq + (2 * r + 1) * 128 + 8 * c), b1 = *(const f32x4*)(Lq + (2 * r + 1) * 128 + 8 * c + 4); \
      a0 += b0; a1 += b1; \
      float v[8] = {a0[0], a0[1], a0[2], a0[3], a1[0], a1[1], a1[2], a1[3]}; \
      _Pragma("unroll") for (int k = 0; k < 8; ++k) { v[k] = pair16(v[k], v[k]); v[k] = pair32(v[k], v[k]); } \
      _Pragma("unroll") for (int k = 0; k < 4; ++k) q[k] = cvtpk(v[2 * k], v[2 * k + 1]); \
      STAMP(2) \
      KQ(0) KQ(1) KQ(2) KQ(3) \
    } \
    if ((N) == QGET + 1) { KQ(4) KQ(5) KQ(6) KQ(7) KQ(8) KQ(9) KQ(10) } \
    if ((N) > QGET) KQ(N) \
    __builtin_amdgcn_sched_barrier(0);
  REP16(M)
#undef M
  sc[7] = fin_score(treduce16(d, c));
  if (W0 < 5) { PRE(16) } if (W0 < 4) { PRE(17) }
  STAMP(5)
  unsigned tag_lo, tag_hi;
  {
    const v4u e = aread<27>();
    const u64 tag = (((u64)(unsigned)__builtin_amdgcn_readfirstlane(e[1]) << 32) | (u64)(unsigned)__builtin_amdgcn_readfirstlane(e[0])) + 1ull;
    tag_lo = (unsigned)tag; tag_hi = (unsigned)(tag >> 32);
    asm volatile("v_accvgpr_write_b32 a114, 1" ::: ACLOB);
  }
  const bool clearer = !has_mlp && w == 0;
  if (clearer && !(ABL & 4)) {
    const v4u sv = {SENT, SENT, SENT, SENT};
    for (int i = 0; i < 16; ++i) st128sc1(lane * 16 + i * 1024, ws + WS_Y + head * 16384, sv);
    if (lane < 32) st128sc1(lane * 16, mlb, sv);
    if (lane < 16) st32sc1(lane * 1024 + xcd * 128 + (head & 3) * 4, ws + WS_CNT, SENT);
  }
  const unsigned wo_vo = (64 * rowsel + 8 * mg) * 1024 + cg * 16;
  constexpr int MODE0 = FB_T1;
  int mode = MODE0;
#define NOMINAL(TILE) ((TILE) <= 8 ? (TILE) * SCH_K : 8 * SCH_K + ((TILE) - 8) * SCH_V)
#define ANCHOR_LD(TILE) if (ANCH && w == 0 && ((TILE) % ANCH_EVERY) == 0) aanchor(((lane >> 2) & 7) * 128 + (lane & 3) * 4 + ((TILE) - 2) * 1024, ws + WS_CNT); else adummy(0u, (u64)xp);
#define FEEDBACK(TILE) if (FB) { const unsigned now_ = (unsigned)__builtin_amdgcn_s_memrealtime(); \
    if (ANCH && w == 0) { \
      if (clearer && (TILE) >= 2 && (((TILE) + 2) % ANCH_EVERY) == 0 && lane == 0) st32sc1((TILE) * 1024 + xcd * 128 + (head & 3) * 4, ws + WS_CNT, now_ & 0xfffffffeu); \
      if ((TILE) >= 4 && ((TILE) % ANCH_EVERY) == 0) { const unsigned a_ = aread_ticket(); const bool ok_ = a_ != SENT; \
        const float sum_ = wave_sum(ok_ ? (float)(int)(a_ - now_) : 0.f); const int n_ = __popcll(__ballot(ok_)); \
        if (n_ >= 32) Lcnt[2] = now_ + (unsigned)((int)(sum_ / (float)n_) - NOMINAL((TILE) - 2) + ANCH_OFF); } } \
    mode = (int)(now_ - (unsigned)__builtin_amdgcn_readfirstlane((int)Lcnt[2])) - NOMINAL(TILE); \
    if (ANCH && (TILE) < 4 && mode < FB_T1) mode = FB_T1; }
#define FEEDBACK2(TILE) if (FB) { const unsigned now_ = (unsigned)__builtin_amdgcn_s_memrealtime(); \
    mode = (int)(now_ - (unsigned)__builtin_amdgcn_readfirstlane((int)Lcnt[2])) - NOMINAL(TILE) - ((TILE) < 8 ? SCH_K / 2 : SCH_V / 2); \
    if (ANCH && (TILE) < 4 && mode < FB_T1) mode = FB_T1; }
#define SETPRIO(TILE) if (PRIO == 2) { if (((w >> 2) ^ (TILE)) & 1) __builtin_amdgcn_s_setprio(1); else __builtin_amdgcn_s_setprio(0); }
  for (int tile = 1; tile < 8; ++tile) {
    SETPRIO(tile)
    d[0] = qk(s.step<0, 3, 2>(tile * 4, mode), q);
    switch (tile) {
      case 1: aldw<8, 0>(wo_vo, wo_sb); aldw<9, 1024>(wo_vo, wo_sb); aldw<10, 2048>(wo_vo, wo_sb); break;
      case 2: aldw<11, 3072>(wo_vo, wo_sb); aldw<12, 0>(wo_vo, wo_sb + 4096); aldw<13, 1024>(wo_vo, wo_sb + 4096); break;
      case 3: aldw<14, 2048>(wo_vo, wo_sb + 4096); aldw<15, 3072>(wo_vo, wo_sb + 4096); ald64sc1<110>((lane & 31) * 8, ws + WS_FLAG); break;
      case 4: aldw<16, 0>(wo_vo, wo_sb + 32768); aldw<17, 1024>(wo_vo, wo_sb + 32768); ANCHOR_LD(tile) break;
      case 5: aldw<18, 2048>(wo_vo, wo_sb + 32768); aldw<19, 3072>(wo_vo, wo_sb + 32768); ANCHOR_LD(tile) break;
      case 6: aldw<20, 0>(wo_vo, wo_sb + 36864); aldw<21, 1024>(wo_vo, wo_sb + 36864); ANCHOR_LD(tile) break;
      default: aldw<22, 2048>(wo_vo, wo_sb + 36864); aldw<23, 3072>(wo_vo, wo_sb + 36864); ANCHOR_LD(tile) break;
    }
#define M(U) d[U] = qk(s.step<U, 3, 2>(tile * 4, mode), q); \
    if (U == 7) { \
      { FEEDBACK(tile) } \
        \
      if (tile == 1 && clearer && lane == 0) st64sc1(0u, ws + WS_FLAG + head * 8, tag_lo, tag_hi); \
    } \
    if (U == 15) { FEEDBACK2(tile) } \
    if (U == 8) ald_gu(gu_vo, g_sb, u_sb, tile);
    REP15(M)
#undef M
#pragma unroll
    for (int k = 0; k < 7; ++k) sc[k] = sc[k + 1];
    sc[7] = fin_score(treduce16(d, c));
    if (tile == 1) { STAMP(6) }
    if (tile == 3) {
      const v4u e = aread<27>();
      if (!__all(e[2] == tag_lo && e[3] == tag_hi)) {
        for (unsigned spins = 0; spins < SPIN_CAP; ++spins) {
          __builtin_amdgcn_s_sleep(2);
          const v2u f = ld64sc1_wait((lane & 31) * 8, ws + WS_FLAG);
          if (__all(f[0] == tag_lo && f[1] == tag_hi)) break;
        }
      }
    }
  }
  STAMP(3)
  float mw;
  {
    float m = sc[0];
#pragma unroll
    for (int k = 1; k < 8; ++k) m = fmaxf(m, sc[k]);
    mw = wave_max(m);
    float l = 0.f;
#pragma unroll
    for (int k = 0; k < 8; ++k) { sc[k] = __builtin_amdgcn_exp2f((sc[k] - mw) * LOG2E); l += sc[k]; }
    const float lw = wave_sum(l);
    if (!(ABL & 1) && lane == 0) st64sc1(0u, mlb + (split * 8 + w) * 8, __float_as_uint(mw) & 0xfffffffeu, __float_as_uint(lw) & 0xfffffffeu);
  }
  float acc[8] = {0.f, 0.f, 0.f, 0.f, 0.f, 0.f, 0.f, 0.f};
  for (int tile = 8; tile < 15; ++tile) {
    SETPRIO(tile)
    const float p = sc[0];
#define M(U) fma8(acc, bcast<U>(p), s.step<U, 1, 0>(tile * 4, mode)); \
    if (U == 0) { ANCHOR_LD(tile) if (!(ABL & 1) && tile == 14) ald64sc1<112>(lane * 8, mlb); } \
    if (U == 2 && tile == 8) { \
      const v4u wg = aread<25>(), wu = aread<26>(); \
      float g[16]; \
      _Pragma("unroll") for (int k = 0; k < 4; ++k) { g[2 * k] = xn * lo16(wg[k]); g[2 * k + 1] = xn * hi16(wg[k]); g[8 + 2 * k] = xn * lo16(wu[k]); g[8 + 2 * k + 1] = xn * hi16(wu[k]); } \
      float t = treduce16(g, c); \
      t = pair16(t, t); t = pair32(t, t); \
      if (r == 0) Lg[w * 16 + c] = t; \
      asm volatile("s_waitcnt lgkmcnt(0)" ::: "memory"); \
      if (lane == 0) __hip_atomic_fetch_add((LAS unsigned*)Lcnt, 1u, __ATOMIC_RELAXED, __HIP_MEMORY_SCOPE_WORKGROUP); \
    } \
    if (U == 7) { FEEDBACK(tile) } \
    if (U == 15) { FEEDBACK2(tile) }
    REP16(M)
#undef M
#pragma unroll
    for (int k = 0; k < 7; ++k) sc[k] = sc[k + 1];
  }
  v4u bf[16];
  float scale, ymlp[4];
  {
    const float p = sc[0];
#define M(U) if (LASTL == 0 && U >= DM) s.issue<U, U>(60);
    REP8(M)
#undef M
    if (LASTL > 0) fma8(acc, bcast<0>(p), s.step<0, 0, 0>(60, mode));
    {
      const v4u e = aread<28>();
      unsigned mb = e[0], lb = e[1];
      if (!(ABL & 1) && !__all(mb != SENT && lb != SENT)) {
        for (unsigned spins = 0; spins < SPIN_CAP; ++spins) {
          __builtin_amdgcn_s_sleep(2);
          const v2u t = ld64sc1_wait(lane * 8, mlb);
          mb = t[0]; lb = t[1];
          if (__all(mb != SENT && lb != SENT)) break;
        }
      }
      const float mi = __uint_as_float(mb), li = __uint_as_float(lb);
      const float M = wave_max(mi);
      const float L = wave_sum(li * __builtin_amdgcn_exp2f((mi - M) * LOG2E));
      scale = __builtin_amdgcn_exp2f((mw - M) * LOG2E) / L;
    }
#define M(T) { const v4u A = aread<8 + 2 * T>(), B = aread<9 + 2 * T>(); _Pragma("unroll") for (int k = 0; k < 4; ++k) { \
      bf[(T >> 2) * 8 + 2 * k][T & 3] = plo(B[k], A[k]); bf[(T >> 2) * 8 + 2 * k + 1][T & 3] = phi(B[k], A[k]); } }
    REP8(M)
#undef M
    {
      for (unsigned spins = 0; Lcnt[0] != 8u && spins < SPIN_CAP; ++spins) __builtin_amdgcn_s_sleep(1);
      asm volatile("" ::: "memory");
      const int f = lane >> 3;
      float g = 0.f, u = 0.f;
#pragma unroll
      for (int ww = 0; ww < 8; ++ww) { g += Lg[ww * 16 + f]; u += Lg[ww * 16 + 8 + f]; }
      g = rbf(g); u = rbf(u);
      float hval = rbf(rbf(1.f / (1.f + __expf(-g))) * u);
      if (!has_mlp) hval = 0.f;
      const v4u wd = aread<24>();
      float ym[8];
#pragma unroll
      for (int k = 0; k < 4; ++k) { ym[2 * k] = hval * lo16(wd[k]); ym[2 * k + 1] = hval * hi16(wd[k]); }
#pragma unroll
      for (int k = 0; k < 4; ++k) { float t = pair32(ym[k], ym[k + 4]); t = pair16(t, t); ymlp[k] = t + dpp<XOR8>(t); asm volatile("" : "+v"(ymlp[k])); }
    }
#define M(U) if (U < LASTL) { if (U > 0) fma8(acc, bcast<U>(p), s.step<U, 0, 0>(60, mode)); \
                               if (U == LASTL - 1) { s.issue<LASTL + 5, (LASTL + 5) & 7>(60); s.issue<LASTL + 6, (LASTL + 6) & 7>(60); s.issue<LASTL + 7, (LASTL + 7) & 7>(60); } } \
             else { fma8(acc, bcast<U>(p), aget<U, 7>()); s.issue<8 + U, U>(60); }
    REP8(M)
#undef M
#define M(U) fma8(acc, bcast<8 + U>(p), aget<U, 7 - U>());
    REP8(M)
#undef M
  }
  STAMP(4)
  {
    const float b0 = pair32(acc[0], acc[4]), b1 = pair32(acc[1], acc[5]), b2 = pair32(acc[2], acc[6]), b3 = pair32(acc[3], acc[7]);
    const float c0 = pair16(b0, b2), c1 = pair16(b1, b3);
    Lo[w * 68 + 4 * c + r] = cvtpk(c0 * scale, c1 * scale);
  }
  BARRIER();
  {
    const unsigned* ap = Lo + (mn & 7) * 68 + 32 * (mn >> 3) + 4 * mg;
    const v4u a0 = *(const v4u*)ap, a1 = *(const v4u*)(ap + 16);
    float y[8];
#pragma unroll
    for (int cc = 0; cc < 8; ++cc) {
      f32x4 dd = {0.f, 0.f, 0.f, 0.f};
      dd = __builtin_amdgcn_mfma_f32_16x16x32_bf16(__builtin_bit_cast(bf16x8, a0), __builtin_bit_cast(bf16x8, bf[cc]), dd, 0, 0, 0);
      dd = __builtin_amdgcn_mfma_f32_16x16x32_bf16(__builtin_bit_cast(bf16x8, a1), __builtin_bit_cast(bf16x8, bf[8 + cc]), dd, 0, 0, 0);
      const float t = (dd[0] + dd[1]) + (dd[2] + dd[3]);
      y[cc] = __builtin_bit_cast(float, __builtin_amdgcn_update_dpp(__builtin_bit_cast(int, t), __builtin_bit_cast(int, t), XOR8, 0xc, 0xf, false));
    }
    v4u o;
#pragma unroll
    for (int k = 0; k < 4; ++k) { float t = pair32(y[k], y[k + 4]); t = pair16(t, t); o[k] = __float_as_uint(t + ymlp[k]) & 0xfffffffeu; }
    if (!(ABL & 8) && (lane & 24) == 0) st128sc1((4 * w + (cg >> 1)) * 16384 + chunk * 64 + (cg & 1) * 32 + (lane >> 5) * 16, ws + WS_Y, o);
  }
  if (!has_mlp) {
    unsigned xv;
    ld16(xv, (lane & 15) * 2, (u64)xp + head * 32);
    const u64 yb = ws + WS_Y + head * 16384 + w * 2048;
    v4u y0, y1;
    for (unsigned spins = 0; spins < SPIN_CAP; ++spins) {
      asm volatile("global_load_dwordx4 %0, %3, %4 sc1\n\tglobal_load_dwordx4 %1, %3, %4 offset:1024 sc1\n\ts_waitcnt vmcnt(0)"
                   : "=&v"(y0), "=&v"(y1), "+v"(xv) : "v"(lane * 16), "s"(yb) : "memory");
      bool ok = true;
#pragma unroll
      for (int k = 0; k < 4; ++k) ok = ok && (y0[k] != SENT) && (y1[k] != SENT);
      if (__all(ok)) break;
      if (POLL_SLEEP) __builtin_amdgcn_s_sleep(POLL_SLEEP);
    }
    float v[4];
#pragma unroll
    for (int k = 0; k < 4; ++k) v[k] = __uint_as_float(y0[k]) + __uint_as_float(y1[k]);
    float t = pair16(pair32(v[0], v[2]), pair32(v[1], v[3]));
    t += dpp<XOR8>(t); t += dpp<ROR4>(t);
    if ((lane & 12) == 0) Lr[w * 16 + 4 * (lane & 3) + (lane >> 4)] = t;
    BARRIER();
    if (w == 0 && lane < 16) {
      float sum = 0.f;
#pragma unroll
      for (int ww = 0; ww < 8; ++ww) sum += Lr[ww * 16 + lane];
      const float o = lo16(xv) + sum;
      st16(lane * 2, (u64)outp + head * 32, cvtpk(o, o));
      if (head == 0 && lane == 0) st64sc1(0u, ws + WS_EPOCH, tag_lo, tag_hi);
    }
  }
}

extern "C" void kernel_launch(void* const* d_in, const int* in_sizes, int n_in, void* d_out, int out_size, void* d_ws, size_t ws_size,
                              hipStream_t stream) {
  fused_decode<<<NWG, WAVES * 64, 0, stream>>>(d_in[0], d_in[1], d_in[3], d_in[2], d_in[6], d_in[7], d_in[8], d_in[4], d_ws, d_out);
}
```

```cpp
#include <hip/hip_runtime.h>
#include <hip/hip_bf16.h>

#define WPOL ""
#define KVPOL "nt"
#define PRIO 2
#define FB 1
#define FB_T1 40
#define FB_T2 100
#define FB_HALF 1
#define TICKET 3
#define ABL 0
#define POLL_SLEEP 0
#define BASEMODE 0

typedef unsigned v4u __attribute__((ext_vector_type(4)));
typedef unsigned v2u __attribute__((ext_vector_type(2)));
typedef float f32x4 __attribute__((ext_vector_type(4)));
typedef float f32x2 __attribute__((ext_vector_type(2)));
typedef __bf16 bf16x2 __attribute__((ext_vector_type(2)));
typedef __bf16 bf16x8 __attribute__((ext_vector_type(8)));
typedef unsigned long long u64;
#define LAS __attribute__((address_space(3)))
#define STAMP(k)

constexpr int NWG = 256, WAVES = 8;
#define W0V 3
constexpr int W0 = W0V;
constexpr int DM = 5;
constexpr unsigned WS_EPOCH = 0, WS_FLAG = 256, WS_ML = 4096, WS_CNT = 32768, WS_Y = 65536;
constexpr unsigned SENT = 0xffffffffu;
constexpr unsigned SPIN_CAP = 1u << 20;

__device__ __forceinline__ void ld128w(v4u& dst, unsigned voff, u64 sbase) {
  asm volatile("global_load_dwordx4 %0, %1, %2 " WPOL : "=v"(dst) : "v"(voff), "s"(sbase) : "memory");
}
__device__ __forceinline__ void ld16(unsigned& dst, unsigned voff, u64 sbase) {
  asm volatile("global_load_ushort %0, %1, %2" : "=v"(dst) : "v"(voff), "s"(sbase) : "memory");
}

#define ACLOB "a0","a1","a2","a3","a4","a5","a6","a7","a8","a9","a10","a11","a12","a13","a14","a15","a16","a17","a18","a19","a20","a21","a22","a23","a24","a25","a26","a27","a28","a29","a30","a31", \
  "a32","a33","a34","a35","a36","a37","a38","a39","a40","a41","a42","a43","a44","a45","a46","a47","a48","a49","a50","a51","a52","a53","a54","a55","a56","a57","a58","a59","a60","a61","a62","a63", \
  "a64","a65","a66","a67","a68","a69","a70","a71","a72","a73","a74","a75","a76","a77","a78","a79","a80","a81","a82","a83","a84","a85","a86","a87","a88","a89","a90","a91","a92","a93","a94","a95", \
  "a96","a97","a98","a99","a100","a101","a102","a103","a104","a105","a106","a107","a108","a109","a110","a111","a112","a113","a114","a115"
template <int SLOT, int OFF> __device__ __forceinline__ void ald(unsigned voff, u64 sbase) {
  asm volatile("global_load_dwordx4 a[%c2:%c3], %0, %1 offset:%c4 " KVPOL :: "v"(voff), "s"(sbase), "i"(4 * SLOT), "i"(4 * SLOT + 3), "i"(OFF) : "memory", ACLOB);
}
template <int SLOT, int OFF> __device__ __forceinline__ void aldw(unsigned voff, u64 sbase) {
  asm volatile("global_load_dwordx4 a[%c2:%c3], %0, %1 offset:%c4 " WPOL :: "v"(voff), "s"(sbase), "i"(4 * SLOT), "i"(4 * SLOT + 3), "i"(OFF) : "memory", ACLOB);
}
__device__ __forceinline__ void ald_gu(unsigned voff, u64 gbase, u64 ubase, int idx) {
  const u64 mask = 0xffull << (8 * idx);
  asm volatile("s_mov_b64 exec, %3\n\tglobal_load_dwordx4 a[100:103], %0, %1 " WPOL "\n\tglobal_load_dwordx4 a[104:107], %0, %2 " WPOL "\n\ts_mov_b64 exec, -1"
               :: "v"(voff), "s"(gbase), "s"(ubase), "s"(mask) : "memory", ACLOB);
}
template <int REG> __device__ __forceinline__ void ald64sc1(unsigned voff, u64 sbase) {
  asm volatile("global_load_dwordx2 a[%c2:%c3], %0, %1 sc1" :: "v"(voff), "s"(sbase), "i"(REG), "i"(REG + 1) : "memory", ACLOB);
}
__device__ __forceinline__ void aticket(unsigned zero, u64 sbase) {
  asm volatile("s_mov_b64 exec, 1\n\tglobal_load_dword a115, %0, %1\n\ts_mov_b64 exec, -1" :: "v"(zero), "s"(sbase) : "memory", ACLOB);
}
template <int RSLOT, int N> __device__ __forceinline__ v4u aget(void) {
  unsigned a, b, c, d;
  asm volatile("s_waitcnt vmcnt(%c4)\n\tv_accvgpr_read_b32 %0, a%c5\n\tv_accvgpr_read_b32 %1, a%c6\n\tv_accvgpr_read_b32 %2, a%c7\n\tv_accvgpr_read_b32 %3, a%c8"
               : "=v"(a), "=v"(b), "=v"(c), "=v"(d) : "i"(N), "i"(4 * RSLOT), "i"(4 * RSLOT + 1), "i"(4 * RSLOT + 2), "i"(4 * RSLOT + 3) : "memory", ACLOB);
  return v4u{a, b, c, d};
}
template <int RSLOT, int N5, int N4, int N3, int WSLOT, int OFF> __device__ __forceinline__ v4u astep(unsigned voff, u64 sbase, int mode) {
  unsigned a, b, c, d;
  asm volatile("s_waitcnt vmcnt(%c7)\n\t"
               "s_cmp_gt_i32 %6, %c17\n\ts_cbranch_scc1 1f\n\t"
               "s_waitcnt vmcnt(%c8)\n\t"
               "s_cmp_gt_i32 %6, %c18\n\ts_cbranch_scc1 1f\n\t"
               "s_waitcnt vmcnt(%c9)\n"
               "1:\n\tglobal_load_dwordx4 a[%c14:%c15], %4, %5 offset:%c16 " KVPOL "\n\t"
               "v_accvgpr_read_b32 %0, a%c10\n\tv_accvgpr_read_b32 %1, a%c11\n\tv_accvgpr_read_b32 %2, a%c12\n\tv_accvgpr_read_b32 %3, a%c13"
               : "=&v"(a), "=&v"(b), "=&v"(c), "=&v"(d)
               : "v"(voff), "s"(sbase), "s"(mode), "i"(N5), "i"(N4), "i"(N3),
                 "i"(4 * RSLOT), "i"(4 * RSLOT + 1), "i"(4 * RSLOT + 2), "i"(4 * RSLOT + 3), "i"(4 * WSLOT), "i"(4 * WSLOT + 3), "i"(OFF),
                 "i"(FB_T2), "i"(FB_T1)
               : "memory", "scc", ACLOB);
  return v4u{a, b, c, d};
}
template <int N, int WSLOT, int OFF> __device__ __forceinline__ void apre(unsigned voff, u64 sbase) {
  asm volatile("s_waitcnt vmcnt(%c2)\n\tglobal_load_dwordx4 a[%c3:%c4], %0, %1 offset:%c5 " KVPOL
               :: "v"(voff), "s"(sbase), "i"(N), "i"(4 * WSLOT), "i"(4 * WSLOT + 3), "i"(OFF) : "memory", ACLOB);
}
template <int RSLOT> __device__ __forceinline__ v4u aread(void) {
  unsigned a, b, c, d;
  asm volatile("v_accvgpr_read_b32 %0, a%c4\n\tv_accvgpr_read_b32 %1, a%c5\n\tv_accvgpr_read_b32 %2, a%c6\n\tv_accvgpr_read_b32 %3, a%c7\n\ts_nop 1"
               : "=v"(a), "=v"(b), "=v"(c), "=v"(d) : "i"(4 * RSLOT), "i"(4 * RSLOT + 1), "i"(4 * RSLOT + 2), "i"(4 * RSLOT + 3) : "memory", ACLOB);
  return v4u{a, b, c, d};
}
__device__ __forceinline__ unsigned aread_ticket(void) {
  unsigned a;
  asm volatile("v_accvgpr_read_b32 %0, a115\n\ts_nop 1" : "=v"(a) :: "memory", ACLOB);
  return a;
}
__device__ __forceinline__ void st128sc1(unsigned voff, u64 sbase, const v4u& v) {
  asm volatile("global_store_dwordx4 %0, %1, %2 sc1\n\ts_nop 1" :: "v"(voff), "v"(v), "s"(sbase) : "memory");
}
__device__ __forceinline__ void st64sc1(unsigned voff, u64 sbase, unsigned lo, unsigned hi) {
  const v2u v = {lo, hi};
  asm volatile("global_store_dwordx2 %0, %1, %2 sc1" :: "v"(voff), "v"(v), "s"(sbase) : "memory");
}
__device__ __forceinline__ void st16(unsigned voff, u64 sbase, unsigned v) {
  asm volatile("global_store_short %0, %1, %2" :: "v"(voff), "v"(v), "s"(sbase) : "memory");
}
__device__ __forceinline__ v2u ld64sc1_wait(unsigned voff, u64 sbase) {
  v2u v;
  asm volatile("global_load_dwordx2 %0, %1, %2 sc1\n\ts_waitcnt vmcnt(0)" : "=&v"(v) : "v"(voff), "s"(sbase) : "memory");
  return v;
}

__device__ __forceinline__ float dot2(unsigned a, unsigned b, float acc) {
  return __builtin_amdgcn_fdot2_f32_bf16(__builtin_bit_cast(bf16x2, a), __builtin_bit_cast(bf16x2, b), acc, false);
}
__device__ __forceinline__ float lo16(unsigned v) { return __builtin_bit_cast(float, v << 16); }
__device__ __forceinline__ float hi16(unsigned v) { return __builtin_bit_cast(float, v & 0xffff0000u); }
template <int CTRL> __device__ __forceinline__ float dpp(float x) {
  return __builtin_bit_cast(float, __builtin_amdgcn_mov_dpp(__builtin_bit_cast(int, x), CTRL, 0xf, 0xf, true));
}
template <int CTRL> __device__ __forceinline__ unsigned dppu(unsigned x) { return (unsigned)__builtin_amdgcn_mov_dpp((int)x, CTRL, 0xf, 0xf, true); }
constexpr int XOR1 = 0xB1, XOR2 = 0x4E, XOR7 = 0x141, XOR8 = 0x128, ROR4 = 0x124;
__device__ __forceinline__ unsigned cvtpk(float lo, float hi) {
  const bf16x2 v = {(__bf16)lo, (__bf16)hi};
  return __builtin_bit_cast(unsigned, v);
}
__device__ __forceinline__ float rbf(float x) { return lo16(cvtpk(x, x)); }
__device__ __forceinline__ float pair32(float a, float b) {
  auto s = __builtin_amdgcn_permlane32_swap(__float_as_uint(a), __float_as_uint(b), false, false);
  return __uint_as_float(s[0]) + __uint_as_float(s[1]);
}
__device__ __forceinline__ float pair16(float a, float b) {
  auto s = __builtin_amdgcn_permlane16_swap(__float_as_uint(a), __float_as_uint(b), false, false);
  return __uint_as_float(s[0]) + __uint_as_float(s[1]);
}
__device__ __forceinline__ float wave_sum(float x) {
  x += dpp<XOR1>(x); x += dpp<XOR2>(x); x += dpp<XOR7>(x); x += dpp<XOR8>(x);
  x = pair16(x, x); x = pair32(x, x); return x;
}
__device__ __forceinline__ float wave_max(float x) {
  x = fmaxf(x, dpp<XOR1>(x)); x = fmaxf(x, dpp<XOR2>(x)); x = fmaxf(x, dpp<XOR7>(x)); x = fmaxf(x, dpp<XOR8>(x));
  auto s = __builtin_amdgcn_permlane16_swap(__float_as_uint(x), __float_as_uint(x), false, false);
  x = fmaxf(__uint_as_float(s[0]), __uint_as_float(s[1]));
  auto t = __builtin_amdgcn_permlane32_swap(__float_as_uint(x), __float_as_uint(x), false, false);
  return fmaxf(__uint_as_float(t[0]), __uint_as_float(t[1]));
}
__device__ __forceinline__ float treduce16(float (&x)[16], int c) {
#define TR_STEP(HALF, CTRL, BIT) _Pragma("unroll") for (int h = 0; h < (HALF); ++h) { \
    const float lo_ = x[h], hi_ = x[h + (HALF)]; const float t1_ = lo_ + dpp<CTRL>(lo_), t2_ = hi_ + dpp<CTRL>(hi_); x[h] = (c & (BIT)) ? t2_ : t1_; }
  TR_STEP(8, XOR8, 8) TR_STEP(4, XOR7, 4) TR_STEP(2, XOR2, 2) TR_STEP(1, XOR1, 1)
#undef TR_STEP
  return x[0];
}
template <int T> __device__ __forceinline__ float bcast(float p) { return dpp<0x150 + T>(p); }
__device__ __forceinline__ void fma8(float (&acc)[8], float p, const v4u& v) {
#pragma unroll
  for (int k = 0; k < 4; ++k) { acc[2 * k] = fmaf(p, lo16(v[k]), acc[2 * k]); acc[2 * k + 1] = fmaf(p, hi16(v[k]), acc[2 * k + 1]); }
}
__device__ __forceinline__ unsigned plo(unsigned hi_row, unsigned lo_row) { return __builtin_amdgcn_perm(hi_row, lo_row, 0x05040100u); }
__device__ __forceinline__ unsigned phi(unsigned hi_row, unsigned lo_row) { return __builtin_amdgcn_perm(hi_row, lo_row, 0x07060302u); }

constexpr int younger(int U, int X, int NA, int NB) { return (U + DM - 1 - X) + ((U >= 1 && X <= DM) ? NA : 0) + ((U >= 9 && X <= 8 + DM) ? NB : 0); }
struct Stream {
  u64 K, V; unsigned voff; int rot;
  __device__ __forceinline__ u64 base(int round) const { return ((round < 32) ? K : V) + (u64)(unsigned)(((round + rot) & 31) << 15); }
  template <int J, int SLOT> __device__ __forceinline__ void issue(int round0) const { ald<SLOT, (J & 3) * 1024>(voff, base(round0 + (J >> 2))); }
  template <int U, int NA, int NB> __device__ __forceinline__ v4u step(int round0, int mode) const {
    return astep<(U & 7), younger(U, U, NA, NB), younger(U, U + 1, NA, NB), younger(U, U + 2, NA, NB), ((U + DM) & 7), ((U + DM) & 3) * 1024>(
        voff, base(round0 + ((U + DM) >> 2)), mode);
  }
};
__device__ __forceinline__ float qk(const v4u& k, const v4u& q) { return dot2(k[3], q[3], dot2(k[2], q[2], dot2(k[1], q[1], dot2(k[0], q[0], 0.f)))); }
#define REP16(M) M(0) M(1) M(2) M(3) M(4) M(5) M(6) M(7) M(8) M(9) M(10) M(11) M(12) M(13) M(14) M(15)
#define REP15(M) M(1) M(2) M(3) M(4) M(5) M(6) M(7) M(8) M(9) M(10) M(11) M(12) M(13) M(14) M(15)
#define REP8(M) M(0) M(1) M(2) M(3) M(4) M(5) M(6) M(7)
#define BARRIER() asm volatile("s_waitcnt lgkmcnt(0)\n\ts_barrier" ::: "memory")

constexpr int WQ0 = 4, QPOST = W0 + 8 - WQ0, QGET = 10, XSTEP = 8;
static_assert(QGET > QPOST && QGET < 16 - W0 && XSTEP + WQ0 >= 8 && XSTEP + W0 < 16, "tile-0 schedule");
constexpr int t0_ext(int m) { return m < 0 ? 2 * WQ0 : (m + WQ0 < 8 ? 2 : (m == XSTEP ? 4 : 0)); }
constexpr int t0_wait(int n) { int c = W0 - 1; for (int m = (n - W0 < -1 ? -1 : n - W0); m <= n - 1; ++m) c += t0_ext(m); return c; }

__device__ __forceinline__ float fin_score(float d) { return rbf(rbf(d) * 0.08838834764831845f); }
constexpr float LOG2E = 1.4426950408889634f;

__global__ void __launch_bounds__(WAVES * 64, 2) fused_decode(const void* xp, const void* Kp, const void* Vp, const void* wqp, const void* wop,
                                                             const void* wgp, const void* wup, const void* wdp, void* wsp, void* outp) {
  __shared__ __attribute__((aligned(16))) float lds[8 * 128 + 8 * 16 + 8 * 68 + 8 * 16 + 4];
  float* Lq = lds;
  float* Lg = lds + 1024;
  unsigned* Lo = (unsigned*)(lds + 1024 + 128);
  float* Lr = lds + 1024 + 128 + 8 * 68;
  volatile LAS unsigned* Lcnt = (volatile LAS unsigned*)(Lr + 128);
  u64 tpad; asm volatile("s_memrealtime %0\n\ts_waitcnt lgkmcnt(0)" : "=s"(tpad) :: "memory");
  __builtin_amdgcn_sched_barrier(0);
  STAMP(0)
  const u64 tstart = __builtin_amdgcn_s_memrealtime();
  const int tid = threadIdx.x, lane = tid & 63, w = __builtin_amdgcn_readfirstlane(tid >> 6), r = lane >> 4, c = lane & 15;
  const int b = blockIdx.x, xcd = b & 7, i8 = b >> 3;
  const int head = xcd * 4 + (i8 & 3), split = i8 >> 2, chunk = head * 8 + split;
  const bool has_mlp = i8 < 28;
  const int j = xcd * 28 + (has_mlp ? i8 : 27);
  const u64 ws = (u64)wsp;
  const u64 mlb = ws + WS_ML + head * 512;

  Stream s;
  { const u64 co = ((u64)chunk << 20) + (u64)(w << 12); s.K = (u64)Kp + co; s.V = (u64)Vp + co; s.voff = lane * 16; s.rot = b & 31; }
  v4u x8; unsigned xs; v4u wq[16];
  const unsigned wq_vo = r * 8192 + c * 16; const u64 wq_sb = (u64)wqp + (u64)w * (64 * 8192) + head * 256;
#define WQ_PAIR(P) { ld128w(wq[2 * (P)], wq_vo, wq_sb + (u64)(2 * (P)) * (4 * 8192)); ld128w(wq[2 * (P) + 1], wq_vo, wq_sb + (u64)(2 * (P) + 1) * (4 * 8192)); }
  ld128w(x8, lane * 16, (u64)xp);
  ld16(xs, (4 * c + r) * 2, (u64)xp + w * 128);
  s.issue<0, 0>(0); s.issue<1, 1>(0); s.issue<2, 2>(0); if (W0 > 3) s.issue<3, 3>(0); if (W0 > 4) s.issue<4, 4>(0);
  WQ_PAIR(0) WQ_PAIR(1) WQ_PAIR(2) WQ_PAIR(3)
  if (tid == 0) { Lcnt[0] = 0u; Lcnt[1] = 0u; }
  BARRIER();
  const int mg = lane >> 4, mn = lane & 15, cg = lane & 7, rowsel = (lane >> 3) & 1;
  const unsigned gu_vo = (4 * c + r) * 3584;
  const u64 g_sb = (u64)wgp + (u64)w * (64 * 3584) + j * 16, u_sb = (u64)wup + (u64)w * (64 * 3584) + j * 16;
  const u64 wo_sb = (u64)wop + (u64)head * (128 * 1024) + w * 128;
  float xn; unsigned xpair;
  float qa[8] = {0.f, 0.f, 0.f, 0.f, 0.f, 0.f, 0.f, 0.f};
  v4u q;
  float sc[8];
  float d[16];
#define PRE(N) apre<t0_wait(N), ((N) + W0 < 16 ? (N) + W0 : ((N) + W0) & 7), (((N) + W0) & 3) * 1024>(s.voff, s.base(((N) + W0) >> 2));
#define QP(P) { asm volatile("" : "+v"(wq[2 * (P)]), "+v"(wq[2 * (P) + 1])); const unsigned xx = dppu<0x150 + 2 * (P)>(xpair); \
      _Pragma("unroll") for (int k = 0; k < 4; ++k) { qa[2 * k] = dot2(plo(wq[2 * (P) + 1][k], wq[2 * (P)][k]), xx, qa[2 * k]); \
        qa[2 * k + 1] = dot2(phi(wq[2 * (P) + 1][k], wq[2 * (P)][k]), xx, qa[2 * k + 1]); } }
#define KQ(U) d[U] = qk(aread<U>(), q);
#define M(N) PRE(N) \
    if ((N) + WQ0 < 8) WQ_PAIR(((N) + WQ0) & 7) \
    if ((N) == XSTEP) { ald_gu(gu_vo, g_sb, u_sb, 0); ald64sc1<108>(0u, ws + WS_EPOCH); \
                        aldw<24, 0>((lane >> 3) * 1024 + cg * 16, (u64)wdp + (u64)j * 8192 + w * 128); } \
    if ((N) == 0) { \
        \
      asm volatile("" : "+v"(x8), "+v"(xs)); \
      float xv[8]; \
      _Pragma("unroll") for (int k = 0; k < 4; ++k) { xv[2 * k] = lo16(x8[k]); xv[2 * k + 1] = hi16(x8[k]); } \
      float sum = 0.f; \
      _Pragma("unroll") for (int k = 0; k < 8; ++k) sum += xv[k]; \
      const float mean = wave_sum(sum) * (1.f / 512.f); \
      float sq = 0.f; \
      _Pragma("unroll") for (int k = 0; k < 8; ++k) { const float dlt = xv[k] - mean; sq = fmaf(dlt, dlt, sq); } \
      const float var = wave_sum(sq) * (1.f / 512.f); \
      xn = rbf(rbf(lo16(xs) - rbf(mean)) / rbf(sqrtf(rbf(rbf(var) + 1e-5f)))); \
      const unsigned xb = __float_as_uint(xn); \
      xpair = phi(dppu<XOR1>(xb), xb);     \
      STAMP(1) \
    } \
    if ((N) == W0) { QP(0) QP(1) QP(2) QP(3) } \
    if ((N) > W0 && (N) - W0 - 1 + WQ0 < 8) QP(((N) - W0 - 1 + WQ0) & 7) \
    if ((N) == QPOST) { \
      const float b0 = pair32(qa[0], qa[4]), b1 = pair32(qa[1], qa[5]), b2 = pair32(qa[2], qa[6]), b3 = pair32(qa[3], qa[7]); \
      const float c0 = pair16(b0, b2), c1 = pair16(b1, b3); \
      *(f32x2*)(Lq + w * 128 + 8 * c + 2 * r) = f32x2{c0, c1};     \
      asm volatile("s_waitcnt lgkmcnt(0)" ::: "memory"); \
      if (lane == 0) __hip_atomic_fetch_add((LAS unsigned*)(Lcnt + 1), 1u, __ATOMIC_RELAXED, __HIP_MEMORY_SCOPE_WORKGROUP); \
    } \
    if ((N) == QGET) { \
      for (unsigned spins = 0; Lcnt[1] != 8u && spins < SPIN_CAP; ++spins) __builtin_amdgcn_s_sleep(1); \
      asm volatile("" ::: "memory"); \
      f32x4 a0 = *(const f32x4*)(Lq + (2 * r) * 128 + 8 * c), a1 = *(const f32x4*)(Lq + (2 * r) * 128 + 8 * c + 4); \
      f32x4 b0 = *(const f32x4*)(Lq + (2 * r + 1) * 128 + 8 * c), b1 = *(const f32x4*)(Lq + (2 * r + 1) * 128 + 8 * c + 4); \
      a0 += b0; a1 += b1; \
      float v[8] = {a0[0], a0[1], a0[2], a0[3], a1[0], a1[1], a1[2], a1[3]}; \
      _Pragma("unroll") for (int k = 0; k < 8; ++k) { v[k] = pair16(v[k], v[k]); v[k] = pair32(v[k], v[k]); } \
      _Pragma("unroll") for (int k = 0; k < 4; ++k) q[k] = cvtpk(v[2 * k], v[2 * k + 1]); \
      STAMP(2) \
      KQ(0) KQ(1) KQ(2) KQ(3) \
    } \
    if ((N) == QGET + 1) { KQ(4) KQ(5) KQ(6) KQ(7) KQ(8) KQ(9) KQ(10) } \
    if ((N) > QGET) KQ(N) \
    __builtin_amdgcn_sched_barrier(0);
  REP16(M)
#undef M
  sc[7] = fin_score(treduce16(d, c));
  if (W0 < 5) { PRE(16) } if (W0 < 4) { PRE(17) }
  STAMP(5)
  unsigned tag_lo, tag_hi;
  {
    const v4u e = aread<27>();
    const u64 tag = (((u64)(unsigned)__builtin_amdgcn_readfirstlane(e[1]) << 32) | (u64)(unsigned)__builtin_amdgcn_readfirstlane(e[0])) + 1ull;
    tag_lo = (unsigned)tag; tag_hi = (unsigned)(tag >> 32);
    asm volatile("v_accvgpr_write_b32 a114, 1" ::: ACLOB);
  }
  const bool clearer = !has_mlp && w == 0;
  if (clearer && !(ABL & 4)) {
    const v4u sv = {SENT, SENT, SENT, SENT};
    for (int i = 0; i < 16; ++i) st128sc1(lane * 16 + i * 1024, ws + WS_Y + head * 16384, sv);
    if (lane < 32) st128sc1(lane * 16, mlb, sv);
    if (lane == 0) st64sc1(0u, ws + WS_CNT + head * 128, 0u, 0u);
  }
  const unsigned wo_vo = (64 * rowsel + 8 * mg) * 1024 + cg * 16;
  const u64 cnt_sb = ws + WS_CNT + ((i8 + 4 * w) & 31) * 128;
  constexpr int MODE0 = BASEMODE == 0 ? -1000 : (BASEMODE == 1 ? FB_T1 + 1 : FB_T2 + 1);
  int mode = MODE0;
#define SCH_T0 600
#define SCH_K 472
#define SCH_V 440
#define FEEDBACK(TILE) if (FB) { const int now_ = (int)(unsigned)(__builtin_amdgcn_s_memrealtime() - tstart); \
    mode = now_ - ((TILE) <= 8 ? SCH_T0 + (TILE) * SCH_K : SCH_T0 + 8 * SCH_K + ((TILE) - 8) * SCH_V); }
#define SETPRIO(TILE) if (PRIO == 2) { if (((w >> 2) ^ (TILE)) & 1) __builtin_amdgcn_s_setprio(1); else __builtin_amdgcn_s_setprio(0); }
  for (int tile = 1; tile < 8; ++tile) {
    SETPRIO(tile)
    if (FB_HALF) mode = MODE0;
    d[0] = qk(s.step<0, 3, 2>(tile * 4, mode), q);
    switch (tile) {
      case 1: aldw<8, 0>(wo_vo, wo_sb); aldw<9, 1024>(wo_vo, wo_sb); aldw<10, 2048>(wo_vo, wo_sb); break;
      case 2: aldw<11, 3072>(wo_vo, wo_sb); aldw<12, 0>(wo_vo, wo_sb + 4096); aldw<13, 1024>(wo_vo, wo_sb + 4096); break;
      case 3: aldw<14, 2048>(wo_vo, wo_sb + 4096); aldw<15, 3072>(wo_vo, wo_sb + 4096); ald64sc1<110>((lane & 31) * 8, ws + WS_FLAG); break;
      case 4: aldw<16, 0>(wo_vo, wo_sb + 32768); aldw<17, 1024>(wo_vo, wo_sb + 32768); aticket(0u, cnt_sb); break;
      case 5: aldw<18, 2048>(wo_vo, wo_sb + 32768); aldw<19, 3072>(wo_vo, wo_sb + 32768); aticket(0u, cnt_sb); break;
      case 6: aldw<20, 0>(wo_vo, wo_sb + 36864); aldw<21, 1024>(wo_vo, wo_sb + 36864); aticket(0u, cnt_sb); break;
      default: aldw<22, 2048>(wo_vo, wo_sb + 36864); aldw<23, 3072>(wo_vo, wo_sb + 36864); aticket(0u, cnt_sb); break;
    }
#define M(U) d[U] = qk(s.step<U, 3, 2>(tile * 4, mode), q); \
    if (U == 7) { \
      { FEEDBACK(tile) } \
        \
      if (tile == 1 && clearer && lane == 0) st64sc1(0u, ws + WS_FLAG + head * 8, tag_lo, tag_hi); \
    } \
    if (U == 8) ald_gu(gu_vo, g_sb, u_sb, tile);
    REP15(M)
#undef M
#pragma unroll
    for (int k = 0; k < 7; ++k) sc[k] = sc[k + 1];
    sc[7] = fin_score(treduce16(d, c));
    if (tile == 1) { STAMP(6) }
    if (tile == 3) {
      const v4u e = aread<27>();
      if (!__all(e[2] == tag_lo && e[3] == tag_hi)) {
        for (unsigned spins = 0; spins < SPIN_CAP; ++spins) {
          __builtin_amdgcn_s_sleep(2);
          const v2u f = ld64sc1_wait((lane & 31) * 8, ws + WS_FLAG);
          if (__all(f[0] == tag_lo && f[1] == tag_hi)) break;
        }
      }
    }
  }
  STAMP(3)
  float mw;
  {
    float m = sc[0];
#pragma unroll
    for (int k = 1; k < 8; ++k) m = fmaxf(m, sc[k]);
    mw = wave_max(m);
    float l = 0.f;
#pragma unroll
    for (int k = 0; k < 8; ++k) { sc[k] = __builtin_amdgcn_exp2f((sc[k] - mw) * LOG2E); l += sc[k]; }
    const float lw = wave_sum(l);
    if (!(ABL & 1) && lane == 0) st64sc1(0u, mlb + (split * 8 + w) * 8, __float_as_uint(mw) & 0xfffffffeu, __float_as_uint(lw) & 0xfffffffeu);
  }
  float acc[8] = {0.f, 0.f, 0.f, 0.f, 0.f, 0.f, 0.f, 0.f};
  for (int tile = 8; tile < 15; ++tile) {
    SETPRIO(tile)
    if (FB_HALF) mode = MODE0;
    const float p = sc[0];
#define M(U) fma8(acc, bcast<U>(p), s.step<U, 1, 0>(tile * 4, mode)); \
    if (U == 0) { aticket(0u, cnt_sb); if (!(ABL & 1) && tile == 14) ald64sc1<112>(lane * 8, mlb); } \
    if (U == 2 && tile == 8) { \
      const v4u wg = aread<25>(), wu = aread<26>(); \
      float g[16]; \
      _Pragma("unroll") for (int k = 0; k < 4; ++k) { g[2 * k] = xn * lo16(wg[k]); g[2 * k + 1] = xn * hi16(wg[k]); g[8 + 2 * k] = xn * lo16(wu[k]); g[8 + 2 * k + 1] = xn * hi16(wu[k]); } \
      float t = treduce16(g, c); \
      t = pair16(t, t); t = pair32(t, t); \
      if (r == 0) Lg[w * 16 + c] = t; \
      asm volatile("s_waitcnt lgkmcnt(0)" ::: "memory"); \
      if (lane == 0) __hip_atomic_fetch_add((LAS unsigned*)Lcnt, 1u, __ATOMIC_RELAXED, __HIP_MEMORY_SCOPE_WORKGROUP); \
    } \
    if (U == 7) { FEEDBACK(tile) }
    REP16(M)
#undef M
#pragma unroll
    for (int k = 0; k < 7; ++k) sc[k] = sc[k + 1];
  }
  v4u bf[16];
  float scale, ymlp[4];
  {
    const float p = sc[0];
#define M(U) if (U >= DM) s.issue<U, U>(60);
    REP8(M)
#undef M
    {
      const v4u e = aread<28>();
      unsigned mb = e[0], lb = e[1];
      if (!(ABL & 1) && !__all(mb != SENT && lb != SENT)) {
        for (unsigned spins = 0; spins < SPIN_CAP; ++spins) {
          __builtin_amdgcn_s_sleep(2);
          const v2u t = ld64sc1_wait(lane * 8, mlb);
          mb = t[0]; lb = t[1];
          if (__all(mb != SENT && lb != SENT)) break;
        }
      }
      const float mi = __uint_as_float(mb), li = __uint_as_float(lb);
      const float M = wave_max(mi);
      const float L = wave_sum(li * __builtin_amdgcn_exp2f((mi - M) * LOG2E));
      scale = __builtin_amdgcn_exp2f((mw - M) * LOG2E) / L;
    }
#define M(T) { const v4u A = aread<8 + 2 * T>(), B = aread<9 + 2 * T>(); _Pragma("unroll") for (int k = 0; k < 4; ++k) { \
      bf[(T >> 2) * 8 + 2 * k][T & 3] = plo(B[k], A[k]); bf[(T >> 2) * 8 + 2 * k + 1][T & 3] = phi(B[k], A[k]); } }
    REP8(M)
#undef M
    {
      for (unsigned spins = 0; Lcnt[0] != 8u && spins < SPIN_CAP; ++spins) __builtin_amdgcn_s_sleep(1);
      asm volatile("" ::: "memory");
      const int f = lane >> 3;
      float g = 0.f, u = 0.f;
#pragma unroll
      for (int ww = 0; ww < 8; ++ww) { g += Lg[ww * 16 + f]; u += Lg[ww * 16 + 8 + f]; }
      g = rbf(g); u = rbf(u);
      float hval = rbf(rbf(1.f / (1.f + __expf(-g))) * u);
      if (!has_mlp) hval = 0.f;
      const v4u wd = aread<24>();
      float ym[8];
#pragma unroll
      for (int k = 0; k < 4; ++k) { ym[2 * k] = hval * lo16(wd[k]); ym[2 * k + 1] = hval * hi16(wd[k]); }
#pragma unroll
      for (int k = 0; k < 4; ++k) { float t = pair32(ym[k], ym[k + 4]); t = pair16(t, t); ymlp[k] = t + dpp<XOR8>(t); asm volatile("" : "+v"(ymlp[k])); }
    }
#define M(U) fma8(acc, bcast<U>(p), aget<U, 7>()); s.issue<8 + U, U>(60);
    REP8(M)
#undef M
#define M(U) fma8(acc, bcast<8 + U>(p), aget<U, 7 - U>());
    REP8(M)
#undef M
  }
  STAMP(4)
  {
    const float b0 = pair32(acc[0], acc[4]), b1 = pair32(acc[1], acc[5]), b2 = pair32(acc[2], acc[6]), b3 = pair32(acc[3], acc[7]);
    const float c0 = pair16(b0, b2), c1 = pair16(b1, b3);
    Lo[w * 68 + 4 * c + r] = cvtpk(c0 * scale, c1 * scale);
  }
  BARRIER();
  {
    const unsigned* ap = Lo + (mn & 7) * 68 + 32 * (mn >> 3) + 4 * mg;
    const v4u a0 = *(const v4u*)ap, a1 = *(const v4u*)(ap + 16);
    float y[8];
#pragma unroll
    for (int cc = 0; cc < 8; ++cc) {
      f32x4 dd = {0.f, 0.f, 0.f, 0.f};
      dd = __builtin_amdgcn_mfma_f32_16x16x32_bf16(__builtin_bit_cast(bf16x8, a0), __builtin_bit_cast(bf16x8, bf[cc]), dd, 0, 0, 0);
      dd = __builtin_amdgcn_mfma_f32_16x16x32_bf16(__builtin_bit_cast(bf16x8, a1), __builtin_bit_cast(bf16x8, bf[8 + cc]), dd, 0, 0, 0);
      const float t = (dd[0] + dd[1]) + (dd[2] + dd[3]);
      y[cc] = __builtin_bit_cast(float, __builtin_amdgcn_update_dpp(__builtin_bit_cast(int, t), __builtin_bit_cast(int, t), XOR8, 0xc, 0xf, false));
    }
    v4u o;
#pragma unroll
    for (int k = 0; k < 4; ++k) { float t = pair32(y[k], y[k + 4]); t = pair16(t, t); o[k] = __float_as_uint(t + ymlp[k]) & 0xfffffffeu; }
    if (!(ABL & 8) && (lane & 24) == 0) st128sc1((4 * w + (cg >> 1)) * 16384 + chunk * 64 + (cg & 1) * 32 + (lane >> 5) * 16, ws + WS_Y, o);
  }
  if (!has_mlp) {
    unsigned xv;
    ld16(xv, (lane & 15) * 2, (u64)xp + head * 32);
    const u64 yb = ws + WS_Y + head * 16384 + w * 2048;
    v4u y0, y1;
    for (unsigned spins = 0; spins < SPIN_CAP; ++spins) {
      asm volatile("global_load_dwordx4 %0, %3, %4 sc1\n\tglobal_load_dwordx4 %1, %3, %4 offset:1024 sc1\n\ts_waitcnt vmcnt(0)"
                   : "=&v"(y0), "=&v"(y1), "+v"(xv) : "v"(lane * 16), "s"(yb) : "memory");
      bool ok = true;
#pragma unroll
      for (int k = 0; k < 4; ++k) ok = ok && (y0[k] != SENT) && (y1[k] != SENT);
      if (__all(ok)) break;
      if (POLL_SLEEP) __builtin_amdgcn_s_sleep(POLL_SLEEP);
    }
    float v[4];
#pragma unroll
    for (int k = 0; k < 4; ++k) v[k] = __uint_as_float(y0[k]) + __uint_as_float(y1[k]);
    float t = pair16(pair32(v[0], v[2]), pair32(v[1], v[3]));
    t += dpp<XOR8>(t); t += dpp<ROR4>(t);
    if ((lane & 12) == 0) Lr[w * 16 + 4 * (lane & 3) + (lane >> 4)] = t;
    BARRIER();
    while ((long long)(__builtin_amdgcn_s_memrealtime() - tpad) < 9000) __builtin_amdgcn_s_sleep(1);
    if (w == 0 && lane < 16) {
      float sum = 0.f;
#pragma unroll
      for (int ww = 0; ww < 8; ++ww) sum += Lr[ww * 16 + lane];
      const float o = lo16(xv) + sum;
      st16(lane * 2, (u64)outp + head * 32, cvtpk(o, o));
      if (head == 0 && lane == 0) st64sc1(0u, ws + WS_EPOCH, tag_lo, tag_hi);
    }
  }
}

extern "C" void kernel_launch(void* const* d_in, const int* in_sizes, int n_in, void* d_out, int out_size, void* d_ws, size_t ws_size,
                              hipStream_t stream) {
  fused_decode<<<NWG, WAVES * 64, 0, stream>>>(d_in[0], d_in[1], d_in[2], d_in[3], d_in[4], d_in[6], d_in[7], d_in[8], d_ws, d_out);
}
```

```cpp
#include <hip/hip_runtime.h>
#include <hip/hip_bf16.h>

#define WPOL ""
#define KVPOL "nt"
#define PRIO 2
#define FB 1
#define FB_T0 -100
#define FB_T1 0
#define FB_T2 80
#define FB_HALF 1
#define TICKET 3
#define ABL 0
#define POLL_SLEEP 0
#define SCH_T0 600
#define SCH_K 458
#define SCH_V 432
#define ANCH 1
#define ANCH_EVERY 4
#define ANCH_OFF 50
#define LT_A 0
#define LT_B -50
#define LT_WA 4
#define LT_WB 3
#define LT_END 12
#define TOUCH 3
#define LASTL 0
#define BASEMODE 0

typedef unsigned v4u __attribute__((ext_vector_type(4)));
typedef unsigned v2u __attribute__((ext_vector_type(2)));
typedef float f32x4 __attribute__((ext_vector_type(4)));
typedef float f32x2 __attribute__((ext_vector_type(2)));
typedef __bf16 bf16x2 __attribute__((ext_vector_type(2)));
typedef __bf16 bf16x8 __attribute__((ext_vector_type(8)));
typedef unsigned long long u64;
#define LAS __attribute__((address_space(3)))
#define STAMP(k)

constexpr int NWG = 256, WAVES = 8;
#define W0V 3
constexpr int W0 = W0V;
constexpr int DM = 5;
constexpr unsigned WS_EPOCH = 0, WS_FLAG = 256, WS_ML = 4096, WS_CNT = 32768, WS_Y = 65536;
constexpr unsigned SENT = 0xffffffffu;
constexpr unsigned SPIN_CAP = 1u << 20;

__device__ __forceinline__ void ld128w(v4u& dst, unsigned voff, u64 sbase) {
  asm volatile("global_load_dwordx4 %0, %1, %2 " WPOL : "=v"(dst) : "v"(voff), "s"(sbase) : "memory");
}
__device__ __forceinline__ void ld16(unsigned& dst, unsigned voff, u64 sbase) {
  asm volatile("global_load_ushort %0, %1, %2" : "=v"(dst) : "v"(voff), "s"(sbase) : "memory");
}

#define ACLOB "a0","a1","a2","a3","a4","a5","a6","a7","a8","a9","a10","a11","a12","a13","a14","a15","a16","a17","a18","a19","a20","a21","a22","a23","a24","a25","a26","a27","a28","a29","a30","a31", \
  "a32","a33","a34","a35","a36","a37","a38","a39","a40","a41","a42","a43","a44","a45","a46","a47","a48","a49","a50","a51","a52","a53","a54","a55","a56","a57","a58","a59","a60","a61","a62","a63", \
  "a64","a65","a66","a67","a68","a69","a70","a71","a72","a73","a74","a75","a76","a77","a78","a79","a80","a81","a82","a83","a84","a85","a86","a87","a88","a89","a90","a91","a92","a93","a94","a95", \
  "a96","a97","a98","a99","a100","a101","a102","a103","a104","a105","a106","a107","a108","a109","a110","a111","a112","a113","a114","a115"
template <int SLOT, int OFF> __device__ __forceinline__ void ald(unsigned voff, u64 sbase) {
  asm volatile("global_load_dwordx4 a[%c2:%c3], %0, %1 offset:%c4 " KVPOL :: "v"(voff), "s"(sbase), "i"(4 * SLOT), "i"(4 * SLOT + 3), "i"(OFF) : "memory", ACLOB);
}
template <int SLOT, int OFF> __device__ __forceinline__ void aldw(unsigned voff, u64 sbase) {
  asm volatile("global_load_dwordx4 a[%c2:%c3], %0, %1 offset:%c4 " WPOL :: "v"(voff), "s"(sbase), "i"(4 * SLOT), "i"(4 * SLOT + 3), "i"(OFF) : "memory", ACLOB);
}
__device__ __forceinline__ void ald_gu(unsigned voff, u64 gbase, u64 ubase, int idx) {
  const u64 mask = 0xffull << (8 * idx);
  asm volatile("s_mov_b64 exec, %3\n\tglobal_load_dwordx4 a[100:103], %0, %1 " WPOL "\n\tglobal_load_dwordx4 a[104:107], %0, %2 " WPOL "\n\ts_mov_b64 exec, -1"
               :: "v"(voff), "s"(gbase), "s"(ubase), "s"(mask) : "memory", ACLOB);
}
template <int REG> __device__ __forceinline__ void ald64sc1(unsigned voff, u64 sbase) {
  asm volatile("global_load_dwordx2 a[%c2:%c3], %0, %1 sc1" :: "v"(voff), "s"(sbase), "i"(REG), "i"(REG + 1) : "memory", ACLOB);
}
__device__ __forceinline__ void aanchor(unsigned voff, u64 sbase) {
  asm volatile("global_load_dword a115, %0, %1 sc1" :: "v"(voff), "s"(sbase) : "memory", ACLOB);
}
__device__ __forceinline__ void adummy(unsigned zero, u64 sbase) {
  asm volatile("s_mov_b64 exec, 1\n\tglobal_load_dword a114, %0, %1\n\ts_mov_b64 exec, -1" :: "v"(zero), "s"(sbase) : "memory", ACLOB);
}
__device__ __forceinline__ void st32sc1(unsigned voff, u64 sbase, unsigned v) {
  asm volatile("global_store_dword %0, %1, %2 sc1" :: "v"(voff), "v"(v), "s"(sbase) : "memory");
}
template <int RSLOT, int N> __device__ __forceinline__ v4u aget(void) {
  unsigned a, b, c, d;
  asm volatile("s_waitcnt vmcnt(%c4)\n\tv_accvgpr_read_b32 %0, a%c5\n\tv_accvgpr_read_b32 %1, a%c6\n\tv_accvgpr_read_b32 %2, a%c7\n\tv_accvgpr_read_b32 %3, a%c8"
               : "=v"(a), "=v"(b), "=v"(c), "=v"(d) : "i"(N), "i"(4 * RSLOT), "i"(4 * RSLOT + 1), "i"(4 * RSLOT + 2), "i"(4 * RSLOT + 3) : "memory", ACLOB);
  return v4u{a, b, c, d};
}
template <int RSLOT, int N5, int N4, int N3, int N2, int WSLOT, int OFF> __device__ __forceinline__ v4u astep(unsigned voff, u64 sbase, int mode) {
  unsigned a, b, c, d;
  asm volatile("s_waitcnt vmcnt(%c7)\n\t"
               "s_cmp_gt_i32 %6, %c17\n\ts_cbranch_scc1 1f\n\t"
               "s_waitcnt vmcnt(%c8)\n\t"
               "s_cmp_gt_i32 %6, %c18\n\ts_cbranch_scc1 1f\n\t"
               "s_waitcnt vmcnt(%c9)\n\t"
               "s_cmp_gt_i32 %6, %c19\n\ts_cbranch_scc1 1f\n\t"
               "s_waitcnt vmcnt(%c20)\n"
               "1:\n\tglobal_load_dwordx4 a[%c14:%c15], %4, %5 offset:%c16 " KVPOL "\n\t"
               "v_accvgpr_read_b32 %0, a%c10\n\tv_accvgpr_read_b32 %1, a%c11\n\tv_accvgpr_read_b32 %2, a%c12\n\tv_accvgpr_read_b32 %3, a%c13"
               : "=&v"(a), "=&v"(b), "=&v"(c), "=&v"(d)
               : "v"(voff), "s"(sbase), "s"(mode), "i"(N5), "i"(N4), "i"(N3),
                 "i"(4 * RSLOT), "i"(4 * RSLOT + 1), "i"(4 * RSLOT + 2), "i"(4 * RSLOT + 3), "i"(4 * WSLOT), "i"(4 * WSLOT + 3), "i"(OFF),
                 "i"(FB_T2), "i"(FB_T1), "i"(FB_T0), "i"(N2)
               : "memory", "scc", ACLOB);
  return v4u{a, b, c, d};
}
template <int WSLOT, int OFF> __device__ __forceinline__ void awin(unsigned voff, u64 sbase, int mode) {
  asm volatile("s_cmp_gt_i32 %2, %c6\n\ts_cbranch_scc1 1f\n\t"
               "s_waitcnt vmcnt(%c8)\n\t"
               "s_cmp_gt_i32 %2, %c7\n\ts_cbranch_scc1 1f\n\t"
               "s_waitcnt vmcnt(%c9)\n"
               "1:\n\tglobal_load_dwordx4 a[%c3:%c4], %0, %1 offset:%c5 " KVPOL
               :: "v"(voff), "s"(sbase), "s"(mode), "i"(4 * WSLOT), "i"(4 * WSLOT + 3), "i"(OFF), "i"(LT_A), "i"(LT_B), "i"(LT_WA - 1), "i"(LT_WB - 1)
               : "memory", "scc", ACLOB);
}
template <int N, int WSLOT, int OFF> __device__ __forceinline__ void apre(unsigned voff, u64 sbase) {
  asm volatile("s_waitcnt vmcnt(%c2)\n\tglobal_load_dwordx4 a[%c3:%c4], %0, %1 offset:%c5 " KVPOL
               :: "v"(voff), "s"(sbase), "i"(N), "i"(4 * WSLOT), "i"(4 * WSLOT + 3), "i"(OFF) : "memory", ACLOB);
}
template <int RSLOT> __device__ __forceinline__ v4u aread(void) {
  unsigned a, b, c, d;
  asm volatile("v_accvgpr_read_b32 %0, a%c4\n\tv_accvgpr_read_b32 %1, a%c5\n\tv_accvgpr_read_b32 %2, a%c6\n\tv_accvgpr_read_b32 %3, a%c7\n\ts_nop 1"
               : "=v"(a), "=v"(b), "=v"(c), "=v"(d) : "i"(4 * RSLOT), "i"(4 * RSLOT + 1), "i"(4 * RSLOT + 2), "i"(4 * RSLOT + 3) : "memory", ACLOB);
  return v4u{a, b, c, d};
}
__device__ __forceinline__ unsigned aread_ticket(void) {
  unsigned a;
  asm volatile("v_accvgpr_read_b32 %0, a115\n\ts_nop 1" : "=v"(a) :: "memory", ACLOB);
  return a;
}
__device__ __forceinline__ void st128sc1(unsigned voff, u64 sbase, const v4u& v) {
  asm volatile("global_store_dwordx4 %0, %1, %2 sc1\n\ts_nop 1" :: "v"(voff), "v"(v), "s"(sbase) : "memory");
}
__device__ __forceinline__ void st64sc1(unsigned voff, u64 sbase, unsigned lo, unsigned hi) {
  const v2u v = {lo, hi};
  asm volatile("global_store_dwordx2 %0, %1, %2 sc1" :: "v"(voff), "v"(v), "s"(sbase) : "memory");
}
__device__ __forceinline__ void st16(unsigned voff, u64 sbase, unsigned v) {
  asm volatile("global_store_short %0, %1, %2" :: "v"(voff), "v"(v), "s"(sbase) : "memory");
}
__device__ __forceinline__ v2u ld64sc1_wait(unsigned voff, u64 sbase) {
  v2u v;
  asm volatile("global_load_dwordx2 %0, %1, %2 sc1\n\ts_waitcnt vmcnt(0)" : "=&v"(v) : "v"(voff), "s"(sbase) : "memory");
  return v;
}

__device__ __forceinline__ float dot2(unsigned a, unsigned b, float acc) {
  return __builtin_amdgcn_fdot2_f32_bf16(__builtin_bit_cast(bf16x2, a), __builtin_bit_cast(bf16x2, b), acc, false);
}
__device__ __forceinline__ float lo16(unsigned v) { return __builtin_bit_cast(float, v << 16); }
__device__ __forceinline__ float hi16(unsigned v) { return __builtin_bit_cast(float, v & 0xffff0000u); }
template <int CTRL> __device__ __forceinline__ float dpp(float x) {
  return __builtin_bit_cast(float, __builtin_amdgcn_mov_dpp(__builtin_bit_cast(int, x), CTRL, 0xf, 0xf, true));
}
template <int CTRL> __device__ __forceinline__ unsigned dppu(unsigned x) { return (unsigned)__builtin_amdgcn_mov_dpp((int)x, CTRL, 0xf, 0xf, true); }
constexpr int XOR1 = 0xB1, XOR2 = 0x4E, XOR7 = 0x141, XOR8 = 0x128, ROR4 = 0x124;
__device__ __forceinline__ unsigned cvtpk(float lo, float hi) {
  const bf16x2 v = {(__bf16)lo, (__bf16)hi};
  return __builtin_bit_cast(unsigned, v);
}
__device__ __forceinline__ float rbf(float x) { return lo16(cvtpk(x, x)); }
__device__ __forceinline__ float pair32(float a, float b) {
  auto s = __builtin_amdgcn_permlane32_swap(__float_as_uint(a), __float_as_uint(b), false, false);
  return __uint_as_float(s[0]) + __uint_as_float(s[1]);
}
__device__ __forceinline__ float pair16(float a, float b) {
  auto s = __builtin_amdgcn_permlane16_swap(__float_as_uint(a), __float_as_uint(b), false, false);
  return __uint_as_float(s[0]) + __uint_as_float(s[1]);
}
__device__ __forceinline__ float wave_sum(float x) {
  x += dpp<XOR1>(x); x += dpp<XOR2>(x); x += dpp<XOR7>(x); x += dpp<XOR8>(x);
  x = pair16(x, x); x = pair32(x, x); return x;
}
__device__ __forceinline__ float wave_max(float x) {
  x = fmaxf(x, dpp<XOR1>(x)); x = fmaxf(x, dpp<XOR2>(x)); x = fmaxf(x, dpp<XOR7>(x)); x = fmaxf(x, dpp<XOR8>(x));
  auto s = __builtin_amdgcn_permlane16_swap(__float_as_uint(x), __float_as_uint(x), false, false);
  x = fmaxf(__uint_as_float(s[0]), __uint_as_float(s[1]));
  auto t = __builtin_amdgcn_permlane32_swap(__float_as_uint(x), __float_as_uint(x), false, false);
  return fmaxf(__uint_as_float(t[0]), __uint_as_float(t[1]));
}
__device__ __forceinline__ float treduce16(float (&x)[16], int c) {
#define TR_STEP(HALF, CTRL, BIT) _Pragma("unroll") for (int h = 0; h < (HALF); ++h) { \
    const float lo_ = x[h], hi_ = x[h + (HALF)]; const float t1_ = lo_ + dpp<CTRL>(lo_), t2_ = hi_ + dpp<CTRL>(hi_); x[h] = (c & (BIT)) ? t2_ : t1_; }
  TR_STEP(8, XOR8, 8) TR_STEP(4, XOR7, 4) TR_STEP(2, XOR2, 2) TR_STEP(1, XOR1, 1)
#undef TR_STEP
  return x[0];
}
template <int T> __device__ __forceinline__ float bcast(float p) { return dpp<0x150 + T>(p); }
__device__ __forceinline__ void fma8(float (&acc)[8], float p, const v4u& v) {
#pragma unroll
  for (int k = 0; k < 4; ++k) { acc[2 * k] = fmaf(p, lo16(v[k]), acc[2 * k]); acc[2 * k + 1] = fmaf(p, hi16(v[k]), acc[2 * k + 1]); }
}
__device__ __forceinline__ unsigned plo(unsigned hi_row, unsigned lo_row) { return __builtin_amdgcn_perm(hi_row, lo_row, 0x05040100u); }
__device__ __forceinline__ unsigned phi(unsigned hi_row, unsigned lo_row) { return __builtin_amdgcn_perm(hi_row, lo_row, 0x07060302u); }

constexpr int younger(int U, int X, int NA, int NB) { return (U + DM - 1 - X) + ((U >= 1 && X <= DM) ? NA : 0) + ((U >= 9 && X <= 8 + DM) ? NB : 0); }
struct Stream {
  u64 K, V; unsigned voff; int rot;
  __device__ __forceinline__ u64 base(int round) const { return ((round < 32) ? K : V) + (u64)(unsigned)(((round + rot) & 31) << 15); }
  template <int J, int SLOT> __device__ __forceinline__ void issue(int round0) const { ald<SLOT, (J & 3) * 1024>(voff, base(round0 + (J >> 2))); }
  template <int U, int NA, int NB> __device__ __forceinline__ v4u step(int round0, int mode) const {
    return astep<(U & 7), younger(U, U, NA, NB), younger(U, U + 1, NA, NB), younger(U, U + 2, NA, NB), younger(U, U + 3, NA, NB), ((U + DM) & 7), ((U + DM) & 3) * 1024>(
        voff, base(round0 + ((U + DM) >> 2)), mode);
  }
};
__device__ __forceinline__ float qk(const v4u& k, const v4u& q) { return dot2(k[3], q[3], dot2(k[2], q[2], dot2(k[1], q[1], dot2(k[0], q[0], 0.f)))); }
#define REP16(M) M(0) M(1) M(2) M(3) M(4) M(5) M(6) M(7) M(8) M(9) M(10) M(11) M(12) M(13) M(14) M(15)
#define REP15(M) M(1) M(2) M(3) M(4) M(5) M(6) M(7) M(8) M(9) M(10) M(11) M(12) M(13) M(14) M(15)
#define REP8(M) M(0) M(1) M(2) M(3) M(4) M(5) M(6) M(7)
#define BARRIER() asm volatile("s_waitcnt lgkmcnt(0)\n\ts_barrier" ::: "memory")

constexpr int WQ0 = 4, QPOST = W0 + 8 - WQ0, QGET = 10, XSTEP = 8;
static_assert(QGET > QPOST && QGET < 16 - W0 && XSTEP + WQ0 >= 8 && XSTEP + W0 < 16, "tile-0 schedule");
constexpr int t0_ext(int m) { return m < 0 ? 2 * WQ0 : (m + WQ0 < 8 ? 2 : (m == XSTEP ? 4 : 0)); }
constexpr int t0_wait(int n) { int c = W0 - 1; for (int m = (n - W0 < -1 ? -1 : n - W0); m <= n - 1; ++m) c += t0_ext(m); return c; }

__device__ __forceinline__ float fin_score(float d) { return rbf(rbf(d) * 0.08838834764831845f); }
constexpr float LOG2E = 1.4426950408889634f;

__global__ void __launch_bounds__(WAVES * 64, 2) fused_decode(const void* xp, const void* Kp, const void* wqp, const void* Vp, const void* wgp,
                                                             const void* wup, const void* wdp, const void* wop, void* wsp, void* outp) {
  __shared__ __attribute__((aligned(16))) float lds[8 * 128 + 8 * 16 + 8 * 68 + 8 * 32 + 4];
  float* Lq = lds;
  float* Lg = lds + 1024;
  unsigned* Lo = (unsigned*)(lds + 1024 + 128);
  float* Lr = lds + 1024 + 128 + 8 * 68;
  volatile LAS unsigned* Lcnt = (volatile LAS unsigned*)(Lr + 256);
  asm volatile("" :: "s"(wsp), "s"(outp), "s"(xp));
  STAMP(0)
  const int tid = threadIdx.x, lane = tid & 63, w = __builtin_amdgcn_readfirstlane(tid >> 6), r = lane >> 4, c = lane & 15;
  const int b = blockIdx.x, xcd = b & 7, i8 = b >> 3;
  const int head = xcd * 4 + (i8 & 3), split = i8 >> 2, chunk = head * 8 + split;
  const bool has_mlp = i8 < 28;
  const int j = xcd * 28 + (has_mlp ? i8 : 27);
  const u64 ws = (u64)wsp;
  const u64 mlb = ws + WS_ML + head * 512;

  Stream s;
  { const u64 co = ((u64)chunk << 20) + (u64)(w << 12); s.K = (u64)Kp + co; s.V = (u64)Vp + co; s.voff = lane * 16; s.rot = b & 31; }
  v4u x8; unsigned xs; v4u wq[16];
  const unsigned wq_vo = r * 8192 + c * 16; const u64 wq_sb = (u64)wqp + (u64)w * (64 * 8192) + head * 256;
#define WQ_PAIR(P) { ld128w(wq[2 * (P)], wq_vo, wq_sb + (u64)(2 * (P)) * (4 * 8192)); ld128w(wq[2 * (P) + 1], wq_vo, wq_sb + (u64)(2 * (P) + 1) * (4 * 8192)); }
  ld128w(x8, lane * 16, (u64)xp);
  ld16(xs, (4 * c + r) * 2, (u64)xp + w * 128);
  s.issue<0, 0>(0); s.issue<1, 1>(0); s.issue<2, 2>(0);
  __builtin_amdgcn_sched_barrier(0);
  const u64 tstart = __builtin_amdgcn_s_memrealtime();
  if (W0 > 3) s.issue<3, 3>(0); if (W0 > 4) s.issue<4, 4>(0);
  WQ_PAIR(0) WQ_PAIR(1) WQ_PAIR(2) WQ_PAIR(3)
  if (tid == 0) { Lcnt[0] = 0u; Lcnt[1] = 0u; Lcnt[2] = (unsigned)tstart + SCH_T0; }
  BARRIER();
  const int mg = lane >> 4, mn = lane & 15, cg = mn >> 1, rowsel = lane & 1;
  const unsigned gu_vo = (4 * c + r) * 3584;
  const u64 g_sb = (u64)wgp + (u64)w * (64 * 3584) + j * 16, u_sb = (u64)wup + (u64)w * (64 * 3584) + j * 16;
  const u64 wo_sb = (u64)wop + (u64)head * (128 * 1024) + w * 128;
  unsigned tch[8] = {0, 0, 0, 0, 0, 0, 0, 0};
  if (TOUCH & 1) {
#define T(I, A) asm volatile("s_load_dword %0, %1, 0x0" : "=s"(tch[I]) : "s"((u64)(A) & ~3ull) : "memory");
    T(0, g_sb) T(1, u_sb) T(2, (u64)wdp + (u64)j * 8192) T(3, ws + 2048) T(4, wo_sb) T(5, s.V) T(6, (u64)outp) T(7, g_sb + 63 * 3584)
#undef T
  }
  float xn; unsigned xpair;
  float qa[8] = {0.f, 0.f, 0.f, 0.f, 0.f, 0.f, 0.f, 0.f};
  v4u q;
  float sc[8];
  float d[16];
#define PRE(N) apre<t0_wait(N), ((N) + W0 < 16 ? (N) + W0 : ((N) + W0) & 7), (((N) + W0) & 3) * 1024>(s.voff, s.base(((N) + W0) >> 2));
#define QP(P) { asm volatile("" : "+v"(wq[2 * (P)]), "+v"(wq[2 * (P) + 1])); const unsigned xx = dppu<0x150 + 2 * (P)>(xpair); \
      _Pragma("unroll") for (int k = 0; k < 4; ++k) { qa[2 * k] = dot2(plo(wq[2 * (P) + 1][k], wq[2 * (P)][k]), xx, qa[2 * k]); \
        qa[2 * k + 1] = dot2(phi(wq[2 * (P) + 1][k], wq[2 * (P)][k]), xx, qa[2 * k + 1]); } }
#define KQ(U) d[U] = qk(aread<U>(), q);
#define M(N) PRE(N) \
    if ((N) + WQ0 < 8) WQ_PAIR(((N) + WQ0) & 7) \
    if ((N) == XSTEP) { ald_gu(gu_vo, g_sb, u_sb, 0); ald64sc1<108>(0u, ws + WS_EPOCH); \
                        aldw<24, 0>((lane >> 3) * 1024 + (lane & 7) * 16, (u64)wdp + (u64)j * 8192 + w * 128); } \
    if ((N) == 0) { \
        \
      asm volatile("" : "+v"(x8), "+v"(xs)); \
      float xv[8]; \
      _Pragma("unroll") for (int k = 0; k < 4; ++k) { xv[2 * k] = lo16(x8[k]); xv[2 * k + 1] = hi16(x8[k]); } \
      float sum = 0.f; \
      _Pragma("unroll") for (int k = 0; k < 8; ++k) sum += xv[k]; \
      const float mean = wave_sum(sum) * (1.f / 512.f); \
      float sq = 0.f; \
      _Pragma("unroll") for (int k = 0; k < 8; ++k) { const float dlt = xv[k] - mean; sq = fmaf(dlt, dlt, sq); } \
      const float var = wave_sum(sq) * (1.f / 512.f); \
      xn = rbf(rbf(lo16(xs) - rbf(mean)) / rbf(sqrtf(rbf(rbf(var) + 1e-5f)))); \
      const unsigned xb = __float_as_uint(xn); \
      xpair = phi(dppu<XOR1>(xb), xb);     \
      STAMP(1) \
    } \
    if ((N) == W0) { QP(0) QP(1) QP(2) QP(3) } \
    if ((N) > W0 && (N) - W0 - 1 + WQ0 < 8) QP(((N) - W0 - 1 + WQ0) & 7) \
    if ((N) == QPOST) { \
      const float b0 = pair32(qa[0], qa[4]), b1 = pair32(qa[1], qa[5]), b2 = pair32(qa[2], qa[6]), b3 = pair32(qa[3], qa[7]); \
      const float c0 = pair16(b0, b2), c1 = pair16(b1, b3); \
      *(f32x2*)(Lq + w * 128 + 8 * c + 2 * r) = f32x2{c0, c1};     \
      asm volatile("s_waitcnt lgkmcnt(0)" : "+s"(tch[0]), "+s"(tch[1]), "+s"(tch[2]), "+s"(tch[3]), "+s"(tch[4]), "+s"(tch[5]), "+s"(tch[6]), "+s"(tch[7]) :: "memory"); \
      if (lane == 0) __hip_atomic_fetch_add((LAS unsigned*)(Lcnt + 1), 1u, __ATOMIC_RELAXED, __HIP_MEMORY_SCOPE_WORKGROUP); \
    } \
    if ((N) == QGET) { \
      for (unsigned spins = 0; Lcnt[1] != 8u && spins < SPIN_CAP; ++spins) __builtin_amdgcn_s_sleep(1); \
      asm volatile("" ::: "memory"); \
      f32x4 a0 = *(const f32x4*)(Lq + (2 * r) * 128 + 8 * c), a1 = *(const f32x4*)(Lq + (2 * r) * 128 + 8 * c + 4); \
      f32x4 b0 = *(const f32x4*)(Lq + (2 * r + 1) * 128 + 8 * c), b1 = *(const f32x4*)(Lq + (2 * r + 1) * 128 + 8 * c + 4); \
      a0 += b0; a1 += b1; \
      float v[8] = {a0[0], a0[1], a0[2], a0[3], a1[0], a1[1], a1[2], a1[3]}; \
      _Pragma("unroll") for (int k = 0; k < 8; ++k) { v[k] = pair16(v[k], v[k]); v[k] = pair32(v[k], v[k]); } \
      _Pragma("unroll") for (int k = 0; k < 4; ++k) q[k] = cvtpk(v[2 * k], v[2 * k + 1]); \
      STAMP(2) \
      KQ(0) KQ(1) KQ(2) KQ(3) \
    } \
    if ((N) == QGET + 1) { KQ(4) KQ(5) KQ(6) KQ(7) KQ(8) KQ(9) KQ(10) } \
    if ((N) > QGET) KQ(N) \
    __builtin_amdgcn_sched_barrier(0);
  REP16(M)
#undef M
  sc[7] = fin_score(treduce16(d, c));
  if (W0 < 5) { PRE(16) } if (W0 < 4) { PRE(17) }
  unsigned tag_lo, tag_hi;
  {
    const v4u e = aread<27>();
    const u64 tag = (((u64)(unsigned)__builtin_amdgcn_readfirstlane(e[1]) << 32) | (u64)(unsigned)__builtin_amdgcn_readfirstlane(e[0])) + 1ull;
    tag_lo = (unsigned)tag; tag_hi = (unsigned)(tag >> 32);
    asm volatile("v_accvgpr_write_b32 a114, 1" ::: ACLOB);
  }
  const bool clearer = !has_mlp && w == 0;
  if (clearer && !(ABL & 4)) {
    const v4u sv = {SENT, SENT, SENT, SENT};
    for (int i = 0; i < 16; ++i) st128sc1(lane * 16 + i * 1024, ws + WS_Y + head * 16384, sv);
    if (lane < 32) st128sc1(lane * 16, mlb, sv);
    if (lane < 16) st32sc1(lane * 1024 + xcd * 128 + (head & 3) * 4, ws + WS_CNT, SENT);
  }
  const unsigned wo_vo = (64 * rowsel + 8 * mg) * 1024 + cg * 16;
  constexpr int MODE0 = FB_T1;
  int mode = MODE0;
#define NOMINAL(TILE) ((TILE) <= 8 ? (TILE) * SCH_K : 8 * SCH_K + ((TILE) - 8) * SCH_V)
#define ANCHOR_LD(TILE) if (ANCH && w == 0 && ((TILE) % ANCH_EVERY) == 0) aanchor(((lane >> 2) & 7) * 128 + (lane & 3) * 4 + ((TILE) - 2) * 1024, ws + WS_CNT); else adummy(0u, (u64)xp);
#define FEEDBACK(TILE) if (FB) { const unsigned now_ = (unsigned)__builtin_amdgcn_s_memrealtime(); \
    if (ANCH && w == 0) { \
      if (clearer && (TILE) >= 2 && (((TILE) + 2) % ANCH_EVERY) == 0 && lane == 0) st32sc1((TILE) * 1024 + xcd * 128 + (head & 3) * 4, ws + WS_CNT, now_ & 0xfffffffeu); \
      if ((TILE) >= 4 && ((TILE) % ANCH_EVERY) == 0) { const unsigned a_ = aread_ticket(); const bool ok_ = a_ != SENT; \
        const float sum_ = wave_sum(ok_ ? (float)(int)(a_ - now_) : 0.f); const int n_ = __popcll(__ballot(ok_)); \
        if (n_ >= 32) Lcnt[2] = now_ + (unsigned)((int)(sum_ / (float)n_) - NOMINAL((TILE) - 2) + ANCH_OFF); } } \
    mode = (int)(now_ - (unsigned)__builtin_amdgcn_readfirstlane((int)Lcnt[2])) - NOMINAL(TILE); \
    if (ANCH && (TILE) < 4 && mode < FB_T1) mode = FB_T1; }
#define FEEDBACK2(TILE) if (FB) { const unsigned now_ = (unsigned)__builtin_amdgcn_s_memrealtime(); \
    mode = (int)(now_ - (unsigned)__builtin_amdgcn_readfirstlane((int)Lcnt[2])) - NOMINAL(TILE) - ((TILE) < 8 ? SCH_K / 2 : SCH_V / 2); \
    if (ANCH && (TILE) < 4 && mode < FB_T1) mode = FB_T1; }
#define SETPRIO(TILE) if (PRIO == 2) { if (((w >> 2) ^ (TILE)) & 1) __builtin_amdgcn_s_setprio(1); else __builtin_amdgcn_s_setprio(0); }
  for (int tile = 1; tile < 8; ++tile) {
    SETPRIO(tile)
    d[0] = qk(s.step<0, 3, 2>(tile * 4, mode), q);
    switch (tile) {
      case 1: aldw<8, 0>(wo_vo, wo_sb); aldw<9, 1024>(wo_vo, wo_sb); aldw<10, 2048>(wo_vo, wo_sb); break;
      case 2: aldw<11, 3072>(wo_vo, wo_sb); aldw<12, 0>(wo_vo, wo_sb + 4096); aldw<13, 1024>(wo_vo, wo_sb + 4096); break;
      case 3: aldw<14, 2048>(wo_vo, wo_sb + 4096); aldw<15, 3072>(wo_vo, wo_sb + 4096); ald64sc1<110>((lane & 31) * 8, ws + WS_FLAG); break;
      case 4: aldw<16, 0>(wo_vo, wo_sb + 32768); aldw<17, 1024>(wo_vo, wo_sb + 32768); ANCHOR_LD(tile) break;
      case 5: aldw<18, 2048>(wo_vo, wo_sb + 32768); aldw<19, 3072>(wo_vo, wo_sb + 32768); ANCHOR_LD(tile) break;
      case 6: aldw<20, 0>(wo_vo, wo_sb + 36864); aldw<21, 1024>(wo_vo, wo_sb + 36864); ANCHOR_LD(tile) break;
      default: aldw<22, 2048>(wo_vo, wo_sb + 36864); aldw<23, 3072>(wo_vo, wo_sb + 36864); ANCHOR_LD(tile) break;
    }
#define M(U) d[U] = qk(s.step<U, 3, 2>(tile * 4, mode), q); \
    if (U == 7) { \
      { FEEDBACK(tile) } \
        \
      if (tile == 1 && clearer && lane == 0) st64sc1(0u, ws + WS_FLAG + head * 8, tag_lo, tag_hi); \
    } \
    if (U == 15) { FEEDBACK2(tile) } \
    if (U == 8) ald_gu(gu_vo, g_sb, u_sb, tile);
    REP15(M)
#undef M
#pragma unroll
    for (int k = 0; k < 7; ++k) sc[k] = sc[k + 1];
    sc[7] = fin_score(treduce16(d, c));
    if (tile == 1) { STAMP(6) }
    if (tile == 3) {
      const v4u e = aread<27>();
      if (!__all(e[2] == tag_lo && e[3] == tag_hi)) {
        for (unsigned spins = 0; spins < SPIN_CAP; ++spins) {
          __builtin_amdgcn_s_sleep(2);
          const v2u f = ld64sc1_wait((lane & 31) * 8, ws + WS_FLAG);
          if (__all(f[0] == tag_lo && f[1] == tag_hi)) break;
        }
      }
    }
  }
  STAMP(3)
  float mw;
  {
    float m = sc[0];
#pragma unroll
    for (int k = 1; k < 8; ++k) m = fmaxf(m, sc[k]);
    mw = wave_max(m);
    float l = 0.f;
#pragma unroll
    for (int k = 0; k < 8; ++k) { sc[k] = __builtin_amdgcn_exp2f((sc[k] - mw) * LOG2E); l += sc[k]; }
    const float lw = wave_sum(l);
    if (!(ABL & 1) && lane == 0) st64sc1(0u, mlb + (split * 8 + w) * 8, __float_as_uint(mw) & 0xfffffffeu, __float_as_uint(lw) & 0xfffffffeu);
  }
  float acc[8] = {0.f, 0.f, 0.f, 0.f, 0.f, 0.f, 0.f, 0.f};
  for (int tile = 8; tile < 15; ++tile) {
    SETPRIO(tile)
    const float p = sc[0];
#define M(U) fma8(acc, bcast<U>(p), s.step<U, 1, 0>(tile * 4, mode)); \
    if (U == 0) { ANCHOR_LD(tile) if (!(ABL & 1) && tile == 14) ald64sc1<112>(lane * 8, mlb); } \
    if (U == 2 && tile == 8) { \
      const v4u wg = aread<25>(), wu = aread<26>(); \
      float g[16]; \
      _Pragma("unroll") for (int k = 0; k < 4; ++k) { g[2 * k] = xn * lo16(wg[k]); g[2 * k + 1] = xn * hi16(wg[k]); g[8 + 2 * k] = xn * lo16(wu[k]); g[8 + 2 * k + 1] = xn * hi16(wu[k]); } \
      float t = treduce16(g, c); \
      t = pair16(t, t); t = pair32(t, t); \
      if (r == 0) Lg[w * 16 + c] = t; \
      asm volatile("s_waitcnt lgkmcnt(0)" ::: "memory"); \
      if (lane == 0) __hip_atomic_fetch_add((LAS unsigned*)Lcnt, 1u, __ATOMIC_RELAXED, __HIP_MEMORY_SCOPE_WORKGROUP); \
    } \
    if (U == 7) { FEEDBACK(tile) } \
    if (U == 15) { FEEDBACK2(tile) }
    REP16(M)
#undef M
#pragma unroll
    for (int k = 0; k < 7; ++k) sc[k] = sc[k + 1];
  }
  v4u bf[16];
  float scale, ymlp[4];
  {
    const float p = sc[0];
#define LISSUE(J) { if ((J) >= LT_END) s.issue<J, (J) & 7>(60); else awin<(J) & 7, ((J) & 3) * 1024>(s.voff, s.base(60 + ((J) >> 2)), mode); }
    LISSUE(5)
    {
      const v4u e = aread<28>();
      unsigned mb = e[0], lb = e[1];
      if (!(ABL & 1) && !__all(mb != SENT && lb != SENT)) {
        for (unsigned spins = 0; spins < SPIN_CAP; ++spins) {
          __builtin_amdgcn_s_sleep(2);
          const v2u t = ld64sc1_wait(lane * 8, mlb);
          mb = t[0]; lb = t[1];
          if (__all(mb != SENT && lb != SENT)) break;
        }
      }
      const float mi = __uint_as_float(mb), li = __uint_as_float(lb);
      const float M = wave_max(mi);
      const float L = wave_sum(li * __builtin_amdgcn_exp2f((mi - M) * LOG2E));
      scale = __builtin_amdgcn_exp2f((mw - M) * LOG2E) / L;
    }
    LISSUE(6)
#define M(T) { const v4u A = aread<8 + 2 * T>(), B = aread<9 + 2 * T>(); _Pragma("unroll") for (int k = 0; k < 4; ++k) { \
      bf[(T >> 2) * 8 + 2 * k][T & 3] = plo(B[k], A[k]); bf[(T >> 2) * 8 + 2 * k + 1][T & 3] = phi(B[k], A[k]); } }
    REP8(M)
#undef M
    LISSUE(7)
    {
      for (unsigned spins = 0; Lcnt[0] != 8u && spins < SPIN_CAP; ++spins) __builtin_amdgcn_s_sleep(1);
      asm volatile("" ::: "memory");
      const int f = lane >> 3;
      float g = 0.f, u = 0.f;
#pragma unroll
      for (int ww = 0; ww < 8; ++ww) { g += Lg[ww * 16 + f]; u += Lg[ww * 16 + 8 + f]; }
      g = rbf(g); u = rbf(u);
      float hval = rbf(rbf(1.f / (1.f + __expf(-g))) * u);
      if (!has_mlp) hval = 0.f;
      const v4u wd = aread<24>();
      float ym[8];
#pragma unroll
      for (int k = 0; k < 4; ++k) { ym[2 * k] = hval * lo16(wd[k]); ym[2 * k + 1] = hval * hi16(wd[k]); }
#pragma unroll
      for (int k = 0; k < 4; ++k) { float t = pair32(ym[k], ym[k + 4]); t = pair16(t, t); t += dpp<XOR8>(t);
        ymlp[k] = __builtin_bit_cast(float, __builtin_amdgcn_ds_bpermute((rowsel * 32 + cg) * 4, __builtin_bit_cast(int, t)));
        asm volatile("" : "+v"(ymlp[k])); }
    }
#define M(U) { const v4u v_ = aget<U, 7>(); LISSUE(8 + U) fma8(acc, bcast<U>(p), v_); }
    REP8(M)
#undef M
#define M(U) fma8(acc, bcast<8 + U>(p), aget<U, 7 - U>());
    REP8(M)
#undef M
  }
  STAMP(4)
  {
    const float b0 = pair32(acc[0], acc[4]), b1 = pair32(acc[1], acc[5]), b2 = pair32(acc[2], acc[6]), b3 = pair32(acc[3], acc[7]);
    const float c0 = pair16(b0, b2), c1 = pair16(b1, b3);
    Lo[w * 68 + 4 * c + r] = cvtpk(c0 * scale, c1 * scale);
  }
  BARRIER();
  {
    const unsigned* ap = Lo + (mn & 7) * 68 + 32 * (mn >> 3) + 4 * mg;
    const v4u a0 = *(const v4u*)ap, a1 = *(const v4u*)(ap + 16);
    f32x4 dd[8];
#define MF0(D, B) "v_mfma_f32_16x16x32_bf16 %" #D ", %8, %" #B ", 0\n\t"
#define MF1(D, B) "v_mfma_f32_16x16x32_bf16 %" #D ", %9, %" #B ", %" #D "\n\t"
    asm volatile("s_nop 1\n\t" MF0(0, 10) MF0(1, 11) MF0(2, 12) MF0(3, 13) MF0(4, 14) MF0(5, 15) MF0(6, 16) MF0(7, 17)
                 MF1(0, 18) MF1(1, 19) MF1(2, 20) MF1(3, 21) MF1(4, 22) MF1(5, 23) MF1(6, 24) MF1(7, 25) "s_nop 7"
                 : "=&v"(dd[0]), "=&v"(dd[1]), "=&v"(dd[2]), "=&v"(dd[3]), "=&v"(dd[4]), "=&v"(dd[5]), "=&v"(dd[6]), "=&v"(dd[7])
                 : "v"(a0), "v"(a1), "v"(bf[0]), "v"(bf[1]), "v"(bf[2]), "v"(bf[3]), "v"(bf[4]), "v"(bf[5]), "v"(bf[6]), "v"(bf[7]),
                   "v"(bf[8]), "v"(bf[9]), "v"(bf[10]), "v"(bf[11]), "v"(bf[12]), "v"(bf[13]), "v"(bf[14]), "v"(bf[15]));
    v4u o;
#pragma unroll
    for (int k = 0; k < 4; ++k) {
      const float t0 = (dd[k][0] + dd[k][1]) + (dd[k][2] + dd[k][3]), t1 = (dd[k + 4][0] + dd[k + 4][1]) + (dd[k + 4][2] + dd[k + 4][3]);
      auto sw = __builtin_amdgcn_permlane32_swap(__float_as_uint(t0), __float_as_uint(t1), false, false);
      float v = __uint_as_float(rowsel ? sw[1] : sw[0]);
      v += dpp<XOR1>(v);
      v = pair16(v, v);
      auto sv = __builtin_amdgcn_permlane32_swap(__float_as_uint(v), __float_as_uint(v), false, false);
      o[k] = __float_as_uint(__uint_as_float(rowsel ? sv[1] : sv[0]) + ymlp[k]) & 0xfffffffeu;
    }
    if (lane < 16) st128sc1((2 * w + (lane >> 3)) * 32768 + chunk * 128 + (lane & 7) * 16, ws + WS_Y, o);
  }
  STAMP(5)
  if (!has_mlp && (head & 1) == 0) {
    unsigned xv;
    ld16(xv, (lane & 31) * 2, (u64)xp + head * 32);
    if (TOUCH & 2) adummy(0u, (u64)outp + head * 32);
    const u64 yb = ws + WS_Y + head * 16384 + w * 4096;
    v4u y0, y1, y2, y3;
    for (unsigned spins = 0; spins < SPIN_CAP; ++spins) {
      asm volatile("global_load_dwordx4 %0, %5, %6 sc1\n\tglobal_load_dwordx4 %1, %5, %6 offset:1024 sc1\n\t"
                   "global_load_dwordx4 %2, %5, %6 offset:2048 sc1\n\tglobal_load_dwordx4 %3, %5, %6 offset:3072 sc1\n\ts_waitcnt vmcnt(0)"
                   : "=&v"(y0), "=&v"(y1), "=&v"(y2), "=&v"(y3), "+v"(xv) : "v"(lane * 16), "s"(yb) : "memory");
      const v4u t = (y0 | y1) | (y2 | y3);
      if (__all((((t[0] | t[1]) | (t[2] | t[3])) & 1u) == 0u)) break;
      if (POLL_SLEEP) __builtin_amdgcn_s_sleep(POLL_SLEEP);
    }
    float v[4];
#pragma unroll
    for (int k = 0; k < 4; ++k) v[k] = (__uint_as_float(y0[k]) + __uint_as_float(y1[k])) + (__uint_as_float(y2[k]) + __uint_as_float(y3[k]));
    float t = pair16(pair32(v[0], v[2]), pair32(v[1], v[3]));
    t += dpp<XOR8>(t);
    if ((lane & 8) == 0) Lr[w * 32 + 4 * (lane & 7) + (lane >> 4)] = t;
    BARRIER();
    if (w == 0 && lane < 32) {
      float sum = 0.f;
#pragma unroll
      for (int ww = 0; ww < 8; ++ww) sum += Lr[ww * 32 + lane];
      const float o = lo16(xv) + sum;
      st16(lane * 2, (u64)outp + head * 32, cvtpk(o, o));
      if (head == 0 && lane == 0) st64sc1(0u, ws + WS_EPOCH, tag_lo, tag_hi);
    }
  }
}

extern "C" void kernel_launch(void* const* d_in, const int* in_sizes, int n_in, void* d_out, int out_size, void* d_ws, size_t ws_size,
                              hipStream_t stream) {
  fused_decode<<<NWG, WAVES * 64, 0, stream>>>(d_in[0], d_in[1], d_in[3], d_in[2], d_in[6], d_in[7], d_in[8], d_in[4], d_ws, d_out);
}
```

```cpp
#include <hip/hip_runtime.h>
#include <hip/hip_bf16.h>

#define WPOL ""
#define KVPOL "nt"
#define PRIO 2
#define FB 1
#define FB_T0 -100
#define FB_T1 0
#define FB_T2 80
#define FB_HALF 1
#define TICKET 3
#define ABL 0
#define POLL_SLEEP 0
#define SCH_T0 600
#define SCH_K 450
#define SCH_V 426
#define ANCH 1
#define ANCH_MASK 0x55f0
#define ANCH_OFF 65
#define LT_A 30
#define LT_B -20
#define LT_WA 4
#define LT_WB 3
#define LT_END 13
#define TOUCH 3
#define LT_PRIO 0
#define H2 0
#define H3 0
#define ROTM 0
#define ROTF (ROTM == 0 ? (b & 31) : ROTM == 1 ? 0 : ROTM == 2 ? (i8 & 31) : ROTM == 3 ? ((chunk * 5) & 31) : ROTM == 4 ? (chunk & 31) : ROTM == 5 ? ((b * 5) & 31) : ROTM == 6 ? ((xcd * 4 + (i8 >> 3)) & 31) : ((i8 * 4 + xcd) & 31))
#define LASTL 0
#define BASEMODE 0

typedef unsigned v4u __attribute__((ext_vector_type(4)));
typedef unsigned v2u __attribute__((ext_vector_type(2)));
typedef float f32x4 __attribute__((ext_vector_type(4)));
typedef float f32x2 __attribute__((ext_vector_type(2)));
typedef __bf16 bf16x2 __attribute__((ext_vector_type(2)));
typedef __bf16 bf16x8 __attribute__((ext_vector_type(8)));
typedef unsigned long long u64;
#define LAS __attribute__((address_space(3)))
#define STAMP(k)

constexpr int NWG = 256, WAVES = 8;
#define W0V 3
constexpr int W0 = W0V;
constexpr int DM = 5;
constexpr unsigned WS_EPOCH = 0, WS_FLAG = 1024, WS_ML = 4096, WS_CNT = 32768, WS_Y = 65536;
constexpr unsigned SENT = 0xffffffffu;
constexpr unsigned SPIN_CAP = 1u << 20;

__device__ __forceinline__ void ld128w(v4u& dst, unsigned voff, u64 sbase) {
  asm volatile("global_load_dwordx4 %0, %1, %2 " WPOL : "=v"(dst) : "v"(voff), "s"(sbase) : "memory");
}
__device__ __forceinline__ void ld16(unsigned& dst, unsigned voff, u64 sbase) {
  asm volatile("global_load_ushort %0, %1, %2" : "=v"(dst) : "v"(voff), "s"(sbase) : "memory");
}

#define ACLOB "a0","a1","a2","a3","a4","a5","a6","a7","a8","a9","a10","a11","a12","a13","a14","a15","a16","a17","a18","a19","a20","a21","a22","a23","a24","a25","a26","a27","a28","a29","a30","a31", \
  "a32","a33","a34","a35","a36","a37","a38","a39","a40","a41","a42","a43","a44","a45","a46","a47","a48","a49","a50","a51","a52","a53","a54","a55","a56","a57","a58","a59","a60","a61","a62","a63", \
  "a64","a65","a66","a67","a68","a69","a70","a71","a72","a73","a74","a75","a76","a77","a78","a79","a80","a81","a82","a83","a84","a85","a86","a87","a88","a89","a90","a91","a92","a93","a94","a95", \
  "a96","a97","a98","a99","a100","a101","a102","a103","a104","a105","a106","a107","a108","a109","a110","a111","a112","a113","a114","a115"
template <int SLOT, int OFF> __device__ __forceinline__ void ald(unsigned voff, u64 sbase) {
  asm volatile("global_load_dwordx4 a[%c2:%c3], %0, %1 offset:%c4 " KVPOL :: "v"(voff), "s"(sbase), "i"(4 * SLOT), "i"(4 * SLOT + 3), "i"(OFF) : "memory", ACLOB);
}
template <int SLOT, int OFF> __device__ __forceinline__ void aldw(unsigned voff, u64 sbase) {
  asm volatile("global_load_dwordx4 a[%c2:%c3], %0, %1 offset:%c4 " WPOL :: "v"(voff), "s"(sbase), "i"(4 * SLOT), "i"(4 * SLOT + 3), "i"(OFF) : "memory", ACLOB);
}
__device__ __forceinline__ void ald_gu(unsigned voff, u64 gbase, u64 ubase, int idx) {
  const u64 mask = 0xffull << (8 * idx);
  asm volatile("s_mov_b64 exec, %3\n\tglobal_load_dwordx4 a[100:103], %0, %1 " WPOL "\n\tglobal_load_dwordx4 a[104:107], %0, %2 " WPOL "\n\ts_mov_b64 exec, -1"
               :: "v"(voff), "s"(gbase), "s"(ubase), "s"(mask) : "memory", ACLOB);
}
template <int REG> __device__ __forceinline__ void ald64sc1(unsigned voff, u64 sbase) {
  asm volatile("global_load_dwordx2 a[%c2:%c3], %0, %1 sc1" :: "v"(voff), "s"(sbase), "i"(REG), "i"(REG + 1) : "memory", ACLOB);
}
__device__ __forceinline__ void aanchor(unsigned voff, u64 sbase) {
  asm volatile("global_load_dword a115, %0, %1 sc1" :: "v"(voff), "s"(sbase) : "memory", ACLOB);
}
__device__ __forceinline__ void adummy(unsigned zero, u64 sbase) {
  asm volatile("s_mov_b64 exec, 1\n\tglobal_load_dword a114, %0, %1\n\ts_mov_b64 exec, -1" :: "v"(zero), "s"(sbase) : "memory", ACLOB);
}
__device__ __forceinline__ void st32sc1(unsigned voff, u64 sbase, unsigned v) {
  asm volatile("global_store_dword %0, %1, %2 sc1" :: "v"(voff), "v"(v), "s"(sbase) : "memory");
}
template <int RSLOT, int N> __device__ __forceinline__ v4u aget(void) {
  unsigned a, b, c, d;
  asm volatile("s_waitcnt vmcnt(%c4)\n\tv_accvgpr_read_b32 %0, a%c5\n\tv_accvgpr_read_b32 %1, a%c6\n\tv_accvgpr_read_b32 %2, a%c7\n\tv_accvgpr_read_b32 %3, a%c8"
               : "=v"(a), "=v"(b), "=v"(c), "=v"(d) : "i"(N), "i"(4 * RSLOT), "i"(4 * RSLOT + 1), "i"(4 * RSLOT + 2), "i"(4 * RSLOT + 3) : "memory", ACLOB);
  return v4u{a, b, c, d};
}
template <int RSLOT, int N5, int N4, int N3, int N2, int WSLOT, int OFF> __device__ __forceinline__ v4u astep(unsigned voff, u64 sbase, int mode) {
  unsigned a, b, c, d;
  asm volatile("s_waitcnt vmcnt(%c7)\n\t"
               "s_cmp_gt_i32 %6, %c17\n\ts_cbranch_scc1 1f\n\t"
               "s_waitcnt vmcnt(%c8)\n\t"
               "s_cmp_gt_i32 %6, %c18\n\ts_cbranch_scc1 1f\n\t"
               "s_waitcnt vmcnt(%c9)\n\t"
               "s_cmp_gt_i32 %6, %c19\n\ts_cbranch_scc1 1f\n\t"
               "s_waitcnt vmcnt(%c20)\n"
               "1:\n\tglobal_load_dwordx4 a[%c14:%c15], %4, %5 offset:%c16 " KVPOL "\n\t"
               "v_accvgpr_read_b32 %0, a%c10\n\tv_accvgpr_read_b32 %1, a%c11\n\tv_accvgpr_read_b32 %2, a%c12\n\tv_accvgpr_read_b32 %3, a%c13"
               : "=&v"(a), "=&v"(b), "=&v"(c), "=&v"(d)
               : "v"(voff), "s"(sbase), "s"(mode), "i"(N5), "i"(N4), "i"(N3),
                 "i"(4 * RSLOT), "i"(4 * RSLOT + 1), "i"(4 * RSLOT + 2), "i"(4 * RSLOT + 3), "i"(4 * WSLOT), "i"(4 * WSLOT + 3), "i"(OFF),
                 "i"(FB_T2), "i"(FB_T1), "i"(FB_T0), "i"(N2)
               : "memory", "scc", ACLOB);
  return v4u{a, b, c, d};
}
template <int WSLOT, int OFF> __device__ __forceinline__ void awin(unsigned voff, u64 sbase, int mode) {
  asm volatile("s_cmp_gt_i32 %2, %c6\n\ts_cbranch_scc1 1f\n\t"
               "s_waitcnt vmcnt(%c8)\n\t"
               "s_cmp_gt_i32 %2, %c7\n\ts_cbranch_scc1 1f\n\t"
               "s_waitcnt vmcnt(%c9)\n"
               "1:\n\tglobal_load_dwordx4 a[%c3:%c4], %0, %1 offset:%c5 " KVPOL
               :: "v"(voff), "s"(sbase), "s"(mode), "i"(4 * WSLOT), "i"(4 * WSLOT + 3), "i"(OFF), "i"(LT_A), "i"(LT_B), "i"(LT_WA - 1), "i"(LT_WB - 1)
               : "memory", "scc", ACLOB);
}
template <int N, int WSLOT, int OFF> __device__ __forceinline__ void apre(unsigned voff, u64 sbase) {
  asm volatile("s_waitcnt vmcnt(%c2)\n\tglobal_load_dwordx4 a[%c3:%c4], %0, %1 offset:%c5 " KVPOL
               :: "v"(voff), "s"(sbase), "i"(N), "i"(4 * WSLOT), "i"(4 * WSLOT + 3), "i"(OFF) : "memory", ACLOB);
}
template <int RSLOT> __device__ __forceinline__ v4u aread(void) {
  unsigned a, b, c, d;
  asm volatile("v_accvgpr_read_b32 %0, a%c4\n\tv_accvgpr_read_b32 %1, a%c5\n\tv_accvgpr_read_b32 %2, a%c6\n\tv_accvgpr_read_b32 %3, a%c7\n\ts_nop 1"
               : "=v"(a), "=v"(b), "=v"(c), "=v"(d) : "i"(4 * RSLOT), "i"(4 * RSLOT + 1), "i"(4 * RSLOT + 2), "i"(4 * RSLOT + 3) : "memory", ACLOB);
  return v4u{a, b, c, d};
}
__device__ __forceinline__ unsigned aread_ticket(void) {
  unsigned a;
  asm volatile("v_accvgpr_read_b32 %0, a115\n\ts_nop 1" : "=v"(a) :: "memory", ACLOB);
  return a;
}
__device__ __forceinline__ void st128sc1(unsigned voff, u64 sbase, const v4u& v) {
  asm volatile("global_store_dwordx4 %0, %1, %2 sc1\n\ts_nop 1" :: "v"(voff), "v"(v), "s"(sbase) : "memory");
}
__device__ __forceinline__ void st64sc1(unsigned voff, u64 sbase, unsigned lo, unsigned hi) {
  const v2u v = {lo, hi};
  asm volatile("global_store_dwordx2 %0, %1, %2 sc1" :: "v"(voff), "v"(v), "s"(sbase) : "memory");
}
__device__ __forceinline__ void st16(unsigned voff, u64 sbase, unsigned v) {
  asm volatile("global_store_short %0, %1, %2" :: "v"(voff), "v"(v), "s"(sbase) : "memory");
}
__device__ __forceinline__ v2u ld64sc1_wait(unsigned voff, u64 sbase) {
  v2u v;
  asm volatile("global_load_dwordx2 %0, %1, %2 sc1\n\ts_waitcnt vmcnt(0)" : "=&v"(v) : "v"(voff), "s"(sbase) : "memory");
  return v;
}

__device__ __forceinline__ float dot2(unsigned a, unsigned b, float acc) {
  return __builtin_amdgcn_fdot2_f32_bf16(__builtin_bit_cast(bf16x2, a), __builtin_bit_cast(bf16x2, b), acc, false);
}
__device__ __forceinline__ float lo16(unsigned v) { return __builtin_bit_cast(float, v << 16); }
__device__ __forceinline__ float hi16(unsigned v) { return __builtin_bit_cast(float, v & 0xffff0000u); }
template <int CTRL> __device__ __forceinline__ float dpp(float x) {
  return __builtin_bit_cast(float, __builtin_amdgcn_mov_dpp(__builtin_bit_cast(int, x), CTRL, 0xf, 0xf, true));
}
template <int CTRL> __device__ __forceinline__ unsigned dppu(unsigned x) { return (unsigned)__builtin_amdgcn_mov_dpp((int)x, CTRL, 0xf, 0xf, true); }
constexpr int XOR1 = 0xB1, XOR2 = 0x4E, XOR7 = 0x141, XOR8 = 0x128, ROR4 = 0x124;
__device__ __forceinline__ unsigned cvtpk(float lo, float hi) {
  const bf16x2 v = {(__bf16)lo, (__bf16)hi};
  return __builtin_bit_cast(unsigned, v);
}
__device__ __forceinline__ float rbf(float x) { return lo16(cvtpk(x, x)); }
__device__ __forceinline__ float pair32(float a, float b) {
  auto s = __builtin_amdgcn_permlane32_swap(__float_as_uint(a), __float_as_uint(b), false, false);
  return __uint_as_float(s[0]) + __uint_as_float(s[1]);
}
__device__ __forceinline__ float pair16(float a, float b) {
  auto s = __builtin_amdgcn_permlane16_swap(__float_as_uint(a), __float_as_uint(b), false, false);
  return __uint_as_float(s[0]) + __uint_as_float(s[1]);
}
__device__ __forceinline__ float wave_sum(float x) {
  x += dpp<XOR1>(x); x += dpp<XOR2>(x); x += dpp<XOR7>(x); x += dpp<XOR8>(x);
  x = pair16(x, x); x = pair32(x, x); return x;
}
__device__ __forceinline__ float wave_max(float x) {
  x = fmaxf(x, dpp<XOR1>(x)); x = fmaxf(x, dpp<XOR2>(x)); x = fmaxf(x, dpp<XOR7>(x)); x = fmaxf(x, dpp<XOR8>(x));
  auto s = __builtin_amdgcn_permlane16_swap(__float_as_uint(x), __float_as_uint(x), false, false);
  x = fmaxf(__uint_as_float(s[0]), __uint_as_float(s[1]));
  auto t = __builtin_amdgcn_permlane32_swap(__float_as_uint(x), __float_as_uint(x), false, false);
  return fmaxf(__uint_as_float(t[0]), __uint_as_float(t[1]));
}
__device__ __forceinline__ float treduce16(float (&x)[16], int c) {
#define TR_STEP(HALF, CTRL, BIT) _Pragma("unroll") for (int h = 0; h < (HALF); ++h) { \
    const float lo_ = x[h], hi_ = x[h + (HALF)]; const float t1_ = lo_ + dpp<CTRL>(lo_), t2_ = hi_ + dpp<CTRL>(hi_); x[h] = (c & (BIT)) ? t2_ : t1_; }
  TR_STEP(8, XOR8, 8) TR_STEP(4, XOR7, 4) TR_STEP(2, XOR2, 2) TR_STEP(1, XOR1, 1)
#undef TR_STEP
  return x[0];
}
template <int T> __device__ __forceinline__ float bcast(float p) { return dpp<0x150 + T>(p); }
__device__ __forceinline__ void fma8(float (&acc)[8], float p, const v4u& v) {
#pragma unroll
  for (int k = 0; k < 4; ++k) { acc[2 * k] = fmaf(p, lo16(v[k]), acc[2 * k]); acc[2 * k + 1] = fmaf(p, hi16(v[k]), acc[2 * k + 1]); }
}
__device__ __forceinline__ unsigned plo(unsigned hi_row, unsigned lo_row) { return __builtin_amdgcn_perm(hi_row, lo_row, 0x05040100u); }
__device__ __forceinline__ unsigned phi(unsigned hi_row, unsigned lo_row) { return __builtin_amdgcn_perm(hi_row, lo_row, 0x07060302u); }

constexpr int younger(int U, int X, int NA, int NB) { return (U + DM - 1 - X) + ((U >= 1 && X <= DM) ? NA : 0) + ((U >= 9 && X <= 8 + DM) ? NB : 0); }
#define MAPSET 0x1000, 0x800, 0x80, 0x100, 0x400,  0x40, 0x8, 0x4,  0x600, 0x10, 0x20,  0x100, 0x200, 0, 0, 0
struct Stream {
  u64 K, V; unsigned voff, cofs; unsigned mr[5];
  __device__ __forceinline__ u64 base(int round) const {
    unsigned t = cofs;
#pragma unroll
    for (int k = 0; k < 5; ++k) t ^= ((round >> k) & 1) ? mr[k] : 0u;
    return ((round < 32) ? K : V) + (u64)t;
  }
  template <int J, int SLOT> __device__ __forceinline__ void issue(int round0) const { ald<SLOT, (J & 3) * 1024>(voff, base(round0 + (J >> 2))); }
  template <int U, int NA, int NB> __device__ __forceinline__ v4u step(int round0, int mode) const {
    return astep<(U & 7), younger(U, U, NA, NB), younger(U, U + 1, NA, NB), younger(U, U + ((H3 && (U % H3) == 0) ? 1 : 2), NA, NB), younger(U, U + ((H2 && (U % H2) != 0) ? 2 : 3), NA, NB), ((U + DM) & 7), ((U + DM) & 3) * 1024>(
        voff, base(round0 + ((U + DM) >> 2)), mode);
  }
};
__device__ __forceinline__ float qk(const v4u& k, const v4u& q) { return dot2(k[3], q[3], dot2(k[2], q[2], dot2(k[1], q[1], dot2(k[0], q[0], 0.f)))); }
#define REP16(M) M(0) M(1) M(2) M(3) M(4) M(5) M(6) M(7) M(8) M(9) M(10) M(11) M(12) M(13) M(14) M(15)
#define REP15(M) M(1) M(2) M(3) M(4) M(5) M(6) M(7) M(8) M(9) M(10) M(11) M(12) M(13) M(14) M(15)
#define REP8(M) M(0) M(1) M(2) M(3) M(4) M(5) M(6) M(7)
#define BARRIER() asm volatile("s_waitcnt lgkmcnt(0)\n\ts_barrier" ::: "memory")

constexpr int WQ0 = 4, QPOST = W0 + 8 - WQ0, QGET = 10, XSTEP = 8;
static_assert(QGET > QPOST && QGET < 16 - W0 && XSTEP + WQ0 >= 8 && XSTEP + W0 < 16, "tile-0 schedule");
constexpr int t0_ext(int m) { return m < 0 ? 2 * WQ0 : (m + WQ0 < 8 ? 2 : (m == XSTEP ? 4 : 0)); }
constexpr int t0_wait(int n) { int c = W0 - 1; for (int m = (n - W0 < -1 ? -1 : n - W0); m <= n - 1; ++m) c += t0_ext(m); return c; }

__device__ __forceinline__ float fin_score(float d) { return rbf(rbf(d) * 0.08838834764831845f); }
constexpr float LOG2E = 1.4426950408889634f;

__global__ void __launch_bounds__(WAVES * 64, 2) fused_decode(const void* xp, const void* Kp, const void* wqp, const void* Vp, const void* wgp,
                                                             const void* wup, const void* wdp, const void* wop, void* wsp, void* outp) {
  __shared__ __attribute__((aligned(16))) float lds[8 * 128 + 8 * 16 + 8 * 68 + 8 * 32 + 4];
  float* Lq = lds;
  float* Lg = lds + 1024;
  unsigned* Lo = (unsigned*)(lds + 1024 + 128);
  float* Lr = lds + 1024 + 128 + 8 * 68;
  volatile LAS unsigned* Lcnt = (volatile LAS unsigned*)(Lr + 256);
  asm volatile("" :: "s"(wsp), "s"(outp), "s"(xp));
  STAMP(0)
  const int tid = threadIdx.x, lane = tid & 63, w = __builtin_amdgcn_readfirstlane(tid >> 6), r = lane >> 4, c = lane & 15;
  const int b = blockIdx.x, xcd = b & 7, i8 = b >> 3;
  const int head = xcd * 4 + (i8 & 3), split = i8 >> 2, chunk = head * 8 + split;
  const bool has_mlp = i8 < 28;
  const int j = xcd * 28 + (has_mlp ? i8 : 27);
  const u64 ws = (u64)wsp;
  const u64 mlb = ws + WS_ML + head * 512;

  Stream s;
  {
    constexpr unsigned mp[16] = {MAPSET};
    unsigned t = 0;
#pragma unroll
    for (int k = 0; k < 3; ++k) t ^= (((w >> k) & 1) ? mp[5 + k] : 0u) ^ (((split >> k) & 1) ? mp[8 + k] : 0u);
#pragma unroll
    for (int k = 0; k < 5; ++k) { t ^= ((head >> k) & 1) ? mp[11 + k] : 0u; s.mr[k] = mp[k] << 10; }
    s.cofs = t << 10; s.K = (u64)Kp + ((u64)head << 23); s.V = (u64)Vp + ((u64)head << 23); s.voff = lane * 16;
  }
  v4u x8; unsigned xs; v4u wq[16];
  const unsigned wq_vo = r * 8192 + c * 16; const u64 wq_sb = (u64)wqp + (u64)w * (64 * 8192) + head * 256;
#define WQ_PAIR(P) { ld128w(wq[2 * (P)], wq_vo, wq_sb + (u64)(2 * (P)) * (4 * 8192)); ld128w(wq[2 * (P) + 1], wq_vo, wq_sb + (u64)(2 * (P) + 1) * (4 * 8192)); }
  ld128w(x8, lane * 16, (u64)xp);
  ld16(xs, (4 * c + r) * 2, (u64)xp + w * 128);
  s.issue<0, 0>(0); s.issue<1, 1>(0); s.issue<2, 2>(0);
  __builtin_amdgcn_sched_barrier(0);
  const u64 tstart = __builtin_amdgcn_s_memrealtime();
  if (W0 > 3) s.issue<3, 3>(0); if (W0 > 4) s.issue<4, 4>(0);
  WQ_PAIR(0) WQ_PAIR(1) WQ_PAIR(2) WQ_PAIR(3)
  if (tid == 0) { Lcnt[0] = 0u; Lcnt[1] = 0u; Lcnt[2] = (unsigned)tstart + SCH_T0; Lcnt[3] = 0u; }
  BARRIER();
  const int mg = lane >> 4, mn = lane & 15, cg = mn >> 1, rowsel = lane & 1;
  const unsigned gu_vo = (4 * c + r) * 3584;
  const u64 g_sb = (u64)wgp + (u64)w * (64 * 3584) + j * 16, u_sb = (u64)wup + (u64)w * (64 * 3584) + j * 16;
  const u64 wo_sb = (u64)wop + (u64)head * (128 * 1024) + w * 128;
  unsigned tch[8] = {0, 0, 0, 0, 0, 0, 0, 0};
  if (TOUCH & 1) {
#define T(I, A) asm volatile("s_load_dword %0, %1, 0x0" : "=s"(tch[I]) : "s"((u64)(A) & ~3ull) : "memory");
    T(0, g_sb) T(1, u_sb) T(2, (u64)wdp + (u64)j * 8192) T(3, ws + 3072) T(4, wo_sb) T(5, s.base(32)) T(6, (u64)outp) T(7, g_sb + 63 * 3584)
#undef T
  }
  float xn; unsigned xpair;
  float qa[8] = {0.f, 0.f, 0.f, 0.f, 0.f, 0.f, 0.f, 0.f};
  v4u q;
  float sc[8];
  float d[16];
#define PRE(N) apre<t0_wait(N), ((N) + W0 < 16 ? (N) + W0 : ((N) + W0) & 7), (((N) + W0) & 3) * 1024>(s.voff, s.base(((N) + W0) >> 2));
#define QP(P) { asm volatile("" : "+v"(wq[2 * (P)]), "+v"(wq[2 * (P) + 1])); const unsigned xx = dppu<0x150 + 2 * (P)>(xpair); \
      _Pragma("unroll") for (int k = 0; k < 4; ++k) { qa[2 * k] = dot2(plo(wq[2 * (P) + 1][k], wq[2 * (P)][k]), xx, qa[2 * k]); \
        qa[2 * k + 1] = dot2(phi(wq[2 * (P) + 1][k], wq[2 * (P)][k]), xx, qa[2 * k + 1]); } }
#define KQ(U) d[U] = qk(aread<U>(), q);
#define M(N) PRE(N) \
    if ((N) + WQ0 < 8) WQ_PAIR(((N) + WQ0) & 7) \
    if ((N) == XSTEP) { ald_gu(gu_vo, g_sb, u_sb, 0); ald64sc1<108>(0u, ws + WS_EPOCH); \
                        aldw<24, 0>((lane >> 3) * 1024 + (lane & 7) * 16, (u64)wdp + (u64)j * 8192 + w * 128); } \
    if ((N) == 0) { \
        \
      asm volatile("" : "+v"(x8), "+v"(xs)); \
      float xv[8]; \
      _Pragma("unroll") for (int k = 0; k < 4; ++k) { xv[2 * k] = lo16(x8[k]); xv[2 * k + 1] = hi16(x8[k]); } \
      float sum = 0.f; \
      _Pragma("unroll") for (int k = 0; k < 8; ++k) sum += xv[k]; \
      const float mean = wave_sum(sum) * (1.f / 512.f); \
      float sq = 0.f; \
      _Pragma("unroll") for (int k = 0; k < 8; ++k) { const float dlt = xv[k] - mean; sq = fmaf(dlt, dlt, sq); } \
      const float var = wave_sum(sq) * (1.f / 512.f); \
      xn = rbf(rbf(lo16(xs) - rbf(mean)) / rbf(sqrtf(rbf(rbf(var) + 1e-5f)))); \
      const unsigned xb = __float_as_uint(xn); \
      xpair = phi(dppu<XOR1>(xb), xb);     \
      STAMP(1) \
    } \
    if ((N) == W0) { QP(0) QP(1) QP(2) QP(3) } \
    if ((N) > W0 && (N) - W0 - 1 + WQ0 < 8) QP(((N) - W0 - 1 + WQ0) & 7) \
    if ((N) == QPOST) { \
      const float b0 = pair32(qa[0], qa[4]), b1 = pair32(qa[1], qa[5]), b2 = pair32(qa[2], qa[6]), b3 = pair32(qa[3], qa[7]); \
      const float c0 = pair16(b0, b2), c1 = pair16(b1, b3); \
      *(f32x2*)(Lq + w * 128 + 8 * c + 2 * r) = f32x2{c0, c1};     \
      asm volatile("s_waitcnt lgkmcnt(0)" : "+s"(tch[0]), "+s"(tch[1]), "+s"(tch[2]), "+s"(tch[3]), "+s"(tch[4]), "+s"(tch[5]), "+s"(tch[6]), "+s"(tch[7]) :: "memory"); \
      if (lane == 0) __hip_atomic_fetch_add((LAS unsigned*)(Lcnt + 1), 1u, __ATOMIC_RELAXED, __HIP_MEMORY_SCOPE_WORKGROUP); \
    } \
    if ((N) == QGET) { \
      for (unsigned spins = 0; Lcnt[1] != 8u && spins < SPIN_CAP; ++spins) __builtin_amdgcn_s_sleep(1); \
      asm volatile("" ::: "memory"); \
      f32x4 a0 = *(const f32x4*)(Lq + (2 * r) * 128 + 8 * c), a1 = *(const f32x4*)(Lq + (2 * r) * 128 + 8 * c + 4); \
      f32x4 b0 = *(const f32x4*)(Lq + (2 * r + 1) * 128 + 8 * c), b1 = *(const f32x4*)(Lq + (2 * r + 1) * 128 + 8 * c + 4); \
      a0 += b0; a1 += b1; \
      float v[8] = {a0[0], a0[1], a0[2], a0[3], a1[0], a1[1], a1[2], a1[3]}; \
      _Pragma("unroll") for (int k = 0; k < 8; ++k) { v[k] = pair16(v[k], v[k]); v[k] = pair32(v[k], v[k]); } \
      _Pragma("unroll") for (int k = 0; k < 4; ++k) q[k] = cvtpk(v[2 * k], v[2 * k + 1]); \
      STAMP(2) \
      KQ(0) KQ(1) KQ(2) KQ(3) \
    } \
    if ((N) == QGET + 1) { KQ(4) KQ(5) KQ(6) KQ(7) KQ(8) KQ(9) KQ(10) } \
    if ((N) > QGET) KQ(N) \
    __builtin_amdgcn_sched_barrier(0);
  REP16(M)
#undef M
  sc[7] = fin_score(treduce16(d, c));
  if (W0 < 5) { PRE(16) } if (W0 < 4) { PRE(17) }
  unsigned tag_lo, tag_hi;
  {
    const v4u e = aread<27>();
    const u64 tag = (((u64)(unsigned)__builtin_amdgcn_readfirstlane(e[1]) << 32) | (u64)(unsigned)__builtin_amdgcn_readfirstlane(e[0])) + 1ull;
    tag_lo = (unsigned)tag; tag_hi = (unsigned)(tag >> 32);
    asm volatile("v_accvgpr_write_b32 a114, 1" ::: ACLOB);
  }
  const bool clearer = !has_mlp && w == 0;
  if (clearer && !(ABL & 4)) {
    const v4u sv = {SENT, SENT, SENT, SENT};
    for (int i = 0; i < 16; ++i) st128sc1(lane * 16 + i * 1024, ws + WS_Y + head * 16384, sv);
    if (lane < 32) st128sc1(lane * 16, mlb, sv);
    if (lane < 16) st32sc1(lane * 1024 + xcd * 128 + (head & 3) * 4, ws + WS_CNT, SENT);
  }
  const unsigned fl_vo = ((lane >> 2) & 7) * 128 + (lane & 3) * 8;
  const unsigned wo_vo = (64 * rowsel + 8 * mg) * 1024 + cg * 16;
  constexpr int MODE0 = FB_T1;
  int mode = MODE0;
#define NOMINAL(TILE) ((TILE) <= 8 ? (TILE) * SCH_K : 8 * SCH_K + ((TILE) - 8) * SCH_V)
#define ANCHOR_LD(TILE) if (ANCH && w == 0 && ((ANCH_MASK >> (TILE)) & 1)) aanchor(((lane >> 2) & 7) * 128 + (lane & 3) * 4 + ((TILE) - 2) * 1024, ws + WS_CNT); else adummy(0u, (u64)xp);
#define FEEDBACK(TILE) if (FB) { const unsigned now_ = (unsigned)__builtin_amdgcn_s_memrealtime(); \
    if (ANCH && w == 0) { \
      if (clearer && ((ANCH_MASK >> ((TILE) + 2)) & 1) && lane == 0) st32sc1((TILE) * 1024 + xcd * 128 + (head & 3) * 4, ws + WS_CNT, now_ & 0xfffffffeu); \
      if ((ANCH_MASK >> (TILE)) & 1) { const unsigned a_ = aread_ticket(); const bool ok_ = a_ != SENT; \
        const float sum_ = wave_sum(ok_ ? (float)(int)(a_ - now_) : 0.f); const int n_ = __popcll(__ballot(ok_)); \
        if (n_ >= 32) Lcnt[2] = now_ + (unsigned)((int)(sum_ / (float)n_) - NOMINAL((TILE) - 2) + ANCH_OFF); } } \
    mode = (int)(now_ - (unsigned)__builtin_amdgcn_readfirstlane((int)Lcnt[2])) - NOMINAL(TILE); \
    if (ANCH && (TILE) < 4 && mode < FB_T1) mode = FB_T1; }
#define FEEDBACK2(TILE) if (FB) { const unsigned now_ = (unsigned)__builtin_amdgcn_s_memrealtime(); \
    mode = (int)(now_ - (unsigned)__builtin_amdgcn_readfirstlane((int)Lcnt[2])) - NOMINAL(TILE) - ((TILE) < 8 ? SCH_K / 2 : SCH_V / 2); \
    if (ANCH && (TILE) < 4 && mode < FB_T1) mode = FB_T1; }
#define SETPRIO(TILE) if (PRIO == 2) { if (((w >> 2) ^ (TILE)) & 1) __builtin_amdgcn_s_setprio(1); else __builtin_amdgcn_s_setprio(0); }
  for (int tile = 1; tile < 8; ++tile) {
    SETPRIO(tile)
    d[0] = qk(s.step<0, 3, 2>(tile * 4, mode), q);
    switch (tile) {
      case 1: aldw<8, 0>(wo_vo, wo_sb); aldw<9, 1024>(wo_vo, wo_sb); aldw<10, 2048>(wo_vo, wo_sb); break;
      case 2: aldw<11, 3072>(wo_vo, wo_sb); aldw<12, 0>(wo_vo, wo_sb + 4096); aldw<13, 1024>(wo_vo, wo_sb + 4096); break;
      case 3: aldw<14, 2048>(wo_vo, wo_sb + 4096); aldw<15, 3072>(wo_vo, wo_sb + 4096); if (w == 0) ald64sc1<110>(fl_vo, ws + WS_FLAG); else adummy(0u, (u64)xp); break;
      case 4: aldw<16, 0>(wo_vo, wo_sb + 32768); aldw<17, 1024>(wo_vo, wo_sb + 32768); ANCHOR_LD(tile) break;
      case 5: aldw<18, 2048>(wo_vo, wo_sb + 32768); aldw<19, 3072>(wo_vo, wo_sb + 32768); ANCHOR_LD(tile) break;
      case 6: aldw<20, 0>(wo_vo, wo_sb + 36864); aldw<21, 1024>(wo_vo, wo_sb + 36864); ANCHOR_LD(tile) break;
      default: aldw<22, 2048>(wo_vo, wo_sb + 36864); aldw<23, 3072>(wo_vo, wo_sb + 36864); ANCHOR_LD(tile) break;
    }
#define M(U) d[U] = qk(s.step<U, 3, 2>(tile * 4, mode), q); \
    if (U == 7) { \
      { FEEDBACK(tile) } \
        \
      if (tile == 1 && clearer && lane == 0) st64sc1(0u, ws + WS_FLAG + xcd * 128 + (head & 3) * 8, tag_lo, tag_hi); \
    } \
    if (U == 15) { FEEDBACK2(tile) } \
    if (U == 8) ald_gu(gu_vo, g_sb, u_sb, tile);
    REP15(M)
#undef M
#pragma unroll
    for (int k = 0; k < 7; ++k) sc[k] = sc[k + 1];
    sc[7] = fin_score(treduce16(d, c));
    if (tile == 1) { STAMP(6) }
    if (tile == 3 && w == 0) {
      const v4u e = aread<27>();
      if (!__all(e[2] == tag_lo && e[3] == tag_hi)) {
        for (unsigned spins = 0; spins < SPIN_CAP; ++spins) {
          __builtin_amdgcn_s_sleep(2);
          const v2u f = ld64sc1_wait(fl_vo, ws + WS_FLAG);
          if (__all(f[0] == tag_lo && f[1] == tag_hi)) break;
        }
      }
      Lcnt[3] = 1u;
    }
  }
  STAMP(3)
  float mw;
  {
    float m = sc[0];
#pragma unroll
    for (int k = 1; k < 8; ++k) m = fmaxf(m, sc[k]);
    mw = wave_max(m);
    float l = 0.f;
#pragma unroll
    for (int k = 0; k < 8; ++k) { sc[k] = __builtin_amdgcn_exp2f((sc[k] - mw) * LOG2E); l += sc[k]; }
    const float lw = wave_sum(l);
    for (unsigned spins = 0; Lcnt[3] == 0u && spins < SPIN_CAP; ++spins) __builtin_amdgcn_s_sleep(1);
    asm volatile("" ::: "memory");
    if (!(ABL & 1) && lane == 0) st64sc1(0u, mlb + (split * 8 + w) * 8, __float_as_uint(mw) & 0xfffffffeu, __float_as_uint(lw) & 0xfffffffeu);
  }
  float acc[8] = {0.f, 0.f, 0.f, 0.f, 0.f, 0.f, 0.f, 0.f};
  for (int tile = 8; tile < 15; ++tile) {
    SETPRIO(tile)
    const float p = sc[0];
#define M(U) fma8(acc, bcast<U>(p), s.step<U, 1, 0>(tile * 4, mode)); \
    if (U == 0) { ANCHOR_LD(tile) if (!(ABL & 1) && tile == 14) ald64sc1<112>(lane * 8, mlb); } \
    if (U == 2 && tile == 8) { \
      const v4u wg = aread<25>(), wu = aread<26>(); \
      float g[16]; \
      _Pragma("unroll") for (int k = 0; k < 4; ++k) { g[2 * k] = xn * lo16(wg[k]); g[2 * k + 1] = xn * hi16(wg[k]); g[8 + 2 * k] = xn * lo16(wu[k]); g[8 + 2 * k + 1] = xn * hi16(wu[k]); } \
      float t = treduce16(g, c); \
      t = pair16(t, t); t = pair32(t, t); \
      if (r == 0) Lg[w * 16 + c] = t; \
      asm volatile("s_waitcnt lgkmcnt(0)" ::: "memory"); \
      if (lane == 0) __hip_atomic_fetch_add((LAS unsigned*)Lcnt, 1u, __ATOMIC_RELAXED, __HIP_MEMORY_SCOPE_WORKGROUP); \
    } \
    if (U == 7) { FEEDBACK(tile) } \
    if (U == 15) { FEEDBACK2(tile) }
    REP16(M)
#undef M
#pragma unroll
    for (int k = 0; k < 7; ++k) sc[k] = sc[k + 1];
  }
  v4u bf[16];
  float scale, ymlp[4];
  {
    const float p = sc[0];
    if (LT_PRIO >= 0) { SETPRIO(15) }
#define LISSUE(J) { if ((J) >= LT_END) s.issue<J, (J) & 7>(60); else awin<(J) & 7, ((J) & 3) * 1024>(s.voff, s.base(60 + ((J) >> 2)), mode); }
    LISSUE(5)
    {
      const v4u e = aread<28>();
      unsigned mb = e[0], lb = e[1];
      if (!(ABL & 1) && !__all(mb != SENT && lb != SENT)) {
        for (unsigned spins = 0; spins < SPIN_CAP; ++spins) {
          __builtin_amdgcn_s_sleep(2);
          const v2u t = ld64sc1_wait(lane * 8, mlb);
          mb = t[0]; lb = t[1];
          if (__all(mb != SENT && lb != SENT)) break;
        }
      }
      const float mi = __uint_as_float(mb), li = __uint_as_float(lb);
      const float M = wave_max(mi);
      const float L = wave_sum(li * __builtin_amdgcn_exp2f((mi - M) * LOG2E));
      scale = __builtin_amdgcn_exp2f((mw - M) * LOG2E) / L;
    }
    LISSUE(6)
    if (LT_PRIO == 100) { SETPRIO(14) }
#define M(T) { const v4u A = aread<8 + 2 * T>(), B = aread<9 + 2 * T>(); _Pragma("unroll") for (int k = 0; k < 4; ++k) { \
      bf[(T >> 2) * 8 + 2 * k][T & 3] = plo(B[k], A[k]); bf[(T >> 2) * 8 + 2 * k + 1][T & 3] = phi(B[k], A[k]); } }
    REP8(M)
#undef M
    LISSUE(7)
    if (LT_PRIO == 101) { SETPRIO(14) }
    {
      for (unsigned spins = 0; Lcnt[0] != 8u && spins < SPIN_CAP; ++spins) __builtin_amdgcn_s_sleep(1);
      asm volatile("" ::: "memory");
      const int f = lane >> 3;
      float g = 0.f, u = 0.f;
#pragma unroll
      for (int ww = 0; ww < 8; ++ww) { g += Lg[ww * 16 + f]; u += Lg[ww * 16 + 8 + f]; }
      g = rbf(g); u = rbf(u);
      float hval = rbf(rbf(1.f / (1.f + __expf(-g))) * u);
      if (!has_mlp) hval = 0.f;
      const v4u wd = aread<24>();
      float ym[8];
#pragma unroll
      for (int k = 0; k < 4; ++k) { ym[2 * k] = hval * lo16(wd[k]); ym[2 * k + 1] = hval * hi16(wd[k]); }
#pragma unroll
      for (int k = 0; k < 4; ++k) { float t = pair32(ym[k], ym[k + 4]); t = pair16(t, t); t += dpp<XOR8>(t);
        ymlp[k] = __builtin_bit_cast(float, __builtin_amdgcn_ds_bpermute((rowsel * 32 + cg) * 4, __builtin_bit_cast(int, t)));
        asm volatile("" : "+v"(ymlp[k])); }
    }
#define M(U) { const v4u v_ = aget<U, 7>(); LISSUE(8 + U) fma8(acc, bcast<U>(p), v_); if (U == LT_PRIO) { SETPRIO(14) } }
    REP8(M)
#undef M
#define M(U) fma8(acc, bcast<8 + U>(p), aget<U, 7 - U>());
    REP8(M)
#undef M
  }
  STAMP(4)
  {
    const float b0 = pair32(acc[0], acc[4]), b1 = pair32(acc[1], acc[5]), b2 = pair32(acc[2], acc[6]), b3 = pair32(acc[3], acc[7]);
    const float c0 = pair16(b0, b2), c1 = pair16(b1, b3);
    Lo[w * 68 + 4 * c + r] = cvtpk(c0 * scale, c1 * scale);
  }
  BARRIER();
  {
    const unsigned* ap = Lo + (mn & 7) * 68 + 32 * (mn >> 3) + 4 * mg;
    const v4u a0 = *(const v4u*)ap, a1 = *(const v4u*)(ap + 16);
    f32x4 dd[8];
#define MF0(D, B) "v_mfma_f32_16x16x32_bf16 %" #D ", %8, %" #B ", 0\n\t"
#define MF1(D, B) "v_mfma_f32_16x16x32_bf16 %" #D ", %9, %" #B ", %" #D "\n\t"
    asm volatile("s_nop 1\n\t" MF0(0, 10) MF0(1, 11) MF0(2, 12) MF0(3, 13) MF0(4, 14) MF0(5, 15) MF0(6, 16) MF0(7, 17)
                 MF1(0, 18) MF1(1, 19) MF1(2, 20) MF1(3, 21) MF1(4, 22) MF1(5, 23) MF1(6, 24) MF1(7, 25) "s_nop 7"
                 : "=&v"(dd[0]), "=&v"(dd[1]), "=&v"(dd[2]), "=&v"(dd[3]), "=&v"(dd[4]), "=&v"(dd[5]), "=&v"(dd[6]), "=&v"(dd[7])
                 : "v"(a0), "v"(a1), "v"(bf[0]), "v"(bf[1]), "v"(bf[2]), "v"(bf[3]), "v"(bf[4]), "v"(bf[5]), "v"(bf[6]), "v"(bf[7]),
                   "v"(bf[8]), "v"(bf[9]), "v"(bf[10]), "v"(bf[11]), "v"(bf[12]), "v"(bf[13]), "v"(bf[14]), "v"(bf[15]));
    v4u o;
#pragma unroll
    for (int k = 0; k < 4; ++k) {
      const float t0 = (dd[k][0] + dd[k][1]) + (dd[k][2] + dd[k][3]), t1 = (dd[k + 4][0] + dd[k + 4][1]) + (dd[k + 4][2] + dd[k + 4][3]);
      auto sw = __builtin_amdgcn_permlane32_swap(__float_as_uint(t0), __float_as_uint(t1), false, false);
      float v = __uint_as_float(rowsel ? sw[1] : sw[0]);
      v += dpp<XOR1>(v);
      v = pair16(v, v);
      auto sv = __builtin_amdgcn_permlane32_swap(__float_as_uint(v), __float_as_uint(v), false, false);
      o[k] = __float_as_uint(__uint_as_float(rowsel ? sv[1] : sv[0]) + ymlp[k]) & 0xfffffffeu;
    }
    if (lane < 16) st128sc1((2 * w + (lane >> 3)) * 32768 + chunk * 128 + (lane & 7) * 16, ws + WS_Y, o);
  }
  STAMP(5)
  if (!has_mlp && (head & 1) == 0) {
    unsigned xv;
    ld16(xv, (lane & 31) * 2, (u64)xp + head * 32);
    if (TOUCH & 2) adummy(0u, (u64)outp + head * 32);
    const u64 yb = ws + WS_Y + head * 16384 + w * 4096;
    v4u y0, y1, y2, y3;
    for (unsigned spins = 0; spins < SPIN_CAP; ++spins) {
      asm volatile("global_load_dwordx4 %0, %5, %6 sc1\n\tglobal_load_dwordx4 %1, %5, %6 offset:1024 sc1\n\t"
                   "global_load_dwordx4 %2, %5, %6 offset:2048 sc1\n\tglobal_load_dwordx4 %3, %5, %6 offset:3072 sc1\n\ts_waitcnt vmcnt(0)"
                   : "=&v"(y0), "=&v"(y1), "=&v"(y2), "=&v"(y3), "+v"(xv) : "v"(lane * 16), "s"(yb) : "memory");
      const v4u t = (y0 | y1) | (y2 | y3);
      if (__all((((t[0] | t[1]) | (t[2] | t[3])) & 1u) == 0u)) break;
      if (POLL_SLEEP) __builtin_amdgcn_s_sleep(POLL_SLEEP);
    }
    float v[4];
#pragma unroll
    for (int k = 0; k < 4; ++k) v[k] = (__uint_as_float(y0[k]) + __uint_as_float(y1[k])) + (__uint_as_float(y2[k]) + __uint_as_float(y3[k]));
    float t = pair16(pair32(v[0], v[2]), pair32(v[1], v[3]));
    t += dpp<XOR8>(t);
    if ((lane & 8) == 0) Lr[w * 32 + 4 * (lane & 7) + (lane >> 4)] = t;
    BARRIER();
    if (w == 0 && lane < 32) {
      float sum = 0.f;
#pragma unroll
      for (int ww = 0; ww < 8; ++ww) sum += Lr[ww * 32 + lane];
      const float o = lo16(xv) + sum;
      st16(lane * 2, (u64)outp + head * 32, cvtpk(o, o));
      if (head == 0 && lane == 0) st64sc1(0u, ws + WS_EPOCH, tag_lo, tag_hi);
    }
  }
}

extern "C" void kernel_launch(void* const* d_in, const int* in_sizes, int n_in, void* d_out, int out_size, void* d_ws, size_t ws_size,
                              hipStream_t stream) {
  fused_decode<<<NWG, WAVES * 64, 0, stream>>>(d_in[0], d_in[1], d_in[3], d_in[2], d_in[6], d_in[7], d_in[8], d_in[4], d_ws, d_out);
}
```

```cpp
#include <hip/hip_runtime.h>
#include <hip/hip_bf16.h>

typedef unsigned v4u __attribute__((ext_vector_type(4)));
typedef unsigned v2u __attribute__((ext_vector_type(2)));
typedef float f32x4 __attribute__((ext_vector_type(4)));
typedef float f32x2 __attribute__((ext_vector_type(2)));
typedef __bf16 bf16x2 __attribute__((ext_vector_type(2)));
typedef unsigned long long u64;
#define LAS __attribute__((address_space(3)))
#define STAMP(k)

constexpr int NWG = 256, WAVES = 8;
constexpr int W0 = 3;
constexpr int DM = 5;
constexpr int FB_T0 = -100, FB_T1 = 0, FB_T2 = 80, SCH_T0 = 600, SCH_K = 450, SCH_V = 426, ANCH_OFF = 65;
constexpr int ANCH_MASK = 0x55f0;
constexpr int LT_A = 30, LT_B = -20, LT_WA = 4, LT_WB = 3, LT_END = 13;
constexpr unsigned WS_EPOCH = 0, WS_FLAG = 1024, WS_ML = 4096, WS_CNT = 32768, WS_Y = 65536;
constexpr unsigned SENT = 0xffffffffu;
constexpr unsigned SPIN_CAP = 1u << 20, LSPIN_CAP = 1u << 26;
#define WPOL ""
#define KVPOL "nt"

__device__ __forceinline__ void ld128w(v4u& dst, unsigned voff, u64 sbase) {
  asm volatile("global_load_dwordx4 %0, %1, %2 " WPOL : "=v"(dst) : "v"(voff), "s"(sbase) : "memory");
}
__device__ __forceinline__ void ld16(unsigned& dst, unsigned voff, u64 sbase) {
  asm volatile("global_load_ushort %0, %1, %2" : "=v"(dst) : "v"(voff), "s"(sbase) : "memory");
}

#define ACLOB "a0","a1","a2","a3","a4","a5","a6","a7","a8","a9","a10","a11","a12","a13","a14","a15","a16","a17","a18","a19","a20","a21","a22","a23","a24","a25","a26","a27","a28","a29","a30","a31", \
  "a32","a33","a34","a35","a36","a37","a38","a39","a40","a41","a42","a43","a44","a45","a46","a47","a48","a49","a50","a51","a52","a53","a54","a55","a56","a57","a58","a59","a60","a61","a62","a63", \
  "a64","a65","a66","a67","a68","a69","a70","a71","a72","a73","a74","a75","a76","a77","a78","a79","a80","a81","a82","a83","a84","a85","a86","a87","a88","a89","a90","a91","a92","a93","a94","a95", \
  "a96","a97","a98","a99","a100","a101","a102","a103","a104","a105","a106","a107","a108","a109","a110","a111","a112","a113","a114","a115"
template <int SLOT, int OFF> __device__ __forceinline__ void ald(unsigned voff, u64 sbase) {
  asm volatile("global_load_dwordx4 a[%c2:%c3], %0, %1 offset:%c4 " KVPOL :: "v"(voff), "s"(sbase), "i"(4 * SLOT), "i"(4 * SLOT + 3), "i"(OFF) : "memory", ACLOB);
}
template <int SLOT, int OFF> __device__ __forceinline__ void aldw(unsigned voff, u64 sbase) {
  asm volatile("global_load_dwordx4 a[%c2:%c3], %0, %1 offset:%c4 " WPOL :: "v"(voff), "s"(sbase), "i"(4 * SLOT), "i"(4 * SLOT + 3), "i"(OFF) : "memory", ACLOB);
}
__device__ __forceinline__ void ald_gu(unsigned voff, u64 gbase, u64 ubase, int idx) {
  const u64 mask = 0xffull << (8 * idx);
  asm volatile("s_mov_b64 exec, %3\n\tglobal_load_dwordx4 a[100:103], %0, %1 " WPOL "\n\tglobal_load_dwordx4 a[104:107], %0, %2 " WPOL "\n\ts_mov_b64 exec, -1"
               :: "v"(voff), "s"(gbase), "s"(ubase), "s"(mask) : "memory", ACLOB);
}
template <int REG> __device__ __forceinline__ void ald64sc1(unsigned voff, u64 sbase) {
  asm volatile("global_load_dwordx2 a[%c2:%c3], %0, %1 sc1" :: "v"(voff), "s"(sbase), "i"(REG), "i"(REG + 1) : "memory", ACLOB);
}
__device__ __forceinline__ void aanchor(unsigned voff, u64 sbase) {
  asm volatile("global_load_dword a115, %0, %1 sc1" :: "v"(voff), "s"(sbase) : "memory", ACLOB);
}
__device__ __forceinline__ void adummy(unsigned zero, u64 sbase) {
  asm volatile("s_mov_b64 exec, 1\n\tglobal_load_dword a114, %0, %1\n\ts_mov_b64 exec, -1" :: "v"(zero), "s"(sbase) : "memory", ACLOB);
}
__device__ __forceinline__ void st32sc1(unsigned voff, u64 sbase, unsigned v) {
  asm volatile("global_store_dword %0, %1, %2 sc1" :: "v"(voff), "v"(v), "s"(sbase) : "memory");
}
template <int RSLOT, int N> __device__ __forceinline__ v4u aget(void) {
  unsigned a, b, c, d;
  asm volatile("s_waitcnt vmcnt(%c4)\n\tv_accvgpr_read_b32 %0, a%c5\n\tv_accvgpr_read_b32 %1, a%c6\n\tv_accvgpr_read_b32 %2, a%c7\n\tv_accvgpr_read_b32 %3, a%c8"
               : "=v"(a), "=v"(b), "=v"(c), "=v"(d) : "i"(N), "i"(4 * RSLOT), "i"(4 * RSLOT + 1), "i"(4 * RSLOT + 2), "i"(4 * RSLOT + 3) : "memory", ACLOB);
  return v4u{a, b, c, d};
}
template <int RSLOT, int N5, int N4, int N3, int N2, int WSLOT, int OFF> __device__ __forceinline__ v4u astep(unsigned voff, u64 sbase, int mode) {
  unsigned a, b, c, d;
  asm volatile("s_waitcnt vmcnt(%c7)\n\t"
               "s_cmp_gt_i32 %6, %c17\n\ts_cbranch_scc1 1f\n\t"
               "s_waitcnt vmcnt(%c8)\n\t"
               "s_cmp_gt_i32 %6, %c18\n\ts_cbranch_scc1 1f\n\t"
               "s_waitcnt vmcnt(%c9)\n\t"
               "s_cmp_gt_i32 %6, %c19\n\ts_cbranch_scc1 1f\n\t"
               "s_waitcnt vmcnt(%c20)\n"
               "1:\n\tglobal_load_dwordx4 a[%c14:%c15], %4, %5 offset:%c16 " KVPOL "\n\t"
               "v_accvgpr_read_b32 %0, a%c10\n\tv_accvgpr_read_b32 %1, a%c11\n\tv_accvgpr_read_b32 %2, a%c12\n\tv_accvgpr_read_b32 %3, a%c13"
               : "=&v"(a), "=&v"(b), "=&v"(c), "=&v"(d)
               : "v"(voff), "s"(sbase), "s"(mode), "i"(N5), "i"(N4), "i"(N3),
                 "i"(4 * RSLOT), "i"(4 * RSLOT + 1), "i"(4 * RSLOT + 2), "i"(4 * RSLOT + 3), "i"(4 * WSLOT), "i"(4 * WSLOT + 3), "i"(OFF),
                 "i"(FB_T2), "i"(FB_T1), "i"(FB_T0), "i"(N2)
               : "memory", "scc", ACLOB);
  return v4u{a, b, c, d};
}
template <int WSLOT, int OFF> __device__ __forceinline__ void awin(unsigned voff, u64 sbase, int mode) {
  asm volatile("s_cmp_gt_i32 %2, %c6\n\ts_cbranch_scc1 1f\n\t"
               "s_waitcnt vmcnt(%c8)\n\t"
               "s_cmp_gt_i32 %2, %c7\n\ts_cbranch_scc1 1f\n\t"
               "s_waitcnt vmcnt(%c9)\n"
               "1:\n\tglobal_load_dwordx4 a[%c3:%c4], %0, %1 offset:%c5 " KVPOL
               :: "v"(voff), "s"(sbase), "s"(mode), "i"(4 * WSLOT), "i"(4 * WSLOT + 3), "i"(OFF), "i"(LT_A), "i"(LT_B), "i"(LT_WA - 1), "i"(LT_WB - 1)
               : "memory", "scc", ACLOB);
}
template <int N, int WSLOT, int OFF> __device__ __forceinline__ void apre(unsigned voff, u64 sbase) {
  asm volatile("s_waitcnt vmcnt(%c2)\n\tglobal_load_dwordx4 a[%c3:%c4], %0, %1 offset:%c5 " KVPOL
               :: "v"(voff), "s"(sbase), "i"(N), "i"(4 * WSLOT), "i"(4 * WSLOT + 3), "i"(OFF) : "memory", ACLOB);
}
template <int RSLOT> __device__ __forceinline__ v4u aread(void) {
  unsigned a, b, c, d;
  asm volatile("v_accvgpr_read_b32 %0, a%c4\n\tv_accvgpr_read_b32 %1, a%c5\n\tv_accvgpr_read_b32 %2, a%c6\n\tv_accvgpr_read_b32 %3, a%c7\n\ts_nop 1"
               : "=v"(a), "=v"(b), "=v"(c), "=v"(d) : "i"(4 * RSLOT), "i"(4 * RSLOT + 1), "i"(4 * RSLOT + 2), "i"(4 * RSLOT + 3) : "memory", ACLOB);
  return v4u{a, b, c, d};
}
template <int SLOT> __device__ __forceinline__ void awrite(unsigned a, unsigned b, unsigned c, unsigned d) {
  asm volatile("v_accvgpr_write_b32 a%c4, %0\n\tv_accvgpr_write_b32 a%c5, %1\n\tv_accvgpr_write_b32 a%c6, %2\n\tv_accvgpr_write_b32 a%c7, %3"
               :: "v"(a), "v"(b), "v"(c), "v"(d), "i"(4 * SLOT), "i"(4 * SLOT + 1), "i"(4 * SLOT + 2), "i"(4 * SLOT + 3) : ACLOB);
}
template <int S> __device__ __forceinline__ void wo_frag(void);
__device__ __forceinline__ unsigned aread_samples(void) {
  unsigned a;
  asm volatile("v_accvgpr_read_b32 %0, a115\n\ts_nop 1" : "=v"(a) :: "memory", ACLOB);
  return a;
}
__device__ __forceinline__ void st128sc1(unsigned voff, u64 sbase, const v4u& v) {
  asm volatile("global_store_dwordx4 %0, %1, %2 sc1\n\ts_nop 1" :: "v"(voff), "v"(v), "s"(sbase) : "memory");
}
__device__ __forceinline__ void st64sc1(unsigned voff, u64 sbase, unsigned lo, unsigned hi) {
  const v2u v = {lo, hi};
  asm volatile("global_store_dwordx2 %0, %1, %2 sc1" :: "v"(voff), "v"(v), "s"(sbase) : "memory");
}
__device__ __forceinline__ void st16(unsigned voff, u64 sbase, unsigned v) {
  asm volatile("global_store_short %0, %1, %2" :: "v"(voff), "v"(v), "s"(sbase) : "memory");
}
__device__ __forceinline__ v2u ld64sc1_wait(unsigned voff, u64 sbase) {
  v2u v;
  asm volatile("global_load_dwordx2 %0, %1, %2 sc1\n\ts_waitcnt vmcnt(0)" : "=&v"(v) : "v"(voff), "s"(sbase) : "memory");
  return v;
}

__device__ __forceinline__ float dot2(unsigned a, unsigned b, float acc) {
  return __builtin_amdgcn_fdot2_f32_bf16(__builtin_bit_cast(bf16x2, a), __builtin_bit_cast(bf16x2, b), acc, false);
}
__device__ __forceinline__ float lo16(unsigned v) { return __builtin_bit_cast(float, v << 16); }
__device__ __forceinline__ float hi16(unsigned v) { return __builtin_bit_cast(float, v & 0xffff0000u); }
template <int CTRL> __device__ __forceinline__ float dpp(float x) {
  return __builtin_bit_cast(float, __builtin_amdgcn_mov_dpp(__builtin_bit_cast(int, x), CTRL, 0xf, 0xf, true));
}
template <int CTRL> __device__ __forceinline__ unsigned dppu(unsigned x) { return (unsigned)__builtin_amdgcn_mov_dpp((int)x, CTRL, 0xf, 0xf, true); }
constexpr int XOR1 = 0xB1, XOR2 = 0x4E, XOR7 = 0x141, XOR8 = 0x128, ROR4 = 0x124;
__device__ __forceinline__ unsigned cvtpk(float lo, float hi) {
  const bf16x2 v = {(__bf16)lo, (__bf16)hi};
  return __builtin_bit_cast(unsigned, v);
}
__device__ __forceinline__ float rbf(float x) { return lo16(cvtpk(x, x)); }
__device__ __forceinline__ float pair32(float a, float b) {
  auto s = __builtin_amdgcn_permlane32_swap(__float_as_uint(a), __float_as_uint(b), false, false);
  return __uint_as_float(s[0]) + __uint_as_float(s[1]);
}
__device__ __forceinline__ float pair16(float a, float b) {
  auto s = __builtin_amdgcn_permlane16_swap(__float_as_uint(a), __float_as_uint(b), false, false);
  return __uint_as_float(s[0]) + __uint_as_float(s[1]);
}
__device__ __forceinline__ float wave_sum(float x) {
  x += dpp<XOR1>(x); x += dpp<XOR2>(x); x += dpp<XOR7>(x); x += dpp<XOR8>(x);
  x = pair16(x, x); x = pair32(x, x); return x;
}
__device__ __forceinline__ float wave_max(float x) {
  x = fmaxf(x, dpp<XOR1>(x)); x = fmaxf(x, dpp<XOR2>(x)); x = fmaxf(x, dpp<XOR7>(x)); x = fmaxf(x, dpp<XOR8>(x));
  auto s = __builtin_amdgcn_permlane16_swap(__float_as_uint(x), __float_as_uint(x), false, false);
  x = fmaxf(__uint_as_float(s[0]), __uint_as_float(s[1]));
  auto t = __builtin_amdgcn_permlane32_swap(__float_as_uint(x), __float_as_uint(x), false, false);
  return fmaxf(__uint_as_float(t[0]), __uint_as_float(t[1]));
}
__device__ __forceinline__ float treduce16(float (&x)[16], int c) {
#define TR_STEP(HALF, CTRL, BIT) _Pragma("unroll") for (int h = 0; h < (HALF); ++h) { \
    const float lo_ = x[h], hi_ = x[h + (HALF)]; const float t1_ = lo_ + dpp<CTRL>(lo_), t2_ = hi_ + dpp<CTRL>(hi_); x[h] = (c & (BIT)) ? t2_ : t1_; }
  TR_STEP(8, XOR8, 8) TR_STEP(4, XOR7, 4) TR_STEP(2, XOR2, 2) TR_STEP(1, XOR1, 1)
#undef TR_STEP
  return x[0];
}
template <int CTRL, int BIT> __device__ __forceinline__ float tmerge(float lo, float hi, int c) {
  const float t1 = lo + dpp<CTRL>(lo), t2 = hi + dpp<CTRL>(hi); return (c & BIT) ? t2 : t1;
}
template <int U> __device__ __forceinline__ void tfeed(float (&x)[16], int c) {
  if constexpr ((U & 1) == 1) x[U - 1] = tmerge<XOR1, 1>(x[U - 1], x[U], c);
  if constexpr ((U & 3) == 3) x[U - 3] = tmerge<XOR2, 2>(x[U - 3], x[U - 1], c);
  if constexpr ((U & 7) == 7) x[U - 7] = tmerge<ROR4, 4>(x[U - 7], x[U - 3], c);
  if constexpr (U == 15) x[0] = tmerge<XOR8, 8>(x[0], x[8], c);
  constexpr int L = (U == 15) ? 0 : ((U & 7) == 7) ? U - 7 : ((U & 3) == 3) ? U - 3 : ((U & 1) == 1) ? U - 1 : U;
  asm volatile("" : "+v"(x[L]));
}
template <int T> __device__ __forceinline__ float bcast(float p) { return dpp<0x150 + T>(p); }
__device__ __forceinline__ void fma8(f32x2 (&acc)[4], float p, const v4u& v) {
  const f32x2 pp = {p, p};
#pragma unroll
  for (int k = 0; k < 4; ++k) acc[k] = __builtin_elementwise_fma(pp, f32x2{lo16(v[k]), hi16(v[k])}, acc[k]);
  asm volatile("" : "+v"(acc[0]), "+v"(acc[1]), "+v"(acc[2]), "+v"(acc[3]));
}
__device__ __forceinline__ unsigned plo(unsigned hi_row, unsigned lo_row) { return __builtin_amdgcn_perm(hi_row, lo_row, 0x05040100u); }
__device__ __forceinline__ unsigned phi(unsigned hi_row, unsigned lo_row) { return __builtin_amdgcn_perm(hi_row, lo_row, 0x07060302u); }

template <int S> __device__ __forceinline__ void wo_frag(void) {
  const v4u r0 = aread<S>(), r1 = aread<S + 1>(), r2 = aread<S + 2>(), r3 = aread<S + 3>(), r4 = aread<S + 4>(), r5 = aread<S + 5>(), r6 = aread<S + 6>(), r7 = aread<S + 7>();
#define WF(K) awrite<S + 2 * K>(plo(r1[K], r0[K]), plo(r3[K], r2[K]), plo(r5[K], r4[K]), plo(r7[K], r6[K])); \
              awrite<S + 2 * K + 1>(phi(r1[K], r0[K]), phi(r3[K], r2[K]), phi(r5[K], r4[K]), phi(r7[K], r6[K]));
  WF(0) WF(1) WF(2) WF(3)
#undef WF
}

constexpr int younger(int U, int X, int NA, int NB) { return (U + DM - 1 - X) + ((U >= 1 && X <= DM) ? NA : 0) + ((U >= 9 && X <= 8 + DM) ? NB : 0); }
#define MAPSET 0x1000, 0x800, 0x80, 0x100, 0x400,  0x40, 0x8, 0x4,  0x600, 0x10, 0x20,  0x100, 0x200, 0, 0, 0
struct Stream {
  u64 K, V; unsigned voff, cofs; unsigned mr[5];
  __device__ __forceinline__ u64 base(int round) const {
    unsigned t = cofs;
#pragma unroll
    for (int k = 0; k < 5; ++k) t ^= ((round >> k) & 1) ? mr[k] : 0u;
    return ((round < 32) ? K : V) + (u64)t;
  }
  template <int J, int SLOT> __device__ __forceinline__ void issue(int round0) const { ald<SLOT, (J & 3) * 1024>(voff, base(round0 + (J >> 2))); }
  template <int U, int NA, int NB> __device__ __forceinline__ v4u step(int round0, int mode) const {
    return astep<(U & 7), younger(U, U, NA, NB), younger(U, U + 1, NA, NB), younger(U, U + 2, NA, NB), younger(U, U + 3, NA, NB),
                 ((U + DM) & 7), ((U + DM) & 3) * 1024>(
        voff, base(round0 + ((U + DM) >> 2)), mode);
  }
};
__device__ __forceinline__ float qk(const v4u& k, const v4u& q) { return dot2(k[3], q[3], dot2(k[2], q[2], dot2(k[1], q[1], dot2(k[0], q[0], 0.f)))); }
#define REP16(M) M(0) M(1) M(2) M(3) M(4) M(5) M(6) M(7) M(8) M(9) M(10) M(11) M(12) M(13) M(14) M(15)
#define REP15(M) M(1) M(2) M(3) M(4) M(5) M(6) M(7) M(8) M(9) M(10) M(11) M(12) M(13) M(14) M(15)
#define REP8(M) M(0) M(1) M(2) M(3) M(4) M(5) M(6) M(7)
#define BARRIER() asm volatile("s_waitcnt lgkmcnt(0)\n\ts_barrier" ::: "memory")

constexpr int WQ0 = 4, QPOST = W0 + 8 - WQ0, QGET = 10, XSTEP = 8;
static_assert(!((ANCH_MASK >> 13) & 1) && !(ANCH_MASK & 15), "tile 13 carries the (m,l) gather; samples are read behind the flags");
static_assert(QGET > QPOST && QGET < 16 - W0 && XSTEP + WQ0 >= 8 && XSTEP + W0 < 16, "tile-0 schedule");
constexpr int t0_ext(int m) { return m < 0 ? 2 * WQ0 : (m + WQ0 < 8 ? 2 : (m == XSTEP ? 4 : 0)); }
constexpr int t0_wait(int n) { int c = W0 - 1; for (int m = (n - W0 < -1 ? -1 : n - W0); m <= n - 1; ++m) c += t0_ext(m); return c; }

__device__ __forceinline__ float fin_score(float d) { return rbf(rbf(d) * 0.08838834764831845f); }
constexpr float LOG2E = 1.4426950408889634f;

__global__ void __launch_bounds__(WAVES * 64, 2) fused_decode(const void* xp, const void* Kp, const void* wqp, const void* Vp, const void* wgp,
                                                             const void* wup, const void* wdp, const void* wop, void* wsp, void* outp) {
  __shared__ __attribute__((aligned(16))) float lds[8 * 128 + 8 * 16 + 8 * 68 + 8 * 32 + 4];
  float* Lq = lds;
  float* Lg = lds + 1024;
  unsigned* Lo = (unsigned*)(lds + 1024 + 128);
  float* Lr = lds + 1024 + 128 + 8 * 68;
  volatile LAS unsigned* Lcnt = (volatile LAS unsigned*)(Lr + 256);
  {
    u64 pc; unsigned nk;
    asm volatile("s_getpc_b64 %0\n.Lfd_begin:\n\ts_mov_b32 %1, (.Lfd_end-.Lfd_begin)>>10" : "=s"(pc), "=s"(nk));
    const unsigned sl = (((blockIdx.x >> 3) * 8 + (threadIdx.x >> 6)) * nk) >> 8;
    asm volatile("global_load_dwordx4 a[112:115], %0, %1" :: "v"(sl * 1024 + (threadIdx.x & 63) * 16), "s"(pc & ~63ull) : "memory", ACLOB);
  }
  asm volatile("" :: "s"(wsp), "s"(outp), "s"(xp));
  STAMP(0)
  const int tid = threadIdx.x, lane = tid & 63, w = __builtin_amdgcn_readfirstlane(tid >> 6), r = lane >> 4, c = lane & 15;
  const int b = blockIdx.x, xcd = b & 7, i8 = b >> 3;
  const int head = xcd * 4 + (i8 & 3), split = i8 >> 2, chunk = head * 8 + split;
  const bool has_mlp = i8 < 28;
  const int j = xcd * 28 + (has_mlp ? i8 : 27);
  const u64 ws = (u64)wsp;
  const u64 mlb = ws + WS_ML + head * 512;

  Stream s;
  {
    constexpr unsigned mp[16] = {MAPSET};
    unsigned t = 0;
#pragma unroll
    for (int k = 0; k < 3; ++k) t ^= (((w >> k) & 1) ? mp[5 + k] : 0u) ^ (((split >> k) & 1) ? mp[8 + k] : 0u);
#pragma unroll
    for (int k = 0; k < 5; ++k) { t ^= ((head >> k) & 1) ? mp[11 + k] : 0u; s.mr[k] = mp[k] << 10; }
    s.cofs = t << 10; s.K = (u64)Kp + ((u64)head << 23); s.V = (u64)Vp + ((u64)head << 23); s.voff = lane * 16;
  }
  v4u x8; unsigned xs; v4u wq[16];
  const unsigned wq_vo = r * 8192 + c * 16; const u64 wq_sb = (u64)wqp + (u64)w * (64 * 8192) + head * 256;
#define WQ_PAIR(P) { ld128w(wq[2 * (P)], wq_vo, wq_sb + (u64)(2 * (P)) * (4 * 8192)); ld128w(wq[2 * (P) + 1], wq_vo, wq_sb + (u64)(2 * (P) + 1) * (4 * 8192)); }
  ld128w(x8, lane * 16, (u64)xp);
  ld16(xs, (4 * c + r) * 2, (u64)xp + w * 128);
  s.issue<0, 0>(0); s.issue<1, 1>(0); s.issue<2, 2>(0);
  __builtin_amdgcn_sched_barrier(0);
  const u64 tstart = __builtin_amdgcn_s_memrealtime();
  WQ_PAIR(0) WQ_PAIR(1) WQ_PAIR(2) WQ_PAIR(3)
  if (tid == 0) { Lcnt[0] = 0u; Lcnt[1] = 0u; Lcnt[2] = (unsigned)tstart + SCH_T0; Lcnt[3] = 0u; }
  BARRIER();
  const int mg = lane >> 4, mn = lane & 15, cg = mn >> 1, rowsel = lane & 1;
  const unsigned gu_vo = (4 * c + r) * 3584;
  const u64 g_sb = (u64)wgp + (u64)w * (64 * 3584) + j * 16, u_sb = (u64)wup + (u64)w * (64 * 3584) + j * 16;
  const u64 wo_sb = (u64)wop + (u64)head * (128 * 1024) + w * 128;
  unsigned tch[8];
  {
#define T(I, A) asm volatile("s_load_dword %0, %1, 0x0" : "=s"(tch[I]) : "s"((u64)(A) & ~3ull) : "memory");
    T(0, g_sb) T(1, u_sb) T(2, (u64)wdp + (u64)j * 8192) T(3, ws + 3072) T(4, wo_sb) T(5, s.base(32)) T(6, (u64)outp) T(7, g_sb + 63 * 3584)
#undef T
  }
  float xn; unsigned xpair;
  float qa[8] = {0.f, 0.f, 0.f, 0.f, 0.f, 0.f, 0.f, 0.f};
  v4u q;
  float sc[8];
  float d[16];
#define PRE(N) apre<t0_wait(N), ((N) + W0 < 16 ? (N) + W0 : ((N) + W0) & 7), (((N) + W0) & 3) * 1024>(s.voff, s.base(((N) + W0) >> 2));
#define QP(P) { asm volatile("" : "+v"(wq[2 * (P)]), "+v"(wq[2 * (P) + 1])); const unsigned xx = dppu<0x150 + 2 * (P)>(xpair); \
      _Pragma("unroll") for (int k = 0; k < 4; ++k) { qa[2 * k] = dot2(plo(wq[2 * (P) + 1][k], wq[2 * (P)][k]), xx, qa[2 * k]); \
        qa[2 * k + 1] = dot2(phi(wq[2 * (P) + 1][k], wq[2 * (P)][k]), xx, qa[2 * k + 1]); } }
#define KQ(U) { d[U] = qk(aread<U>(), q); tfeed<U>(d, c); }
#define M(N) PRE(N) \
    if ((N) + WQ0 < 8) WQ_PAIR(((N) + WQ0) & 7) \
    if ((N) == XSTEP) { ald_gu(gu_vo, g_sb, u_sb, 0); if (w == 0) ald64sc1<108>(0u, ws + WS_EPOCH); else adummy(0u, (u64)xp); \
                        aldw<24, 0>((lane >> 3) * 1024 + (lane & 7) * 16, (u64)wdp + (u64)j * 8192 + w * 128); } \
    if ((N) == 0) { \
        \
      asm volatile("" : "+v"(x8), "+v"(xs)); \
      float xv[8]; \
      _Pragma("unroll") for (int k = 0; k < 4; ++k) { xv[2 * k] = lo16(x8[k]); xv[2 * k + 1] = hi16(x8[k]); } \
      float sum = 0.f; \
      _Pragma("unroll") for (int k = 0; k < 8; ++k) sum += xv[k]; \
      const float mean = wave_sum(sum) * (1.f / 512.f); \
      float sq = 0.f; \
      _Pragma("unroll") for (int k = 0; k < 8; ++k) { const float dlt = xv[k] - mean; sq = fmaf(dlt, dlt, sq); } \
      const float var = wave_sum(sq) * (1.f / 512.f); \
      xn = rbf(rbf(lo16(xs) - rbf(mean)) / rbf(sqrtf(rbf(rbf(var) + 1e-5f)))); \
      const unsigned xb = __float_as_uint(xn); \
      xpair = phi(dppu<XOR1>(xb), xb);     \
      STAMP(1) \
    } \
    if ((N) == W0) { QP(0) QP(1) QP(2) QP(3) } \
    if ((N) > W0 && (N) - W0 - 1 + WQ0 < 8) QP(((N) - W0 - 1 + WQ0) & 7) \
    if ((N) == QPOST) { \
      const float b0 = pair32(qa[0], qa[4]), b1 = pair32(qa[1], qa[5]), b2 = pair32(qa[2], qa[6]), b3 = pair32(qa[3], qa[7]); \
      const float c0 = pair16(b0, b2), c1 = pair16(b1, b3); \
      *(f32x2*)(Lq + w * 128 + 8 * c + 2 * r) = f32x2{c0, c1};     \
      asm volatile("s_waitcnt lgkmcnt(0)" : "+s"(tch[0]), "+s"(tch[1]), "+s"(tch[2]), "+s"(tch[3]), "+s"(tch[4]), "+s"(tch[5]), "+s"(tch[6]), "+s"(tch[7]) :: "memory"); \
      if (lane == 0) __hip_atomic_fetch_add((LAS unsigned*)(Lcnt + 1), 1u, __ATOMIC_RELAXED, __HIP_MEMORY_SCOPE_WORKGROUP); \
    } \
    if ((N) == QGET) { \
      for (unsigned spins = 0; Lcnt[1] != 8u && spins < LSPIN_CAP; ++spins) __builtin_amdgcn_s_sleep(1); \
      asm volatile("" ::: "memory"); \
      f32x4 a0 = *(const f32x4*)(Lq + (2 * r) * 128 + 8 * c), a1 = *(const f32x4*)(Lq + (2 * r) * 128 + 8 * c + 4); \
      f32x4 b0 = *(const f32x4*)(Lq + (2 * r + 1) * 128 + 8 * c), b1 = *(const f32x4*)(Lq + (2 * r + 1) * 128 + 8 * c + 4); \
      a0 += b0; a1 += b1; \
      float v[8] = {a0[0], a0[1], a0[2], a0[3], a1[0], a1[1], a1[2], a1[3]}; \
      _Pragma("unroll") for (int k = 0; k < 8; ++k) { v[k] = pair16(v[k], v[k]); v[k] = pair32(v[k], v[k]); } \
      _Pragma("unroll") for (int k = 0; k < 4; ++k) q[k] = cvtpk(v[2 * k], v[2 * k + 1]); \
      STAMP(2) \
      KQ(0) KQ(1) KQ(2) KQ(3) \
    } \
    if ((N) == QGET + 1) { KQ(4) KQ(5) KQ(6) KQ(7) KQ(8) KQ(9) KQ(10) } \
    if ((N) > QGET) KQ(N) \
    __builtin_amdgcn_sched_barrier(0);
  REP16(M)
#undef M
  sc[7] = fin_score(d[0]);
  PRE(16) PRE(17)
  unsigned tag_lo, tag_hi;
  {
    const v4u e = aread<27>();
    const u64 tag = (((u64)(unsigned)__builtin_amdgcn_readfirstlane(e[1]) << 32) | (u64)(unsigned)__builtin_amdgcn_readfirstlane(e[0])) + 1ull;
    tag_lo = (unsigned)tag; tag_hi = (unsigned)(tag >> 32);
  }
  const bool clearer = !has_mlp && w == 0;
  if (clearer) {
    const v4u sv = {SENT, SENT, SENT, SENT};
    for (int i = 0; i < 16; ++i) st128sc1(lane * 16 + i * 1024, ws + WS_Y + head * 16384, sv);
    if (lane < 32) st128sc1(lane * 16, mlb, sv);
    if (lane < 16) st32sc1(lane * 1024 + xcd * 128 + (head & 3) * 4, ws + WS_CNT, SENT);
  }
  const unsigned fl_vo = ((lane >> 2) & 7) * 128 + (lane & 3) * 8;
  const unsigned wo_vo = (64 * rowsel + 8 * mg) * 1024 + cg * 16;
  int mode = FB_T1;
#define NOMINAL(TILE) ((TILE) <= 8 ? (TILE) * SCH_K : 8 * SCH_K + ((TILE) - 8) * SCH_V)
#define ANCHOR_LD(TILE) if (w == 0 && ((ANCH_MASK >> (TILE)) & 1)) aanchor(((lane >> 2) & 7) * 128 + (lane & 3) * 4 + ((TILE) - 2) * 1024, ws + WS_CNT); else adummy(0u, (u64)xp);
#define FEEDBACK(TILE) { const unsigned now_ = (unsigned)__builtin_amdgcn_s_memrealtime(); \
    if (w == 0) { \
      if (clearer && ((ANCH_MASK >> ((TILE) + 2)) & 1) && lane == 0) st32sc1((TILE) * 1024 + xcd * 128 + (head & 3) * 4, ws + WS_CNT, now_ & 0xfffffffeu); \
      if ((ANCH_MASK >> (TILE)) & 1) { const unsigned a_ = aread_samples(); const bool ok_ = a_ != SENT; \
        const float sum_ = wave_sum(ok_ ? (float)(int)(a_ - now_) : 0.f); const int n_ = __popcll(__ballot(ok_)); \
        if (n_ >= 32) Lcnt[2] = now_ + (unsigned)((int)(sum_ / (float)n_) - NOMINAL((TILE) - 2) + ANCH_OFF); } } \
    mode = (int)(now_ - (unsigned)__builtin_amdgcn_readfirstlane((int)Lcnt[2])) - NOMINAL(TILE); \
    if ((TILE) < 4 && mode < FB_T1) mode = FB_T1; }
#define FEEDBACK2(TILE) { const unsigned now_ = (unsigned)__builtin_amdgcn_s_memrealtime(); \
    mode = (int)(now_ - (unsigned)__builtin_amdgcn_readfirstlane((int)Lcnt[2])) - NOMINAL(TILE) - ((TILE) < 8 ? SCH_K / 2 : SCH_V / 2); \
    if ((TILE) < 4 && mode < FB_T1) mode = FB_T1; }
#define SETPRIO(TILE) { if (((w >> 2) ^ (TILE)) & 1) __builtin_amdgcn_s_setprio(1); else __builtin_amdgcn_s_setprio(0); }
#pragma nounroll
  for (int tile = 1; tile < 8; ++tile) {
    SETPRIO(tile)
    d[0] = qk(s.step<0, 3, 2>(tile * 4, mode), q); tfeed<0>(d, c);
    switch (tile) {
      case 1: aldw<8, 0>(wo_vo, wo_sb); aldw<9, 1024>(wo_vo, wo_sb); aldw<10, 2048>(wo_vo, wo_sb); break;
      case 2: aldw<11, 3072>(wo_vo, wo_sb); aldw<12, 0>(wo_vo, wo_sb + 4096); aldw<13, 1024>(wo_vo, wo_sb + 4096); break;
      case 3: aldw<14, 2048>(wo_vo, wo_sb + 4096); aldw<15, 3072>(wo_vo, wo_sb + 4096); if (w == 0) ald64sc1<110>(fl_vo, ws + WS_FLAG); else adummy(0u, (u64)xp); break;
      case 4: aldw<16, 0>(wo_vo, wo_sb + 32768); aldw<17, 1024>(wo_vo, wo_sb + 32768); ANCHOR_LD(tile) break;
      case 5: aldw<18, 2048>(wo_vo, wo_sb + 32768); aldw<19, 3072>(wo_vo, wo_sb + 32768); ANCHOR_LD(tile) break;
      case 6: aldw<20, 0>(wo_vo, wo_sb + 36864); aldw<21, 1024>(wo_vo, wo_sb + 36864); ANCHOR_LD(tile) break;
      default: aldw<22, 2048>(wo_vo, wo_sb + 36864); aldw<23, 3072>(wo_vo, wo_sb + 36864); ANCHOR_LD(tile) break;
    }
#define M(U) d[U] = qk(s.step<U, 3, 2>(tile * 4, mode), q); tfeed<U>(d, c); \
    if (U == 7) { \
      { FEEDBACK(tile) } \
        \
      if (tile == 1 && clearer && lane == 0) st64sc1(0u, ws + WS_FLAG + xcd * 128 + (head & 3) * 8, tag_lo, tag_hi); \
    } \
    if (U == 15) { FEEDBACK2(tile) } \
    if (U == 8) ald_gu(gu_vo, g_sb, u_sb, tile);
    REP15(M)
#undef M
#pragma unroll
    for (int k = 0; k < 7; ++k) sc[k] = sc[k + 1];
    sc[7] = fin_score(d[0]);
    if (tile == 1) { STAMP(6) }
    if (tile == 3 && w == 0) {
      const v4u e = aread<27>();
      if (!__all(e[2] == tag_lo && e[3] == tag_hi)) {
        for (unsigned spins = 0; spins < SPIN_CAP; ++spins) {
          __builtin_amdgcn_s_sleep(2);
          const v2u f = ld64sc1_wait(fl_vo, ws + WS_FLAG);
          if (__all(f[0] == tag_lo && f[1] == tag_hi)) break;
        }
      }
      Lcnt[3] = 1u;
    }
  }
  STAMP(3)
  float mw;
  {
    float m = sc[0];
#pragma unroll
    for (int k = 1; k < 8; ++k) m = fmaxf(m, sc[k]);
    mw = wave_max(m);
    float l = 0.f;
#pragma unroll
    for (int k = 0; k < 8; ++k) { sc[k] = __builtin_amdgcn_exp2f((sc[k] - mw) * LOG2E); l += sc[k]; }
    const float lw = wave_sum(l);
    for (unsigned spins = 0; Lcnt[3] == 0u && spins < LSPIN_CAP; ++spins) __builtin_amdgcn_s_sleep(1);
    asm volatile("" ::: "memory");
    if (lane == 0) st64sc1(0u, mlb + (split * 8 + w) * 8, __float_as_uint(mw) & 0xfffffffeu, __float_as_uint(lw) & 0xfffffffeu);
  }
  f32x2 acc[4] = {{0.f, 0.f}, {0.f, 0.f}, {0.f, 0.f}, {0.f, 0.f}};
  float scale = 0.f, ymlp[4] = {0.f, 0.f, 0.f, 0.f};
#pragma nounroll
  for (int tile = 8; tile < 15; ++tile) {
    SETPRIO(tile)
    const float p = sc[0];
#define M(U) fma8(acc, bcast<U>(p), s.step<U, 1, 0>(tile * 4, mode)); \
    if (U == 0) { if (tile == 13) ald64sc1<112>(lane * 8, mlb); else ANCHOR_LD(tile) } \
    if (U == 7) { FEEDBACK(tile) } \
    if (U == 15) { FEEDBACK2(tile) } \
    if (U == 10) { \
        \
      switch (tile) { \
        case 8: {   \
          const v4u wg = aread<25>(), wu = aread<26>(); \
          float g[16]; \
          _Pragma("unroll") for (int k = 0; k < 4; ++k) { g[2 * k] = xn * lo16(wg[k]); g[2 * k + 1] = xn * hi16(wg[k]); g[8 + 2 * k] = xn * lo16(wu[k]); g[8 + 2 * k + 1] = xn * hi16(wu[k]); } \
          float t = treduce16(g, c); \
          t = pair16(t, t); t = pair32(t, t); \
          if (r == 0) Lg[w * 16 + c] = t; \
          asm volatile("s_waitcnt lgkmcnt(0)" ::: "memory"); \
          if (lane == 0) __hip_atomic_fetch_add((LAS unsigned*)Lcnt, 1u, __ATOMIC_RELAXED, __HIP_MEMORY_SCOPE_WORKGROUP); \
        } break; \
        case 9: wo_frag<8>(); break; \
        case 10: wo_frag<16>(); break; \
        case 11: {   \
          for (unsigned spins = 0; Lcnt[0] != 8u && spins < LSPIN_CAP; ++spins) __builtin_amdgcn_s_sleep(1); \
          asm volatile("" ::: "memory"); \
          const int f = lane >> 3; \
          float g = 0.f, u = 0.f; \
          _Pragma("unroll") for (int ww = 0; ww < 8; ++ww) { g += Lg[ww * 16 + f]; u += Lg[ww * 16 + 8 + f]; } \
          g = rbf(g); u = rbf(u); \
          float hval = rbf(rbf(1.f / (1.f + __expf(-g))) * u); \
          if (!has_mlp) hval = 0.f; \
          const v4u wd = aread<24>(); \
          float ym[8]; \
          _Pragma("unroll") for (int k = 0; k < 4; ++k) { ym[2 * k] = hval * lo16(wd[k]); ym[2 * k + 1] = hval * hi16(wd[k]); } \
          _Pragma("unroll") for (int k = 0; k < 4; ++k) { float t = pair32(ym[k], ym[k + 4]); t = pair16(t, t); t += dpp<XOR8>(t); \
            ymlp[k] = __builtin_bit_cast(float, __builtin_amdgcn_ds_bpermute((rowsel * 32 + cg) * 4, __builtin_bit_cast(int, t))); }   \
        } break; \
        case 13: {   \
          const v4u e = aread<28>(); \
          unsigned mb = e[0], lb = e[1]; \
          if (!__all(mb != SENT && lb != SENT)) { \
            for (unsigned spins = 0; spins < SPIN_CAP; ++spins) { \
              __builtin_amdgcn_s_sleep(2); \
              const v2u t = ld64sc1_wait(lane * 8, mlb); \
              mb = t[0]; lb = t[1]; \
              if (__all(mb != SENT && lb != SENT)) break; \
            } \
          } \
          const float mi = __uint_as_float(mb), li = __uint_as_float(lb); \
          const float M_ = wave_max(mi); \
          const float L_ = wave_sum(li * __builtin_amdgcn_exp2f((mi - M_) * LOG2E)); \
          scale = __builtin_amdgcn_exp2f((mw - M_) * LOG2E) / L_; \
        } break; \
        default: break; \
      } \
    }
    REP16(M)
#undef M
#pragma unroll
    for (int k = 0; k < 7; ++k) sc[k] = sc[k + 1];
  }
  {
    const float p = sc[0];
    SETPRIO(15)
#define LISSUE(J) { if ((J) >= LT_END) s.issue<J, (J) & 7>(60); else awin<(J) & 7, ((J) & 3) * 1024>(s.voff, s.base(60 + ((J) >> 2)), mode); }
    LISSUE(5) LISSUE(6) LISSUE(7)
#define M(U) { const v4u v_ = aget<U, 7>(); LISSUE(8 + U) fma8(acc, bcast<U>(p), v_); if (U == 3) { SETPRIO(14) } }
    REP8(M)
#undef M
#define M(U) fma8(acc, bcast<8 + U>(p), aget<U, 7 - U>());
    REP8(M)
#undef M
  }
  STAMP(4)
  {
    const float b0 = pair32(acc[0][0], acc[2][0]), b1 = pair32(acc[0][1], acc[2][1]), b2 = pair32(acc[1][0], acc[3][0]), b3 = pair32(acc[1][1], acc[3][1]);
    const float c0 = pair16(b0, b2), c1 = pair16(b1, b3);
    Lo[w * 68 + 4 * c + r] = cvtpk(c0 * scale, c1 * scale);
  }
  BARRIER();
  {
    const unsigned* ap = Lo + (mn & 7) * 68 + 32 * (mn >> 3) + 4 * mg;
    const v4u a0 = *(const v4u*)ap, a1 = *(const v4u*)(ap + 16);
    f32x4 dd[8];
#define MF0(D, B) "v_mfma_f32_16x16x32_bf16 %" #D ", %8, a[" B "], 0\n\t"
#define MF1(D, B) "v_mfma_f32_16x16x32_bf16 %" #D ", %9, a[" B "], %" #D "\n\t"
    asm volatile("s_nop 1\n\t" MF0(0, "32:35") MF0(1, "36:39") MF0(2, "40:43") MF0(3, "44:47") MF0(4, "48:51") MF0(5, "52:55") MF0(6, "56:59") MF0(7, "60:63")
                 MF1(0, "64:67") MF1(1, "68:71") MF1(2, "72:75") MF1(3, "76:79") MF1(4, "80:83") MF1(5, "84:87") MF1(6, "88:91") MF1(7, "92:95") "s_nop 7"
                 : "=&v"(dd[0]), "=&v"(dd[1]), "=&v"(dd[2]), "=&v"(dd[3]), "=&v"(dd[4]), "=&v"(dd[5]), "=&v"(dd[6]), "=&v"(dd[7])
                 : "v"(a0), "v"(a1) : ACLOB);
    v4u o;
#pragma unroll
    for (int k = 0; k < 4; ++k) {
      const float t0 = (dd[k][0] + dd[k][1]) + (dd[k][2] + dd[k][3]), t1 = (dd[k + 4][0] + dd[k + 4][1]) + (dd[k + 4][2] + dd[k + 4][3]);
      auto sw = __builtin_amdgcn_permlane32_swap(__float_as_uint(t0), __float_as_uint(t1), false, false);
      float v = __uint_as_float(rowsel ? sw[1] : sw[0]);
      v += dpp<XOR1>(v);
      v = pair16(v, v);
      auto sv = __builtin_amdgcn_permlane32_swap(__float_as_uint(v), __float_as_uint(v), false, false);
      o[k] = __float_as_uint(__uint_as_float(rowsel ? sv[1] : sv[0]) + ymlp[k]) & 0xfffffffeu;
    }
    if (lane < 16) st128sc1((2 * w + (lane >> 3)) * 32768 + chunk * 128 + (lane & 7) * 16, ws + WS_Y, o);
  }
  STAMP(5)
  if (!has_mlp && (head & 1) == 0) {
    unsigned xv;
    ld16(xv, (lane & 31) * 2, (u64)xp + head * 32);
    adummy(0u, (u64)outp + head * 32);
    const u64 yb = ws + WS_Y + head * 16384 + w * 4096;
    v4u y0, y1, y2, y3;
    for (unsigned spins = 0; spins < SPIN_CAP; ++spins) {
      asm volatile("global_load_dwordx4 %0, %5, %6 sc1\n\tglobal_load_dwordx4 %1, %5, %6 offset:1024 sc1\n\t"
                   "global_load_dwordx4 %2, %5, %6 offset:2048 sc1\n\tglobal_load_dwordx4 %3, %5, %6 offset:3072 sc1\n\ts_waitcnt vmcnt(0)"
                   : "=&v"(y0), "=&v"(y1), "=&v"(y2), "=&v"(y3), "+v"(xv) : "v"(lane * 16), "s"(yb) : "memory");
      const v4u t = (y0 | y1) | (y2 | y3);
      if (__all((((t[0] | t[1]) | (t[2] | t[3])) & 1u) == 0u)) break;
    }
    float v[4];
#pragma unroll
    for (int k = 0; k < 4; ++k) v[k] = (__uint_as_float(y0[k]) + __uint_as_float(y1[k])) + (__uint_as_float(y2[k]) + __uint_as_float(y3[k]));
    float t = pair16(pair32(v[0], v[2]), pair32(v[1], v[3]));
    t += dpp<XOR8>(t);
    if ((lane & 8) == 0) Lr[w * 32 + 4 * (lane & 7) + (lane >> 4)] = t;
    BARRIER();
    if (w == 0 && lane < 32) {
      float sum = 0.f;
#pragma unroll
      for (int ww = 0; ww < 8; ++ww) sum += Lr[ww * 32 + lane];
      const float o = lo16(xv) + sum;
      st16(lane * 2, (u64)outp + head * 32, cvtpk(o, o));
      asm volatile(".Lfd_end:");
      if (head == 0 && lane == 0) st64sc1(0u, ws + WS_EPOCH, tag_lo, tag_hi);
    }
  }
}

extern "C" void kernel_launch(void* const* d_in, const int* in_sizes, int n_in, void* d_out, int out_size, void* d_ws, size_t ws_size,
                              hipStream_t stream) {
  fused_decode<<<NWG, WAVES * 64, 0, stream>>>(d_in[0], d_in[1], d_in[3], d_in[2], d_in[6], d_in[7], d_in[8], d_in[4], d_ws, d_out);
}
```

```cpp
#include <hip/hip_runtime.h>
#include <hip/hip_bf16.h>

typedef unsigned v4u __attribute__((ext_vector_type(4)));
typedef unsigned v2u __attribute__((ext_vector_type(2)));
typedef float f32x4 __attribute__((ext_vector_type(4)));
typedef float f32x2 __attribute__((ext_vector_type(2)));
typedef __bf16 bf16x2 __attribute__((ext_vector_type(2)));
typedef unsigned long long u64;
#define LAS __attribute__((address_space(3)))
#define STAMP(k)

constexpr int NWG = 256, WAVES = 8;
constexpr int W0 = 3;
constexpr int DM = 5;
constexpr int FB_T0 = -85, FB_T1 = 0, SCH_T0 = 600, SCH_K = 450, SCH_V = 426, ANCH_OFF = 65;
constexpr int ANCH_MASK = 0x55f0;
constexpr int LT_A = 30, LT_B = -20, LT_WA = 4, LT_WB = 3, LT_END = 13;
constexpr unsigned WS_EPOCH = 0, WS_FLAG = 1024, WS_ML = 4096, WS_CNT = 32768, WS_Y = 65536;
constexpr unsigned SENT = 0xffffffffu;
constexpr unsigned SPIN_CAP = 1u << 20, LSPIN_CAP = 1u << 26;
#define WPOL ""
#define KVPOL "nt"

__device__ __forceinline__ void ld128w(v4u& dst, unsigned voff, u64 sbase) {
  asm volatile("global_load_dwordx4 %0, %1, %2 " WPOL : "=v"(dst) : "v"(voff), "s"(sbase) : "memory");
}
__device__ __forceinline__ void ld16(unsigned& dst, unsigned voff, u64 sbase) {
  asm volatile("global_load_ushort %0, %1, %2" : "=v"(dst) : "v"(voff), "s"(sbase) : "memory");
}

#define ACLOB "a0","a1","a2","a3","a4","a5","a6","a7","a8","a9","a10","a11","a12","a13","a14","a15","a16","a17","a18","a19","a20","a21","a22","a23","a24","a25","a26","a27","a28","a29","a30","a31", \
  "a32","a33","a34","a35","a36","a37","a38","a39","a40","a41","a42","a43","a44","a45","a46","a47","a48","a49","a50","a51","a52","a53","a54","a55","a56","a57","a58","a59","a60","a61","a62","a63", \
  "a64","a65","a66","a67","a68","a69","a70","a71","a72","a73","a74","a75","a76","a77","a78","a79","a80","a81","a82","a83","a84","a85","a86","a87","a88","a89","a90","a91","a92","a93","a94","a95", \
  "a96","a97","a98","a99","a100","a101","a102","a103","a104","a105","a106","a107","a108","a109","a110","a111","a112","a113","a114","a115"
template <int SLOT, int OFF> __device__ __forceinline__ void ald(unsigned voff, u64 sbase) {
  asm volatile("global_load_dwordx4 a[%c2:%c3], %0, %1 offset:%c4 " KVPOL :: "v"(voff), "s"(sbase), "i"(4 * SLOT), "i"(4 * SLOT + 3), "i"(OFF) : "memory", ACLOB);
}
template <int SLOT, int OFF> __device__ __forceinline__ void aldw(unsigned voff, u64 sbase) {
  asm volatile("global_load_dwordx4 a[%c2:%c3], %0, %1 offset:%c4 " WPOL :: "v"(voff), "s"(sbase), "i"(4 * SLOT), "i"(4 * SLOT + 3), "i"(OFF) : "memory", ACLOB);
}
__device__ __forceinline__ void ald_gu(unsigned voff, u64 gbase, u64 ubase, int idx) {
  const u64 mask = 0xffull << (8 * idx);
  asm volatile("s_mov_b64 exec, %3\n\tglobal_load_dwordx4 a[100:103], %0, %1 " WPOL "\n\tglobal_load_dwordx4 a[104:107], %0, %2 " WPOL "\n\ts_mov_b64 exec, -1"
               :: "v"(voff), "s"(gbase), "s"(ubase), "s"(mask) : "memory", ACLOB);
}
template <int REG> __device__ __forceinline__ void ald64sc1(unsigned voff, u64 sbase) {
  asm volatile("global_load_dwordx2 a[%c2:%c3], %0, %1 sc1" :: "v"(voff), "s"(sbase), "i"(REG), "i"(REG + 1) : "memory", ACLOB);
}
__device__ __forceinline__ void aanchor(unsigned voff, u64 sbase) {
  asm volatile("global_load_dword a115, %0, %1 sc1" :: "v"(voff), "s"(sbase) : "memory", ACLOB);
}
__device__ __forceinline__ void adummy(unsigned zero, u64 sbase) {
  asm volatile("s_mov_b64 exec, 1\n\tglobal_load_dword a114, %0, %1\n\ts_mov_b64 exec, -1" :: "v"(zero), "s"(sbase) : "memory", ACLOB);
}
__device__ __forceinline__ void st32sc1(unsigned voff, u64 sbase, unsigned v) {
  asm volatile("global_store_dword %0, %1, %2 sc1" :: "v"(voff), "v"(v), "s"(sbase) : "memory");
}
template <int RSLOT, int N> __device__ __forceinline__ v4u aget(void) {
  unsigned a, b, c, d;
  asm volatile("s_waitcnt vmcnt(%c4)\n\tv_accvgpr_read_b32 %0, a%c5\n\tv_accvgpr_read_b32 %1, a%c6\n\tv_accvgpr_read_b32 %2, a%c7\n\tv_accvgpr_read_b32 %3, a%c8"
               : "=v"(a), "=v"(b), "=v"(c), "=v"(d) : "i"(N), "i"(4 * RSLOT), "i"(4 * RSLOT + 1), "i"(4 * RSLOT + 2), "i"(4 * RSLOT + 3) : "memory", ACLOB);
  return v4u{a, b, c, d};
}
template <int RSLOT, int N4, int N3, int N2, int WSLOT, int OFF> __device__ __forceinline__ v4u astep(unsigned voff, u64 sbase, int mode) {
  unsigned a, b, c, d;
  asm volatile("s_waitcnt vmcnt(%c7)\n\t"
               "s_cmp_gt_i32 %6, %c17\n\ts_cbranch_scc1 1f\n\t"
               "s_waitcnt vmcnt(%c8)\n\t"
               "s_cmp_gt_i32 %6, %c18\n\ts_cbranch_scc1 1f\n\t"
               "s_waitcnt vmcnt(%c9)\n"
               "1:\n\tglobal_load_dwordx4 a[%c14:%c15], %4, %5 offset:%c16 " KVPOL "\n\t"
               "v_accvgpr_read_b32 %0, a%c10\n\tv_accvgpr_read_b32 %1, a%c11\n\tv_accvgpr_read_b32 %2, a%c12\n\tv_accvgpr_read_b32 %3, a%c13"
               : "=&v"(a), "=&v"(b), "=&v"(c), "=&v"(d)
               : "v"(voff), "s"(sbase), "s"(mode), "i"(N4), "i"(N3), "i"(N2),
                 "i"(4 * RSLOT), "i"(4 * RSLOT + 1), "i"(4 * RSLOT + 2), "i"(4 * RSLOT + 3), "i"(4 * WSLOT), "i"(4 * WSLOT + 3), "i"(OFF),
                 "i"(FB_T1), "i"(FB_T0)
               : "memory", "scc", ACLOB);
  return v4u{a, b, c, d};
}
template <int WSLOT, int OFF> __device__ __forceinline__ void awin(unsigned voff, u64 sbase, int mode) {
  asm volatile("s_cmp_gt_i32 %2, %c6\n\ts_cbranch_scc1 1f\n\t"
               "s_waitcnt vmcnt(%c8)\n\t"
               "s_cmp_gt_i32 %2, %c7\n\ts_cbranch_scc1 1f\n\t"
               "s_waitcnt vmcnt(%c9)\n"
               "1:\n\tglobal_load_dwordx4 a[%c3:%c4], %0, %1 offset:%c5 " KVPOL
               :: "v"(voff), "s"(sbase), "s"(mode), "i"(4 * WSLOT), "i"(4 * WSLOT + 3), "i"(OFF), "i"(LT_A), "i"(LT_B), "i"(LT_WA - 1), "i"(LT_WB - 1)
               : "memory", "scc", ACLOB);
}
template <int N, int WSLOT, int OFF> __device__ __forceinline__ void apre(unsigned voff, u64 sbase) {
  asm volatile("s_waitcnt vmcnt(%c2)\n\tglobal_load_dwordx4 a[%c3:%c4], %0, %1 offset:%c5 " KVPOL
               :: "v"(voff), "s"(sbase), "i"(N), "i"(4 * WSLOT), "i"(4 * WSLOT + 3), "i"(OFF) : "memory", ACLOB);
}
template <int RSLOT> __device__ __forceinline__ v4u aread(void) {
  unsigned a, b, c, d;
  asm volatile("v_accvgpr_read_b32 %0, a%c4\n\tv_accvgpr_read_b32 %1, a%c5\n\tv_accvgpr_read_b32 %2, a%c6\n\tv_accvgpr_read_b32 %3, a%c7\n\ts_nop 1"
               : "=v"(a), "=v"(b), "=v"(c), "=v"(d) : "i"(4 * RSLOT), "i"(4 * RSLOT + 1), "i"(4 * RSLOT + 2), "i"(4 * RSLOT + 3) : "memory", ACLOB);
  return v4u{a, b, c, d};
}
template <int SLOT> __device__ __forceinline__ void awrite(unsigned a, unsigned b, unsigned c, unsigned d) {
  asm volatile("v_accvgpr_write_b32 a%c4, %0\n\tv_accvgpr_write_b32 a%c5, %1\n\tv_accvgpr_write_b32 a%c6, %2\n\tv_accvgpr_write_b32 a%c7, %3"
               :: "v"(a), "v"(b), "v"(c), "v"(d), "i"(4 * SLOT), "i"(4 * SLOT + 1), "i"(4 * SLOT + 2), "i"(4 * SLOT + 3) : ACLOB);
}
template <int S> __device__ __forceinline__ void wo_frag(void);
__device__ __forceinline__ unsigned aread_samples(void) {
  unsigned a;
  asm volatile("v_accvgpr_read_b32 %0, a115\n\ts_nop 1" : "=v"(a) :: "memory", ACLOB);
  return a;
}
__device__ __forceinline__ void st128sc1(unsigned voff, u64 sbase, const v4u& v) {
  asm volatile("global_store_dwordx4 %0, %1, %2 sc1\n\ts_nop 1" :: "v"(voff), "v"(v), "s"(sbase) : "memory");
}
__device__ __forceinline__ void st64sc1(unsigned voff, u64 sbase, unsigned lo, unsigned hi) {
  const v2u v = {lo, hi};
  asm volatile("global_store_dwordx2 %0, %1, %2 sc1" :: "v"(voff), "v"(v), "s"(sbase) : "memory");
}
__device__ __forceinline__ void st16(unsigned voff, u64 sbase, unsigned v) {
  asm volatile("global_store_short %0, %1, %2" :: "v"(voff), "v"(v), "s"(sbase) : "memory");
}
__device__ __forceinline__ v2u ld64sc1_wait(unsigned voff, u64 sbase) {
  v2u v;
  asm volatile("global_load_dwordx2 %0, %1, %2 sc1\n\ts_waitcnt vmcnt(0)" : "=&v"(v) : "v"(voff), "s"(sbase) : "memory");
  return v;
}

__device__ __forceinline__ float dot2(unsigned a, unsigned b, float acc) {
  return __builtin_amdgcn_fdot2_f32_bf16(__builtin_bit_cast(bf16x2, a), __builtin_bit_cast(bf16x2, b), acc, false);
}
__device__ __forceinline__ float lo16(unsigned v) { return __builtin_bit_cast(float, v << 16); }
__device__ __forceinline__ float hi16(unsigned v) { return __builtin_bit_cast(float, v & 0xffff0000u); }
template <int CTRL> __device__ __forceinline__ float dpp(float x) {
  return __builtin_bit_cast(float, __builtin_amdgcn_mov_dpp(__builtin_bit_cast(int, x), CTRL, 0xf, 0xf, true));
}
template <int CTRL> __device__ __forceinline__ unsigned dppu(unsigned x) { return (unsigned)__builtin_amdgcn_mov_dpp((int)x, CTRL, 0xf, 0xf, true); }
constexpr int XOR1 = 0xB1, XOR2 = 0x4E, XOR7 = 0x141, XOR8 = 0x128, ROR4 = 0x124;
__device__ __forceinline__ unsigned cvtpk(float lo, float hi) {
  const bf16x2 v = {(__bf16)lo, (__bf16)hi};
  return __builtin_bit_cast(unsigned, v);
}
__device__ __forceinline__ float rbf(float x) { return lo16(cvtpk(x, x)); }
__device__ __forceinline__ float pair32(float a, float b) {
  auto s = __builtin_amdgcn_permlane32_swap(__float_as_uint(a), __float_as_uint(b), false, false);
  return __uint_as_float(s[0]) + __uint_as_float(s[1]);
}
__device__ __forceinline__ float pair16(float a, float b) {
  auto s = __builtin_amdgcn_permlane16_swap(__float_as_uint(a), __float_as_uint(b), false, false);
  return __uint_as_float(s[0]) + __uint_as_float(s[1]);
}
__device__ __forceinline__ float wave_sum(float x) {
  x += dpp<XOR1>(x); x += dpp<XOR2>(x); x += dpp<XOR7>(x); x += dpp<XOR8>(x);
  x = pair16(x, x); x = pair32(x, x); return x;
}
__device__ __forceinline__ float wave_max(float x) {
  x = fmaxf(x, dpp<XOR1>(x)); x = fmaxf(x, dpp<XOR2>(x)); x = fmaxf(x, dpp<XOR7>(x)); x = fmaxf(x, dpp<XOR8>(x));
  auto s = __builtin_amdgcn_permlane16_swap(__float_as_uint(x), __float_as_uint(x), false, false);
  x = fmaxf(__uint_as_float(s[0]), __uint_as_float(s[1]));
  auto t = __builtin_amdgcn_permlane32_swap(__float_as_uint(x), __float_as_uint(x), false, false);
  return fmaxf(__uint_as_float(t[0]), __uint_as_float(t[1]));
}
__device__ __forceinline__ float treduce16(float (&x)[16], int c) {
#define TR_STEP(HALF, CTRL, BIT) _Pragma("unroll") for (int h = 0; h < (HALF); ++h) { \
    const float lo_ = x[h], hi_ = x[h + (HALF)]; const float t1_ = lo_ + dpp<CTRL>(lo_), t2_ = hi_ + dpp<CTRL>(hi_); x[h] = (c & (BIT)) ? t2_ : t1_; }
  TR_STEP(8, XOR8, 8) TR_STEP(4, XOR7, 4) TR_STEP(2, XOR2, 2) TR_STEP(1, XOR1, 1)
#undef TR_STEP
  return x[0];
}
template <int CTRL, int BIT> __device__ __forceinline__ float tmerge(float lo, float hi, int c) {
  const float t1 = lo + dpp<CTRL>(lo), t2 = hi + dpp<CTRL>(hi); return (c & BIT) ? t2 : t1;
}
template <int U> __device__ __forceinline__ void tfeed(float (&x)[16], int c) {
  if constexpr ((U & 1) == 1) x[U - 1] = tmerge<XOR1, 1>(x[U - 1], x[U], c);
  if constexpr ((U & 3) == 3) x[U - 3] = tmerge<XOR2, 2>(x[U - 3], x[U - 1], c);
  if constexpr ((U & 7) == 7) x[U - 7] = tmerge<ROR4, 4>(x[U - 7], x[U - 3], c);
  if constexpr (U == 15) x[0] = tmerge<XOR8, 8>(x[0], x[8], c);
  constexpr int L = (U == 15) ? 0 : ((U & 7) == 7) ? U - 7 : ((U & 3) == 3) ? U - 3 : ((U & 1) == 1) ? U - 1 : U;
  asm volatile("" : "+v"(x[L]));
}
template <int T> __device__ __forceinline__ float bcast(float p) { return dpp<0x150 + T>(p); }
__device__ __forceinline__ void fma8(f32x2 (&acc)[4], float p, const v4u& v) {
  const f32x2 pp = {p, p};
#pragma unroll
  for (int k = 0; k < 4; ++k) acc[k] = __builtin_elementwise_fma(pp, f32x2{lo16(v[k]), hi16(v[k])}, acc[k]);
  asm volatile("" : "+v"(acc[0]), "+v"(acc[1]), "+v"(acc[2]), "+v"(acc[3]));
}
__device__ __forceinline__ unsigned plo(unsigned hi_row, unsigned lo_row) { return __builtin_amdgcn_perm(hi_row, lo_row, 0x05040100u); }
__device__ __forceinline__ unsigned phi(unsigned hi_row, unsigned lo_row) { return __builtin_amdgcn_perm(hi_row, lo_row, 0x07060302u); }

template <int S> __device__ __forceinline__ void wo_frag(void) {
  const v4u r0 = aread<S>(), r1 = aread<S + 1>(), r2 = aread<S + 2>(), r3 = aread<S + 3>(), r4 = aread<S + 4>(), r5 = aread<S + 5>(), r6 = aread<S + 6>(), r7 = aread<S + 7>();
#define WF(K) awrite<S + 2 * K>(plo(r1[K], r0[K]), plo(r3[K], r2[K]), plo(r5[K], r4[K]), plo(r7[K], r6[K])); \
              awrite<S + 2 * K + 1>(phi(r1[K], r0[K]), phi(r3[K], r2[K]), phi(r5[K], r4[K]), phi(r7[K], r6[K]));
  WF(0) WF(1) WF(2) WF(3)
#undef WF
}

constexpr int younger(int U, int X, int NA, int NB) { return (U + DM - 1 - X) + ((U >= 1 && X <= DM) ? NA : 0) + ((U >= 9 && X <= 8 + DM) ? NB : 0); }
#define MAPSET 0x1000, 0x800, 0x80, 0x100, 0x400,  0x40, 0x8, 0x4,  0x600, 0x10, 0x20,  0x100, 0x200, 0, 0, 0
struct Stream {
  u64 K, V; unsigned voff, cofs; unsigned mr[5];
  __device__ __forceinline__ u64 base(int round) const {
    unsigned t = cofs;
#pragma unroll
    for (int k = 0; k < 5; ++k) t ^= ((round >> k) & 1) ? mr[k] : 0u;
    return ((round < 32) ? K : V) + (u64)t;
  }
  template <int J, int SLOT> __device__ __forceinline__ void issue(int round0) const { ald<SLOT, (J & 3) * 1024>(voff, base(round0 + (J >> 2))); }
  template <int U, int NA, int NB> __device__ __forceinline__ v4u step(int round0, int mode) const {
    return astep<(U & 7), younger(U, U + 1, NA, NB), younger(U, U + 2, NA, NB), younger(U, U + 3, NA, NB),
                 ((U + DM) & 7), ((U + DM) & 3) * 1024>(
        voff, base(round0 + ((U + DM) >> 2)), mode);
  }
};
__device__ __forceinline__ float qk(const v4u& k, const v4u& q) { return dot2(k[3], q[3], dot2(k[2], q[2], dot2(k[1], q[1], dot2(k[0], q[0], 0.f)))); }
#define REP16(M) M(0) M(1) M(2) M(3) M(4) M(5) M(6) M(7) M(8) M(9) M(10) M(11) M(12) M(13) M(14) M(15)
#define REP15(M) M(1) M(2) M(3) M(4) M(5) M(6) M(7) M(8) M(9) M(10) M(11) M(12) M(13) M(14) M(15)
#define REP8(M) M(0) M(1) M(2) M(3) M(4) M(5) M(6) M(7)
#define BARRIER() asm volatile("s_waitcnt lgkmcnt(0)\n\ts_barrier" ::: "memory")

constexpr int WQ0 = 4, QPOST = W0 + 8 - WQ0, QGET = 10, XSTEP = 8;
static_assert(!((ANCH_MASK >> 13) & 1) && !(ANCH_MASK & 15), "tile 13 carries the (m,l) gather; samples are read behind the flags");
static_assert(QGET > QPOST && QGET < 16 - W0 && XSTEP + WQ0 >= 8 && XSTEP + W0 < 16, "tile-0 schedule");
constexpr int t0_ext(int m) { return m < 0 ? 2 * WQ0 : (m + WQ0 < 8 ? 2 : (m == XSTEP ? 4 : 0)); }
constexpr int t0_wait(int n) { int c = W0 - 1; for (int m = (n - W0 < -1 ? -1 : n - W0); m <= n - 1; ++m) c += t0_ext(m); return c; }

__device__ __forceinline__ float fin_score(float d) { return rbf(rbf(d) * 0.08838834764831845f); }
constexpr float LOG2E = 1.4426950408889634f;

__global__ void __launch_bounds__(WAVES * 64, 2) fused_decode(const void* xp, const void* Kp, const void* wqp, const void* Vp, const void* wgp,
                                                             const void* wup, const void* wdp, const void* wop, void* wsp, void* outp) {
  __shared__ __attribute__((aligned(16))) float lds[8 * 128 + 8 * 16 + 8 * 68 + 8 * 32 + 4];
  float* Lq = lds;
  float* Lg = lds + 1024;
  unsigned* Lo = (unsigned*)(lds + 1024 + 128);
  float* Lr = lds + 1024 + 128 + 8 * 68;
  volatile LAS unsigned* Lcnt = (volatile LAS unsigned*)(Lr + 256);
  {
    u64 pc; unsigned nk;
    asm volatile("s_getpc_b64 %0\n.Lfd_begin:\n\ts_mov_b32 %1, (.Lfd_end-.Lfd_begin)>>10" : "=s"(pc), "=s"(nk));
    const unsigned sl = (((blockIdx.x >> 3) * 8 + (threadIdx.x >> 6)) * nk) >> 8;
    asm volatile("global_load_dwordx4 a[112:115], %0, %1" :: "v"(sl * 1024 + (threadIdx.x & 63) * 16), "s"(pc & ~63ull) : "memory", ACLOB);
  }
  asm volatile("" :: "s"(wsp), "s"(outp), "s"(xp));
  STAMP(0)
  const int tid = threadIdx.x, lane = tid & 63, w = __builtin_amdgcn_readfirstlane(tid >> 6), r = lane >> 4, c = lane & 15;
  const int b = blockIdx.x, xcd = b & 7, i8 = b >> 3;
  const int head = xcd * 4 + (i8 & 3), split = i8 >> 2, chunk = head * 8 + split;
  const bool has_mlp = i8 < 28;
  const int j = xcd * 28 + (has_mlp ? i8 : 27);
  const u64 ws = (u64)wsp;
  const u64 mlb = ws + WS_ML + head * 512;

  Stream s;
  {
    constexpr unsigned mp[16] = {MAPSET};
    unsigned t = 0;
#pragma unroll
    for (int k = 0; k < 3; ++k) t ^= (((w >> k) & 1) ? mp[5 + k] : 0u) ^ (((split >> k) & 1) ? mp[8 + k] : 0u);
#pragma unroll
    for (int k = 0; k < 5; ++k) { t ^= ((head >> k) & 1) ? mp[11 + k] : 0u; s.mr[k] = mp[k] << 10; }
    s.cofs = t << 10; s.K = (u64)Kp + ((u64)head << 23); s.V = (u64)Vp + ((u64)head << 23); s.voff = lane * 16;
  }
  v4u x8; unsigned xs; v4u wq[16];
  const unsigned wq_vo = r * 8192 + c * 16; const u64 wq_sb = (u64)wqp + (u64)w * (64 * 8192) + head * 256;
#define WQ_PAIR(P) { ld128w(wq[2 * (P)], wq_vo, wq_sb + (u64)(2 * (P)) * (4 * 8192)); ld128w(wq[2 * (P) + 1], wq_vo, wq_sb + (u64)(2 * (P) + 1) * (4 * 8192)); }
  ld128w(x8, lane * 16, (u64)xp);
  ld16(xs, (4 * c + r) * 2, (u64)xp + w * 128);
  s.issue<0, 0>(0); s.issue<1, 1>(0); s.issue<2, 2>(0);
  __builtin_amdgcn_sched_barrier(0);
  const u64 tstart = __builtin_amdgcn_s_memrealtime();
  WQ_PAIR(0) WQ_PAIR(1) WQ_PAIR(2) WQ_PAIR(3)
  if (tid == 0) { Lcnt[0] = 0u; Lcnt[1] = 0u; Lcnt[2] = (unsigned)tstart + SCH_T0; Lcnt[3] = 0u; }
  BARRIER();
  const int mg = lane >> 4, mn = lane & 15, cg = mn >> 1, rowsel = lane & 1;
  const unsigned gu_vo = (4 * c + r) * 3584;
  const u64 g_sb = (u64)wgp + (u64)w * (64 * 3584) + j * 16, u_sb = (u64)wup + (u64)w * (64 * 3584) + j * 16;
  const u64 wo_sb = (u64)wop + (u64)head * (128 * 1024) + w * 128;
  unsigned tch[8];
  {
#define T(I, A) asm volatile("s_load_dword %0, %1, 0x0" : "=s"(tch[I]) : "s"((u64)(A) & ~3ull) : "memory");
    T(0, g_sb) T(1, u_sb) T(2, (u64)wdp + (u64)j * 8192) T(3, ws + 3072) T(4, wo_sb) T(5, s.base(32)) T(6, (u64)outp) T(7, g_sb + 63 * 3584)
#undef T
  }
  float xn; unsigned xpair;
  float qa[8] = {0.f, 0.f, 0.f, 0.f, 0.f, 0.f, 0.f, 0.f};
  v4u q;
  float sc[8];
  float d[16];
#define PRE(N) apre<t0_wait(N), ((N) + W0 < 16 ? (N) + W0 : ((N) + W0) & 7), (((N) + W0) & 3) * 1024>(s.voff, s.base(((N) + W0) >> 2));
#define QP(P) { asm volatile("" : "+v"(wq[2 * (P)]), "+v"(wq[2 * (P) + 1])); const unsigned xx = dppu<0x150 + 2 * (P)>(xpair); \
      _Pragma("unroll") for (int k = 0; k < 4; ++k) { qa[2 * k] = dot2(plo(wq[2 * (P) + 1][k], wq[2 * (P)][k]), xx, qa[2 * k]); \
        qa[2 * k + 1] = dot2(phi(wq[2 * (P) + 1][k], wq[2 * (P)][k]), xx, qa[2 * k + 1]); } }
#define KQ(U) { d[U] = qk(aread<U>(), q); tfeed<U>(d, c); }
#define M(N) PRE(N) \
    if ((N) + WQ0 < 8) WQ_PAIR(((N) + WQ0) & 7) \
    if ((N) == XSTEP) { ald_gu(gu_vo, g_sb, u_sb, 0); if (w == 0) ald64sc1<108>(0u, ws + WS_EPOCH); else adummy(0u, (u64)xp); \
                        aldw<24, 0>((lane >> 3) * 1024 + (lane & 7) * 16, (u64)wdp + (u64)j * 8192 + w * 128); } \
    if ((N) == 0) { \
        \
      asm volatile("" : "+v"(x8), "+v"(xs)); \
      float xv[8]; \
      _Pragma("unroll") for (int k = 0; k < 4; ++k) { xv[2 * k] = lo16(x8[k]); xv[2 * k + 1] = hi16(x8[k]); } \
      float sum = 0.f; \
      _Pragma("unroll") for (int k = 0; k < 8; ++k) sum += xv[k]; \
      const float mean = wave_sum(sum) * (1.f / 512.f); \
      float sq = 0.f; \
      _Pragma("unroll") for (int k = 0; k < 8; ++k) { const float dlt = xv[k] - mean; sq = fmaf(dlt, dlt, sq); } \
      const float var = wave_sum(sq) * (1.f / 512.f); \
      xn = rbf(rbf(lo16(xs) - rbf(mean)) / rbf(sqrtf(rbf(rbf(var) + 1e-5f)))); \
      const unsigned xb = __float_as_uint(xn); \
      xpair = phi(dppu<XOR1>(xb), xb);     \
      STAMP(1) \
    } \
    if ((N) == W0) { QP(0) QP(1) QP(2) QP(3) } \
    if ((N) > W0 && (N) - W0 - 1 + WQ0 < 8) QP(((N) - W0 - 1 + WQ0) & 7) \
    if ((N) == QPOST) { \
      const float b0 = pair32(qa[0], qa[4]), b1 = pair32(qa[1], qa[5]), b2 = pair32(qa[2], qa[6]), b3 = pair32(qa[3], qa[7]); \
      const float c0 = pair16(b0, b2), c1 = pair16(b1, b3); \
      *(f32x2*)(Lq + w * 128 + 8 * c + 2 * r) = f32x2{c0, c1};     \
      asm volatile("s_waitcnt lgkmcnt(0)" : "+s"(tch[0]), "+s"(tch[1]), "+s"(tch[2]), "+s"(tch[3]), "+s"(tch[4]), "+s"(tch[5]), "+s"(tch[6]), "+s"(tch[7]) :: "memory"); \
      if (lane == 0) __hip_atomic_fetch_add((LAS unsigned*)(Lcnt + 1), 1u, __ATOMIC_RELAXED, __HIP_MEMORY_SCOPE_WORKGROUP); \
    } \
    if ((N) == QGET) { \
      for (unsigned spins = 0; Lcnt[1] != 8u && spins < LSPIN_CAP; ++spins) __builtin_amdgcn_s_sleep(1); \
      asm volatile("" ::: "memory"); \
      f32x4 a0 = *(const f32x4*)(Lq + (2 * r) * 128 + 8 * c), a1 = *(const f32x4*)(Lq + (2 * r) * 128 + 8 * c + 4); \
      f32x4 b0 = *(const f32x4*)(Lq + (2 * r + 1) * 128 + 8 * c), b1 = *(const f32x4*)(Lq + (2 * r + 1) * 128 + 8 * c + 4); \
      a0 += b0; a1 += b1; \
      float v[8] = {a0[0], a0[1], a0[2], a0[3], a1[0], a1[1], a1[2], a1[3]}; \
      _Pragma("unroll") for (int k = 0; k < 8; ++k) { v[k] = pair16(v[k], v[k]); v[k] = pair32(v[k], v[k]); } \
      _Pragma("unroll") for (int k = 0; k < 4; ++k) q[k] = cvtpk(v[2 * k], v[2 * k + 1]); \
      STAMP(2) \
      KQ(0) KQ(1) KQ(2) KQ(3) \
    } \
    if ((N) == QGET + 1) { KQ(4) KQ(5) KQ(6) KQ(7) KQ(8) KQ(9) KQ(10) } \
    if ((N) > QGET) KQ(N) \
    __builtin_amdgcn_sched_barrier(0);
  REP16(M)
#undef M
  sc[7] = fin_score(d[0]);
  PRE(16) PRE(17)
  unsigned tag_lo, tag_hi;
  {
    const v4u e = aread<27>();
    const u64 tag = (((u64)(unsigned)__builtin_amdgcn_readfirstlane(e[1]) << 32) | (u64)(unsigned)__builtin_amdgcn_readfirstlane(e[0])) + 1ull;
    tag_lo = (unsigned)tag; tag_hi = (unsigned)(tag >> 32);
  }
  const bool clearer = !has_mlp && w == 0;
  if (clearer) {
    const v4u sv = {SENT, SENT, SENT, SENT};
    for (int i = 0; i < 16; ++i) st128sc1(lane * 16 + i * 1024, ws + WS_Y + head * 16384, sv);
    if (lane < 32) st128sc1(lane * 16, mlb, sv);
    if (lane < 16) st32sc1(lane * 1024 + xcd * 128 + (head & 3) * 4, ws + WS_CNT, SENT);
  }
  const unsigned fl_vo = ((lane >> 2) & 7) * 128 + (lane & 3) * 8;
  const unsigned wo_vo = (64 * rowsel + 8 * mg) * 1024 + cg * 16;
  int mode = FB_T1;
#define NOMINAL(TILE) ((TILE) <= 8 ? (TILE) * SCH_K : 8 * SCH_K + ((TILE) - 8) * SCH_V)
#define ANCHOR_LD(TILE) if (w == 0 && ((ANCH_MASK >> (TILE)) & 1)) aanchor(((lane >> 2) & 7) * 128 + (lane & 3) * 4 + ((TILE) - 2) * 1024, ws + WS_CNT); else adummy(0u, (u64)xp);
#define FEEDBACK(TILE) { const unsigned now_ = (unsigned)__builtin_amdgcn_s_memrealtime(); \
    if (w == 0) { \
      if (clearer && ((ANCH_MASK >> ((TILE) + 2)) & 1) && lane == 0) st32sc1((TILE) * 1024 + xcd * 128 + (head & 3) * 4, ws + WS_CNT, now_ & 0xfffffffeu); \
      if ((ANCH_MASK >> (TILE)) & 1) { const unsigned a_ = aread_samples(); const bool ok_ = a_ != SENT; \
        const float sum_ = wave_sum(ok_ ? (float)(int)(a_ - now_) : 0.f); const int n_ = __popcll(__ballot(ok_)); \
        if (n_ >= 32) Lcnt[2] = now_ + (unsigned)((int)(sum_ / (float)n_) - NOMINAL((TILE) - 2) + ANCH_OFF); } } \
    mode = (int)(now_ - (unsigned)__builtin_amdgcn_readfirstlane((int)Lcnt[2])) - NOMINAL(TILE); \
    if ((TILE) < 4 && mode < FB_T1) mode = FB_T1; }
#define FEEDBACK2(TILE) { const unsigned now_ = (unsigned)__builtin_amdgcn_s_memrealtime(); \
    mode = (int)(now_ - (unsigned)__builtin_amdgcn_readfirstlane((int)Lcnt[2])) - NOMINAL(TILE) - ((TILE) < 8 ? SCH_K / 2 : SCH_V / 2); \
    if ((TILE) < 4 && mode < FB_T1) mode = FB_T1; }
#define SETPRIO(TILE) { if (((w >> 2) ^ (TILE)) & 1) __builtin_amdgcn_s_setprio(1); else __builtin_amdgcn_s_setprio(0); }
#pragma nounroll
  for (int tile = 1; tile < 8; ++tile) {
    SETPRIO(tile)
    d[0] = qk(s.step<0, 3, 2>(tile * 4, mode), q); tfeed<0>(d, c);
    switch (tile) {
      case 1: aldw<8, 0>(wo_vo, wo_sb); aldw<9, 1024>(wo_vo, wo_sb); aldw<10, 2048>(wo_vo, wo_sb); break;
      case 2: aldw<11, 3072>(wo_vo, wo_sb); aldw<12, 0>(wo_vo, wo_sb + 4096); aldw<13, 1024>(wo_vo, wo_sb + 4096); break;
      case 3: aldw<14, 2048>(wo_vo, wo_sb + 4096); aldw<15, 3072>(wo_vo, wo_sb + 4096); if (w == 0) ald64sc1<110>(fl_vo, ws + WS_FLAG); else adummy(0u, (u64)xp); break;
      case 4: aldw<16, 0>(wo_vo, wo_sb + 32768); aldw<17, 1024>(wo_vo, wo_sb + 32768); ANCHOR_LD(tile) break;
      case 5: aldw<18, 2048>(wo_vo, wo_sb + 32768); aldw<19, 3072>(wo_vo, wo_sb + 32768); ANCHOR_LD(tile) break;
      case 6: aldw<20, 0>(wo_vo, wo_sb + 36864); aldw<21, 1024>(wo_vo, wo_sb + 36864); ANCHOR_LD(tile) break;
      default: aldw<22, 2048>(wo_vo, wo_sb + 36864); aldw<23, 3072>(wo_vo, wo_sb + 36864); ANCHOR_LD(tile) break;
    }
#define M(U) d[U] = qk(s.step<U, 3, 2>(tile * 4, mode), q); tfeed<U>(d, c); \
    if (U == 7) { \
      { FEEDBACK(tile) } \
        \
      if (tile == 1 && clearer && lane == 0) st64sc1(0u, ws + WS_FLAG + xcd * 128 + (head & 3) * 8, tag_lo, tag_hi); \
    } \
    if (U == 15) { FEEDBACK2(tile) } \
    if (U == 8) ald_gu(gu_vo, g_sb, u_sb, tile);
    REP15(M)
#undef M
#pragma unroll
    for (int k = 0; k < 7; ++k) sc[k] = sc[k + 1];
    sc[7] = fin_score(d[0]);
    if (tile == 1) { STAMP(6) }
    if (tile == 3 && w == 0) {
      const v4u e = aread<27>();
      if (!__all(e[2] == tag_lo && e[3] == tag_hi)) {
        for (unsigned spins = 0; spins < SPIN_CAP; ++spins) {
          __builtin_amdgcn_s_sleep(2);
          const v2u f = ld64sc1_wait(fl_vo, ws + WS_FLAG);
          if (__all(f[0] == tag_lo && f[1] == tag_hi)) break;
        }
      }
      Lcnt[3] = 1u;
    }
  }
  STAMP(3)
  float mw;
  {
    float m = sc[0];
#pragma unroll
    for (int k = 1; k < 8; ++k) m = fmaxf(m, sc[k]);
    mw = wave_max(m);
    float l = 0.f;
#pragma unroll
    for (int k = 0; k < 8; ++k) { sc[k] = __builtin_amdgcn_exp2f((sc[k] - mw) * LOG2E); l += sc[k]; }
    const float lw = wave_sum(l);
    for (unsigned spins = 0; Lcnt[3] == 0u && spins < LSPIN_CAP; ++spins) __builtin_amdgcn_s_sleep(1);
    asm volatile("" ::: "memory");
    if (lane == 0) st64sc1(0u, mlb + (split * 8 + w) * 8, __float_as_uint(mw) & 0xfffffffeu, __float_as_uint(lw) & 0xfffffffeu);
  }
  f32x2 acc[4] = {{0.f, 0.f}, {0.f, 0.f}, {0.f, 0.f}, {0.f, 0.f}};
  float scale = 0.f, ymlp[4] = {0.f, 0.f, 0.f, 0.f};
#pragma nounroll
  for (int tile = 8; tile < 15; ++tile) {
    SETPRIO(tile)
    const float p = sc[0];
#define M(U) fma8(acc, bcast<U>(p), s.step<U, 1, 0>(tile * 4, mode)); \
    if (U == 0) { if (tile == 13) ald64sc1<112>(lane * 8, mlb); else ANCHOR_LD(tile) } \
    if (U == 7) { FEEDBACK(tile) } \
    if (U == 15) { FEEDBACK2(tile) } \
    if (U == 10) { \
        \
      switch (tile) { \
        case 8: {   \
          const v4u wg = aread<25>(), wu = aread<26>(); \
          float g[16]; \
          _Pragma("unroll") for (int k = 0; k < 4; ++k) { g[2 * k] = xn * lo16(wg[k]); g[2 * k + 1] = xn * hi16(wg[k]); g[8 + 2 * k] = xn * lo16(wu[k]); g[8 + 2 * k + 1] = xn * hi16(wu[k]); } \
          float t = treduce16(g, c); \
          t = pair16(t, t); t = pair32(t, t); \
          if (r == 0) Lg[w * 16 + c] = t; \
          asm volatile("s_waitcnt lgkmcnt(0)" ::: "memory"); \
          if (lane == 0) __hip_atomic_fetch_add((LAS unsigned*)Lcnt, 1u, __ATOMIC_RELAXED, __HIP_MEMORY_SCOPE_WORKGROUP); \
        } break; \
        case 9: wo_frag<8>(); break; \
        case 10: wo_frag<16>(); break; \
        case 11: {   \
          for (unsigned spins = 0; Lcnt[0] != 8u && spins < LSPIN_CAP; ++spins) __builtin_amdgcn_s_sleep(1); \
          asm volatile("" ::: "memory"); \
          const int f = lane >> 3; \
          float g = 0.f, u = 0.f; \
          _Pragma("unroll") for (int ww = 0; ww < 8; ++ww) { g += Lg[ww * 16 + f]; u += Lg[ww * 16 + 8 + f]; } \
          g = rbf(g); u = rbf(u); \
          float hval = rbf(rbf(1.f / (1.f + __expf(-g))) * u); \
          if (!has_mlp) hval = 0.f; \
          const v4u wd = aread<24>(); \
          float ym[8]; \
          _Pragma("unroll") for (int k = 0; k < 4; ++k) { ym[2 * k] = hval * lo16(wd[k]); ym[2 * k + 1] = hval * hi16(wd[k]); } \
          _Pragma("unroll") for (int k = 0; k < 4; ++k) { float t = pair32(ym[k], ym[k + 4]); t = pair16(t, t); t += dpp<XOR8>(t); \
            ymlp[k] = __builtin_bit_cast(float, __builtin_amdgcn_ds_bpermute((rowsel * 32 + cg) * 4, __builtin_bit_cast(int, t))); }   \
        } break; \
        case 13: {   \
          const v4u e = aread<28>(); \
          unsigned mb = e[0], lb = e[1]; \
          if (!__all(mb != SENT && lb != SENT)) { \
            for (unsigned spins = 0; spins < SPIN_CAP; ++spins) { \
              __builtin_amdgcn_s_sleep(2); \
              const v2u t = ld64sc1_wait(lane * 8, mlb); \
              mb = t[0]; lb = t[1]; \
              if (__all(mb != SENT && lb != SENT)) break; \
            } \
          } \
          const float mi = __uint_as_float(mb), li = __uint_as_float(lb); \
          const float M_ = wave_max(mi); \
          const float L_ = wave_sum(li * __builtin_amdgcn_exp2f((mi - M_) * LOG2E)); \
          scale = __builtin_amdgcn_exp2f((mw - M_) * LOG2E) / L_; \
        } break; \
        default: break; \
      } \
    }
    REP16(M)
#undef M
#pragma unroll
    for (int k = 0; k < 7; ++k) sc[k] = sc[k + 1];
  }
  {
    const float p = sc[0];
    SETPRIO(15)
#define LISSUE(J) { if ((J) >= LT_END) s.issue<J, (J) & 7>(60); else awin<(J) & 7, ((J) & 3) * 1024>(s.voff, s.base(60 + ((J) >> 2)), mode); }
    LISSUE(5) LISSUE(6) LISSUE(7)
#define M(U) { const v4u v_ = aget<U, 7>(); LISSUE(8 + U) fma8(acc, bcast<U>(p), v_); if (U == 3) { SETPRIO(14) } }
    REP8(M)
#undef M
#define M(U) fma8(acc, bcast<8 + U>(p), aget<U, 7 - U>());
    REP8(M)
#undef M
  }
  STAMP(4)
  {
    const float b0 = pair32(acc[0][0], acc[2][0]), b1 = pair32(acc[0][1], acc[2][1]), b2 = pair32(acc[1][0], acc[3][0]), b3 = pair32(acc[1][1], acc[3][1]);
    const float c0 = pair16(b0, b2), c1 = pair16(b1, b3);
    Lo[w * 68 + 4 * c + r] = cvtpk(c0 * scale, c1 * scale);
  }
  BARRIER();
  {
    const unsigned* ap = Lo + (mn & 7) * 68 + 32 * (mn >> 3) + 4 * mg;
    const v4u a0 = *(const v4u*)ap, a1 = *(const v4u*)(ap + 16);
    f32x4 dd[8];
#define MF0(D, B) "v_mfma_f32_16x16x32_bf16 %" #D ", %8, a[" B "], 0\n\t"
#define MF1(D, B) "v_mfma_f32_16x16x32_bf16 %" #D ", %9, a[" B "], %" #D "\n\t"
    asm volatile("s_nop 1\n\t" MF0(0, "32:35") MF0(1, "36:39") MF0(2, "40:43") MF0(3, "44:47") MF0(4, "48:51") MF0(5, "52:55") MF0(6, "56:59") MF0(7, "60:63")
                 MF1(0, "64:67") MF1(1, "68:71") MF1(2, "72:75") MF1(3, "76:79") MF1(4, "80:83") MF1(5, "84:87") MF1(6, "88:91") MF1(7, "92:95") "s_nop 7"
                 : "=&v"(dd[0]), "=&v"(dd[1]), "=&v"(dd[2]), "=&v"(dd[3]), "=&v"(dd[4]), "=&v"(dd[5]), "=&v"(dd[6]), "=&v"(dd[7])
                 : "v"(a0), "v"(a1) : ACLOB);
    v4u o;
#pragma unroll
    for (int k = 0; k < 4; ++k) {
      const float t0 = (dd[k][0] + dd[k][1]) + (dd[k][2] + dd[k][3]), t1 = (dd[k + 4][0] + dd[k + 4][1]) + (dd[k + 4][2] + dd[k + 4][3]);
      auto sw = __builtin_amdgcn_permlane32_swap(__float_as_uint(t0), __float_as_uint(t1), false, false);
      float v = __uint_as_float(rowsel ? sw[1] : sw[0]);
      v += dpp<XOR1>(v);
      v = pair16(v, v);
      auto sv = __builtin_amdgcn_permlane32_swap(__float_as_uint(v), __float_as_uint(v), false, false);
      o[k] = __float_as_uint(__uint_as_float(rowsel ? sv[1] : sv[0]) + ymlp[k]) & 0xfffffffeu;
    }
    if (lane < 16) st128sc1((2 * w + (lane >> 3)) * 32768 + chunk * 128 + (lane & 7) * 16, ws + WS_Y, o);
  }
  STAMP(5)
  if (!has_mlp && (head & 1) == 0) {
    unsigned xv;
    ld16(xv, (lane & 31) * 2, (u64)xp + head * 32);
    adummy(0u, (u64)outp + head * 32);
    const u64 yb = ws + WS_Y + head * 16384 + w * 4096;
    v4u y0, y1, y2, y3;
    for (unsigned spins = 0; spins < SPIN_CAP; ++spins) {
      asm volatile("global_load_dwordx4 %0, %5, %6 sc1\n\tglobal_load_dwordx4 %1, %5, %6 offset:1024 sc1\n\t"
                   "global_load_dwordx4 %2, %5, %6 offset:2048 sc1\n\tglobal_load_dwordx4 %3, %5, %6 offset:3072 sc1\n\ts_waitcnt vmcnt(0)"
                   : "=&v"(y0), "=&v"(y1), "=&v"(y2), "=&v"(y3), "+v"(xv) : "v"(lane * 16), "s"(yb) : "memory");
      const v4u t = (y0 | y1) | (y2 | y3);
      if (__all((((t[0] | t[1]) | (t[2] | t[3])) & 1u) == 0u)) break;
    }
    float v[4];
#pragma unroll
    for (int k = 0; k < 4; ++k) v[k] = (__uint_as_float(y0[k]) + __uint_as_float(y1[k])) + (__uint_as_float(y2[k]) + __uint_as_float(y3[k]));
    float t = pair16(pair32(v[0], v[2]), pair32(v[1], v[3]));
    t += dpp<XOR8>(t);
    if ((lane & 8) == 0) Lr[w * 32 + 4 * (lane & 7) + (lane >> 4)] = t;
    BARRIER();
    if (w == 0 && lane < 32) {
      float sum = 0.f;
#pragma unroll
      for (int ww = 0; ww < 8; ++ww) sum += Lr[ww * 32 + lane];
      const float o = lo16(xv) + sum;
      st16(lane * 2, (u64)outp + head * 32, cvtpk(o, o));
      asm volatile(".Lfd_end:");
      if (head == 0 && lane == 0) st64sc1(0u, ws + WS_EPOCH, tag_lo, tag_hi);
    }
  }
}

extern "C" void kernel_launch(void* const* d_in, const int* in_sizes, int n_in, void* d_out, int out_size, void* d_ws, size_t ws_size,
                              hipStream_t stream) {
  fused_decode<<<NWG, WAVES * 64, 0, stream>>>(d_in[0], d_in[1], d_in[3], d_in[2], d_in[6], d_in[7], d_in[8], d_in[4], d_ws, d_out);
}
```

```cpp
#include <hip/hip_runtime.h>
#include <hip/hip_bf16.h>

typedef unsigned v4u __attribute__((ext_vector_type(4)));
typedef unsigned v2u __attribute__((ext_vector_type(2)));
typedef float f32x4 __attribute__((ext_vector_type(4)));
typedef float f32x2 __attribute__((ext_vector_type(2)));
typedef __bf16 bf16x2 __attribute__((ext_vector_type(2)));
typedef unsigned long long u64;
#define LAS __attribute__((address_space(3)))
#define STAMP(k)

constexpr int NWG = 256, WAVES = 8;
constexpr int W0 = 3;
constexpr int DM = 5;
constexpr int FB_T0 = -100, FB_T1 = 0, FB_T2 = 80, SCH_T0 = 600, SCH_K = 450, SCH_V = 426, ANCH_OFF = 65;
constexpr int ANCH_MASK = 0x55f0;
constexpr int LT_A = 30, LT_B = -20, LT_WA = 4, LT_WB = 3, LT_END = 13;
constexpr unsigned WS_EPOCH = 0, WS_FLAG = 1024, WS_ML = 4096, WS_CNT = 32768, WS_Y = 65536;
constexpr unsigned SENT = 0xffffffffu;
constexpr unsigned SPIN_CAP = 1u << 20, LSPIN_CAP = 1u << 26;
#define WPOL ""
#define KVPOL "nt"

__device__ __forceinline__ void ld128w(v4u& dst, unsigned voff, u64 sbase) {
  asm volatile("global_load_dwordx4 %0, %1, %2 " WPOL : "=v"(dst) : "v"(voff), "s"(sbase) : "memory");
}
__device__ __forceinline__ void ld16(unsigned& dst, unsigned voff, u64 sbase) {
  asm volatile("global_load_ushort %0, %1, %2" : "=v"(dst) : "v"(voff), "s"(sbase) : "memory");
}

#define ACLOB "a0","a1","a2","a3","a4","a5","a6","a7","a8","a9","a10","a11","a12","a13","a14","a15","a16","a17","a18","a19","a20","a21","a22","a23","a24","a25","a26","a27","a28","a29","a30","a31", \
  "a32","a33","a34","a35","a36","a37","a38","a39","a40","a41","a42","a43","a44","a45","a46","a47","a48","a49","a50","a51","a52","a53","a54","a55","a56","a57","a58","a59","a60","a61","a62","a63", \
  "a64","a65","a66","a67","a68","a69","a70","a71","a72","a73","a74","a75","a76","a77","a78","a79","a80","a81","a82","a83","a84","a85","a86","a87","a88","a89","a90","a91","a92","a93","a94","a95", \
  "a96","a97","a98","a99","a100","a101","a102","a103","a104","a105","a106","a107","a108","a109","a110","a111","a112","a113","a114","a115"
template <int SLOT, int OFF> __device__ __forceinline__ void ald(unsigned voff, u64 sbase) {
  asm volatile("global_load_dwordx4 a[%c2:%c3], %0, %1 offset:%c4 " KVPOL :: "v"(voff), "s"(sbase), "i"(4 * SLOT), "i"(4 * SLOT + 3), "i"(OFF) : "memory", ACLOB);
}
template <int SLOT, int OFF> __device__ __forceinline__ void aldw(unsigned voff, u64 sbase) {
  asm volatile("global_load_dwordx4 a[%c2:%c3], %0, %1 offset:%c4 " WPOL :: "v"(voff), "s"(sbase), "i"(4 * SLOT), "i"(4 * SLOT + 3), "i"(OFF) : "memory", ACLOB);
}
__device__ __forceinline__ void ald_gu(unsigned voff, u64 gbase, u64 ubase, int idx) {
  const u64 mask = 0xffull << (8 * idx);
  asm volatile("s_mov_b64 exec, %3\n\tglobal_load_dwordx4 a[100:103], %0, %1 " WPOL "\n\tglobal_load_dwordx4 a[104:107], %0, %2 " WPOL "\n\ts_mov_b64 exec, -1"
               :: "v"(voff), "s"(gbase), "s"(ubase), "s"(mask) : "memory", ACLOB);
}
template <int REG> __device__ __forceinline__ void ald64sc1(unsigned voff, u64 sbase) {
  asm volatile("global_load_dwordx2 a[%c2:%c3], %0, %1 sc1" :: "v"(voff), "s"(sbase), "i"(REG), "i"(REG + 1) : "memory", ACLOB);
}
__device__ __forceinline__ void aanchor(unsigned voff, u64 sbase) {
  asm volatile("global_load_dword a115, %0, %1 sc1" :: "v"(voff), "s"(sbase) : "memory", ACLOB);
}
__device__ __forceinline__ void adummy(unsigned zero, u64 sbase) {
  asm volatile("s_mov_b64 exec, 1\n\tglobal_load_dword a114, %0, %1\n\ts_mov_b64 exec, -1" :: "v"(zero), "s"(sbase) : "memory", ACLOB);
}
__device__ __forceinline__ void st32sc1(unsigned voff, u64 sbase, unsigned v) {
  asm volatile("global_store_dword %0, %1, %2 sc1" :: "v"(voff), "v"(v), "s"(sbase) : "memory");
}
template <int RSLOT, int N> __device__ __forceinline__ v4u aget(void) {
  unsigned a, b, c, d;
  asm volatile("s_waitcnt vmcnt(%c4)\n\tv_accvgpr_read_b32 %0, a%c5\n\tv_accvgpr_read_b32 %1, a%c6\n\tv_accvgpr_read_b32 %2, a%c7\n\tv_accvgpr_read_b32 %3, a%c8"
               : "=v"(a), "=v"(b), "=v"(c), "=v"(d) : "i"(N), "i"(4 * RSLOT), "i"(4 * RSLOT + 1), "i"(4 * RSLOT + 2), "i"(4 * RSLOT + 3) : "memory", ACLOB);
  return v4u{a, b, c, d};
}
template <int RSLOT, int N5, int N4, int N3, int N2, int WSLOT, int OFF> __device__ __forceinline__ v4u astep(unsigned voff, u64 sbase, int mode) {
  unsigned a, b, c, d;
  asm volatile("s_waitcnt vmcnt(%c7)\n\t"
               "s_cmp_gt_i32 %6, %c17\n\ts_cbranch_scc1 1f\n\t"
               "s_waitcnt vmcnt(%c8)\n\t"
               "s_cmp_gt_i32 %6, %c18\n\ts_cbranch_scc1 1f\n\t"
               "s_waitcnt vmcnt(%c9)\n\t"
               "s_cmp_gt_i32 %6, %c19\n\ts_cbranch_scc1 1f\n\t"
               "s_waitcnt vmcnt(%c20)\n"
               "1:\n\tglobal_load_dwordx4 a[%c14:%c15], %4, %5 offset:%c16 " KVPOL "\n\t"
               "v_accvgpr_read_b32 %0, a%c10\n\tv_accvgpr_read_b32 %1, a%c11\n\tv_accvgpr_read_b32 %2, a%c12\n\tv_accvgpr_read_b32 %3, a%c13"
               : "=&v"(a), "=&v"(b), "=&v"(c), "=&v"(d)
               : "v"(voff), "s"(sbase), "s"(mode), "i"(N5), "i"(N4), "i"(N3),
                 "i"(4 * RSLOT), "i"(4 * RSLOT + 1), "i"(4 * RSLOT + 2), "i"(4 * RSLOT + 3), "i"(4 * WSLOT), "i"(4 * WSLOT + 3), "i"(OFF),
                 "i"(FB_T2), "i"(FB_T1), "i"(FB_T0), "i"(N2)
               : "memory", "scc", ACLOB);
  return v4u{a, b, c, d};
}
template <int WSLOT, int OFF> __device__ __forceinline__ void awin(unsigned voff, u64 sbase, int mode) {
  asm volatile("s_cmp_gt_i32 %2, %c6\n\ts_cbranch_scc1 1f\n\t"
               "s_waitcnt vmcnt(%c8)\n\t"
               "s_cmp_gt_i32 %2, %c7\n\ts_cbranch_scc1 1f\n\t"
               "s_waitcnt vmcnt(%c9)\n"
               "1:\n\tglobal_load_dwordx4 a[%c3:%c4], %0, %1 offset:%c5 " KVPOL
               :: "v"(voff), "s"(sbase), "s"(mode), "i"(4 * WSLOT), "i"(4 * WSLOT + 3), "i"(OFF), "i"(LT_A), "i"(LT_B), "i"(LT_WA - 1), "i"(LT_WB - 1)
               : "memory", "scc", ACLOB);
}
template <int N, int WSLOT, int OFF> __device__ __forceinline__ void apre(unsigned voff, u64 sbase) {
  asm volatile("s_waitcnt vmcnt(%c2)\n\tglobal_load_dwordx4 a[%c3:%c4], %0, %1 offset:%c5 " KVPOL
               :: "v"(voff), "s"(sbase), "i"(N), "i"(4 * WSLOT), "i"(4 * WSLOT + 3), "i"(OFF) : "memory", ACLOB);
}
template <int RSLOT> __device__ __forceinline__ v4u aread(void) {
  unsigned a, b, c, d;
  asm volatile("v_accvgpr_read_b32 %0, a%c4\n\tv_accvgpr_read_b32 %1, a%c5\n\tv_accvgpr_read_b32 %2, a%c6\n\tv_accvgpr_read_b32 %3, a%c7\n\ts_nop 1"
               : "=v"(a), "=v"(b), "=v"(c), "=v"(d) : "i"(4 * RSLOT), "i"(4 * RSLOT + 1), "i"(4 * RSLOT + 2), "i"(4 * RSLOT + 3) : "memory", ACLOB);
  return v4u{a, b, c, d};
}
template <int SLOT> __device__ __forceinline__ void awrite(unsigned a, unsigned b, unsigned c, unsigned d) {
  asm volatile("v_accvgpr_write_b32 a%c4, %0\n\tv_accvgpr_write_b32 a%c5, %1\n\tv_accvgpr_write_b32 a%c6, %2\n\tv_accvgpr_write_b32 a%c7, %3"
               :: "v"(a), "v"(b), "v"(c), "v"(d), "i"(4 * SLOT), "i"(4 * SLOT + 1), "i"(4 * SLOT + 2), "i"(4 * SLOT + 3) : ACLOB);
}
template <int S> __device__ __forceinline__ void wo_frag(void);
__device__ __forceinline__ unsigned aread_samples(void) {
  unsigned a;
  asm volatile("v_accvgpr_read_b32 %0, a115\n\ts_nop 1" : "=v"(a) :: "memory", ACLOB);
  return a;
}
__device__ __forceinline__ void st128sc1(unsigned voff, u64 sbase, const v4u& v) {
  asm volatile("global_store_dwordx4 %0, %1, %2 sc1\n\ts_nop 1" :: "v"(voff), "v"(v), "s"(sbase) : "memory");
}
__device__ __forceinline__ void st64sc1(unsigned voff, u64 sbase, unsigned lo, unsigned hi) {
  const v2u v = {lo, hi};
  asm volatile("global_store_dwordx2 %0, %1, %2 sc1" :: "v"(voff), "v"(v), "s"(sbase) : "memory");
}
__device__ __forceinline__ void st16(unsigned voff, u64 sbase, unsigned v) {
  asm volatile("global_store_short %0, %1, %2" :: "v"(voff), "v"(v), "s"(sbase) : "memory");
}
__device__ __forceinline__ v2u ld64sc1_wait(unsigned voff, u64 sbase) {
  v2u v;
  asm volatile("global_load_dwordx2 %0, %1, %2 sc1\n\ts_waitcnt vmcnt(0)" : "=&v"(v) : "v"(voff), "s"(sbase) : "memory");
  return v;
}

__device__ __forceinline__ float dot2(unsigned a, unsigned b, float acc) {
  return __builtin_amdgcn_fdot2_f32_bf16(__builtin_bit_cast(bf16x2, a), __builtin_bit_cast(bf16x2, b), acc, false);
}
__device__ __forceinline__ float lo16(unsigned v) { return __builtin_bit_cast(float, v << 16); }
__device__ __forceinline__ float hi16(unsigned v) { return __builtin_bit_cast(float, v & 0xffff0000u); }
template <int CTRL> __device__ __forceinline__ float dpp(float x) {
  return __builtin_bit_cast(float, __builtin_amdgcn_mov_dpp(__builtin_bit_cast(int, x), CTRL, 0xf, 0xf, true));
}
template <int CTRL> __device__ __forceinline__ unsigned dppu(unsigned x) { return (unsigned)__builtin_amdgcn_mov_dpp((int)x, CTRL, 0xf, 0xf, true); }
constexpr int XOR1 = 0xB1, XOR2 = 0x4E, XOR7 = 0x141, XOR8 = 0x128, ROR4 = 0x124;
__device__ __forceinline__ unsigned cvtpk(float lo, float hi) {
  const bf16x2 v = {(__bf16)lo, (__bf16)hi};
  return __builtin_bit_cast(unsigned, v);
}
__device__ __forceinline__ float rbf(float x) { return lo16(cvtpk(x, x)); }
__device__ __forceinline__ float pair32(float a, float b) {
  auto s = __builtin_amdgcn_permlane32_swap(__float_as_uint(a), __float_as_uint(b), false, false);
  return __uint_as_float(s[0]) + __uint_as_float(s[1]);
}
__device__ __forceinline__ float pair16(float a, float b) {
  auto s = __builtin_amdgcn_permlane16_swap(__float_as_uint(a), __float_as_uint(b), false, false);
  return __uint_as_float(s[0]) + __uint_as_float(s[1]);
}
__device__ __forceinline__ float wave_sum(float x) {
  x += dpp<XOR1>(x); x += dpp<XOR2>(x); x += dpp<XOR7>(x); x += dpp<XOR8>(x);
  x = pair16(x, x); x = pair32(x, x); return x;
}
__device__ __forceinline__ float wave_max(float x) {
  x = fmaxf(x, dpp<XOR1>(x)); x = fmaxf(x, dpp<XOR2>(x)); x = fmaxf(x, dpp<XOR7>(x)); x = fmaxf(x, dpp<XOR8>(x));
  auto s = __builtin_amdgcn_permlane16_swap(__float_as_uint(x), __float_as_uint(x), false, false);
  x = fmaxf(__uint_as_float(s[0]), __uint_as_float(s[1]));
  auto t = __builtin_amdgcn_permlane32_swap(__float_as_uint(x), __float_as_uint(x), false, false);
  return fmaxf(__uint_as_float(t[0]), __uint_as_float(t[1]));
}
__device__ __forceinline__ float treduce16(float (&x)[16], int c) {
#define TR_STEP(HALF, CTRL, BIT) _Pragma("unroll") for (int h = 0; h < (HALF); ++h) { \
    const float lo_ = x[h], hi_ = x[h + (HALF)]; const float t1_ = lo_ + dpp<CTRL>(lo_), t2_ = hi_ + dpp<CTRL>(hi_); x[h] = (c & (BIT)) ? t2_ : t1_; }
  TR_STEP(8, XOR8, 8) TR_STEP(4, XOR7, 4) TR_STEP(2, XOR2, 2) TR_STEP(1, XOR1, 1)
#undef TR_STEP
  return x[0];
}
template <int CTRL, int BIT> __device__ __forceinline__ float tmerge(float lo, float hi, int c) {
  const float t1 = lo + dpp<CTRL>(lo), t2 = hi + dpp<CTRL>(hi); return (c & BIT) ? t2 : t1;
}
template <int U> __device__ __forceinline__ void tfeed(float (&x)[16], int c) {
  if constexpr ((U & 1) == 1) x[U - 1] = tmerge<XOR1, 1>(x[U - 1], x[U], c);
  if constexpr ((U & 3) == 3) x[U - 3] = tmerge<XOR2, 2>(x[U - 3], x[U - 1], c);
  if constexpr ((U & 7) == 7) x[U - 7] = tmerge<ROR4, 4>(x[U - 7], x[U - 3], c);
  if constexpr (U == 15) x[0] = tmerge<XOR8, 8>(x[0], x[8], c);
  constexpr int L = (U == 15) ? 0 : ((U & 7) == 7) ? U - 7 : ((U & 3) == 3) ? U - 3 : ((U & 1) == 1) ? U - 1 : U;
  asm volatile("" : "+v"(x[L]));
}
template <int T> __device__ __forceinline__ float bcast(float p) { return dpp<0x150 + T>(p); }
__device__ __forceinline__ void fma8(f32x2 (&acc)[4], float p, const v4u& v) {
  const f32x2 pp = {p, p};
#pragma unroll
  for (int k = 0; k < 4; ++k) acc[k] = __builtin_elementwise_fma(pp, f32x2{lo16(v[k]), hi16(v[k])}, acc[k]);
  asm volatile("" : "+v"(acc[0]), "+v"(acc[1]), "+v"(acc[2]), "+v"(acc[3]));
}
__device__ __forceinline__ unsigned plo(unsigned hi_row, unsigned lo_row) { return __builtin_amdgcn_perm(hi_row, lo_row, 0x05040100u); }
__device__ __forceinline__ unsigned phi(unsigned hi_row, unsigned lo_row) { return __builtin_amdgcn_perm(hi_row, lo_row, 0x07060302u); }

template <int S> __device__ __forceinline__ void wo_frag(void) {
  const v4u r0 = aread<S>(), r1 = aread<S + 1>(), r2 = aread<S + 2>(), r3 = aread<S + 3>(), r4 = aread<S + 4>(), r5 = aread<S + 5>(), r6 = aread<S + 6>(), r7 = aread<S + 7>();
#define WF(K) awrite<S + 2 * K>(plo(r1[K], r0[K]), plo(r3[K], r2[K]), plo(r5[K], r4[K]), plo(r7[K], r6[K])); \
              awrite<S + 2 * K + 1>(phi(r1[K], r0[K]), phi(r3[K], r2[K]), phi(r5[K], r4[K]), phi(r7[K], r6[K]));
  WF(0) WF(1) WF(2) WF(3)
#undef WF
}

constexpr int younger(int U, int X, int NA, int NB) { return (U + DM - 1 - X) + ((U >= 1 && X <= DM) ? NA : 0) + ((U >= 9 && X <= 8 + DM) ? NB : 0); }
#define MAPSET 0x1000, 0x800, 0x80, 0x100, 0x400,  0x40, 0x8, 0x4,  0x600, 0x10, 0x20,  0x100, 0x200, 0, 0, 0
struct Stream {
  u64 K, V; unsigned voff, cofs; unsigned mr[5];
  __device__ __forceinline__ u64 base(int round) const {
    unsigned t = cofs;
#pragma unroll
    for (int k = 0; k < 5; ++k) t ^= ((round >> k) & 1) ? mr[k] : 0u;
    return ((round < 32) ? K : V) + (u64)t;
  }
  template <int J, int SLOT> __device__ __forceinline__ void issue(int round0) const { ald<SLOT, (J & 3) * 1024>(voff, base(round0 + (J >> 2))); }
  template <int U, int NA, int NB> __device__ __forceinline__ v4u step(int round0, int mode) const {
    return astep<(U & 7), younger(U, U, NA, NB), younger(U, U + 1, NA, NB), younger(U, U + 2, NA, NB), younger(U, U + 3, NA, NB),
                 ((U + DM) & 7), ((U + DM) & 3) * 1024>(
        voff, base(round0 + ((U + DM) >> 2)), mode);
  }
};
__device__ __forceinline__ float qk(const v4u& k, const v4u& q) { return dot2(k[3], q[3], dot2(k[2], q[2], dot2(k[1], q[1], dot2(k[0], q[0], 0.f)))); }
#define REP16(M) M(0) M(1) M(2) M(3) M(4) M(5) M(6) M(7) M(8) M(9) M(10) M(11) M(12) M(13) M(14) M(15)
#define REP15(M) M(1) M(2) M(3) M(4) M(5) M(6) M(7) M(8) M(9) M(10) M(11) M(12) M(13) M(14) M(15)
#define REP8(M) M(0) M(1) M(2) M(3) M(4) M(5) M(6) M(7)
#define BARRIER() asm volatile("s_waitcnt lgkmcnt(0)\n\ts_barrier" ::: "memory")

constexpr int WQ0 = 4, QPOST = W0 + 8 - WQ0, QGET = 10, XSTEP = 8;
static_assert(!((ANCH_MASK >> 13) & 1) && !(ANCH_MASK & 15), "tile 13 carries the (m,l) gather; samples are read behind the flags");
static_assert(QGET > QPOST && QGET < 16 - W0 && XSTEP + WQ0 >= 8 && XSTEP + W0 < 16, "tile-0 schedule");
constexpr int t0_ext(int m) { return m < 0 ? 2 * WQ0 : (m + WQ0 < 8 ? 2 : (m == XSTEP ? 4 : 0)); }
constexpr int t0_wait(int n) { int c = W0 - 1; for (int m = (n - W0 < -1 ? -1 : n - W0); m <= n - 1; ++m) c += t0_ext(m); return c; }

__device__ __forceinline__ float fin_score(float d) { return rbf(rbf(d) * 0.08838834764831845f); }
constexpr float LOG2E = 1.4426950408889634f;

__global__ void __launch_bounds__(WAVES * 64, 2) fused_decode(const void* xp, const void* Kp, const void* wqp, const void* Vp, const void* wgp,
                                                             const void* wup, const void* wdp, const void* wop, void* wsp, void* outp) {
  __shared__ __attribute__((aligned(16))) float lds[8 * 128 + 8 * 16 + 8 * 68 + 8 * 32 + 4];
  float* Lq = lds;
  float* Lg = lds + 1024;
  unsigned* Lo = (unsigned*)(lds + 1024 + 128);
  float* Lr = lds + 1024 + 128 + 8 * 68;
  volatile LAS unsigned* Lcnt = (volatile LAS unsigned*)(Lr + 256);
  {
    u64 pc; unsigned nk;
    asm volatile("s_getpc_b64 %0\n.Lfd_begin:\n\ts_mov_b32 %1, (.Lfd_end-.Lfd_begin)>>10" : "=s"(pc), "=s"(nk));
    const unsigned sl = (((blockIdx.x >> 3) * 8 + (threadIdx.x >> 6)) * nk) >> 8;
    asm volatile("global_load_dwordx4 a[112:115], %0, %1" :: "v"(sl * 1024 + (threadIdx.x & 63) * 16), "s"(pc & ~63ull) : "memory", ACLOB);
  }
  asm volatile("" :: "s"(wsp), "s"(outp), "s"(xp));
  STAMP(0)
  const int tid = threadIdx.x, lane = tid & 63, w = __builtin_amdgcn_readfirstlane(tid >> 6), r = lane >> 4, c = lane & 15;
  const int b = blockIdx.x, xcd = b & 7, i8 = b >> 3;
  const int head = xcd * 4 + (i8 & 3), split = i8 >> 2, chunk = head * 8 + split;
  const bool has_mlp = i8 < 28;
  const int j = xcd * 28 + (has_mlp ? i8 : 27);
  const u64 ws = (u64)wsp;
  const u64 mlb = ws + WS_ML + head * 512;

  Stream s;
  {
    constexpr unsigned mp[16] = {MAPSET};
    unsigned t = 0;
#pragma unroll
    for (int k = 0; k < 3; ++k) t ^= (((w >> k) & 1) ? mp[5 + k] : 0u) ^ (((split >> k) & 1) ? mp[8 + k] : 0u);
#pragma unroll
    for (int k = 0; k < 5; ++k) { t ^= ((head >> k) & 1) ? mp[11 + k] : 0u; s.mr[k] = mp[k] << 10; }
    s.cofs = t << 10; s.K = (u64)Kp + ((u64)head << 23); s.V = (u64)Vp + ((u64)head << 23); s.voff = lane * 16;
  }
  v4u x8; unsigned xs; v4u wq[16];
  const unsigned wq_vo = r * 8192 + c * 16; const u64 wq_sb = (u64)wqp + (u64)w * (64 * 8192) + head * 256;
#define WQ_PAIR(P) { ld128w(wq[2 * (P)], wq_vo, wq_sb + (u64)(2 * (P)) * (4 * 8192)); ld128w(wq[2 * (P) + 1], wq_vo, wq_sb + (u64)(2 * (P) + 1) * (4 * 8192)); }
  ld128w(x8, lane * 16, (u64)xp);
  ld16(xs, (4 * c + r) * 2, (u64)xp + w * 128);
  s.issue<0, 0>(0); s.issue<1, 1>(0); s.issue<2, 2>(0);
  __builtin_amdgcn_sched_barrier(0);
  const u64 tstart = __builtin_amdgcn_s_memrealtime();
  WQ_PAIR(0) WQ_PAIR(1) WQ_PAIR(2) WQ_PAIR(3)
  if (tid == 0) { Lcnt[0] = 0u; Lcnt[1] = 0u; Lcnt[2] = (unsigned)tstart + SCH_T0; Lcnt[3] = 0u; }
  BARRIER();
  const int mg = lane >> 4, mn = lane & 15, cg = mn >> 1, rowsel = lane & 1;
  const unsigned gu_vo = (4 * c + r) * 3584;
  const u64 g_sb = (u64)wgp + (u64)w * (64 * 3584) + j * 16, u_sb = (u64)wup + (u64)w * (64 * 3584) + j * 16;
  const u64 wo_sb = (u64)wop + (u64)head * (128 * 1024) + w * 128;
  unsigned tch[8];
  {
#define T(I, A) asm volatile("s_load_dword %0, %1, 0x0" : "=s"(tch[I]) : "s"((u64)(A) & ~3ull) : "memory");
    T(0, g_sb) T(1, u_sb) T(2, (u64)wdp + (u64)j * 8192) T(3, ws + 3072) T(4, wo_sb) T(5, s.base(32)) T(6, (!has_mlp && (head & 1) == 0) ? (u64)outp + head * 32 : u_sb + 63 * 3584) T(7, g_sb + 63 * 3584)
#undef T
  }
  float xn; unsigned xpair;
  float qa[8] = {0.f, 0.f, 0.f, 0.f, 0.f, 0.f, 0.f, 0.f};
  v4u q;
  float sc[8];
  float d[16];
#define PRE(N) apre<t0_wait(N), ((N) + W0 < 16 ? (N) + W0 : ((N) + W0) & 7), (((N) + W0) & 3) * 1024>(s.voff, s.base(((N) + W0) >> 2));
#define QP(P) { asm volatile("" : "+v"(wq[2 * (P)]), "+v"(wq[2 * (P) + 1])); const unsigned xx = dppu<0x150 + 2 * (P)>(xpair); \
      _Pragma("unroll") for (int k = 0; k < 4; ++k) { qa[2 * k] = dot2(plo(wq[2 * (P) + 1][k], wq[2 * (P)][k]), xx, qa[2 * k]); \
        qa[2 * k + 1] = dot2(phi(wq[2 * (P) + 1][k], wq[2 * (P)][k]), xx, qa[2 * k + 1]); } }
#define KQ(U) { d[U] = qk(aread<U>(), q); tfeed<U>(d, c); }
#define M(N) PRE(N) \
    if ((N) + WQ0 < 8) WQ_PAIR(((N) + WQ0) & 7) \
    if ((N) == XSTEP) { ald_gu(gu_vo, g_sb, u_sb, 0); if (w == 0) ald64sc1<108>(0u, ws + WS_EPOCH); else adummy(0u, (u64)xp); \
                        aldw<24, 0>((lane >> 3) * 1024 + (lane & 7) * 16, (u64)wdp + (u64)j * 8192 + w * 128); } \
    if ((N) == 0) { \
        \
      asm volatile("" : "+v"(x8), "+v"(xs)); \
      float xv[8]; \
      _Pragma("unroll") for (int k = 0; k < 4; ++k) { xv[2 * k] = lo16(x8[k]); xv[2 * k + 1] = hi16(x8[k]); } \
      float sum = 0.f; \
      _Pragma("unroll") for (int k = 0; k < 8; ++k) sum += xv[k]; \
      const float mean = wave_sum(sum) * (1.f / 512.f); \
      float sq = 0.f; \
      _Pragma("unroll") for (int k = 0; k < 8; ++k) { const float dlt = xv[k] - mean; sq = fmaf(dlt, dlt, sq); } \
      const float var = wave_sum(sq) * (1.f / 512.f); \
      xn = rbf(rbf(lo16(xs) - rbf(mean)) / rbf(sqrtf(rbf(rbf(var) + 1e-5f)))); \
      const unsigned xb = __float_as_uint(xn); \
      xpair = phi(dppu<XOR1>(xb), xb);     \
      STAMP(1) \
    } \
    if ((N) == W0) { QP(0) QP(1) QP(2) QP(3) } \
    if ((N) > W0 && (N) - W0 - 1 + WQ0 < 8) QP(((N) - W0 - 1 + WQ0) & 7) \
    if ((N) == QPOST) { \
      const float b0 = pair32(qa[0], qa[4]), b1 = pair32(qa[1], qa[5]), b2 = pair32(qa[2], qa[6]), b3 = pair32(qa[3], qa[7]); \
      const float c0 = pair16(b0, b2), c1 = pair16(b1, b3); \
      *(f32x2*)(Lq + w * 128 + 8 * c + 2 * r) = f32x2{c0, c1};     \
      asm volatile("s_waitcnt lgkmcnt(0)" : "+s"(tch[0]), "+s"(tch[1]), "+s"(tch[2]), "+s"(tch[3]), "+s"(tch[4]), "+s"(tch[5]), "+s"(tch[6]), "+s"(tch[7]) :: "memory"); \
      if (lane == 0) __hip_atomic_fetch_add((LAS unsigned*)(Lcnt + 1), 1u, __ATOMIC_RELAXED, __HIP_MEMORY_SCOPE_WORKGROUP); \
    } \
    if ((N) == QGET) { \
      for (unsigned spins = 0; Lcnt[1] != 8u && spins < LSPIN_CAP; ++spins) __builtin_amdgcn_s_sleep(1); \
      asm volatile("" ::: "memory"); \
      f32x4 a0 = *(const f32x4*)(Lq + (2 * r) * 128 + 8 * c), a1 = *(const f32x4*)(Lq + (2 * r) * 128 + 8 * c + 4); \
      f32x4 b0 = *(const f32x4*)(Lq + (2 * r + 1) * 128 + 8 * c), b1 = *(const f32x4*)(Lq + (2 * r + 1) * 128 + 8 * c + 4); \
      a0 += b0; a1 += b1; \
      float v[8] = {a0[0], a0[1], a0[2], a0[3], a1[0], a1[1], a1[2], a1[3]}; \
      _Pragma("unroll") for (int k = 0; k < 8; ++k) { v[k] = pair16(v[k], v[k]); v[k] = pair32(v[k], v[k]); } \
      _Pragma("unroll") for (int k = 0; k < 4; ++k) q[k] = cvtpk(v[2 * k], v[2 * k + 1]); \
      STAMP(2) \
      KQ(0) KQ(1) KQ(2) KQ(3) \
    } \
    if ((N) == QGET + 1) { KQ(4) KQ(5) KQ(6) KQ(7) KQ(8) KQ(9) KQ(10) } \
    if ((N) > QGET) KQ(N) \
    __builtin_amdgcn_sched_barrier(0);
  REP16(M)
#undef M
  sc[7] = fin_score(d[0]);
  PRE(16) PRE(17)
  unsigned tag_lo, tag_hi;
  {
    const v4u e = aread<27>();
    const u64 tag = (((u64)(unsigned)__builtin_amdgcn_readfirstlane(e[1]) << 32) | (u64)(unsigned)__builtin_amdgcn_readfirstlane(e[0])) + 1ull;
    tag_lo = (unsigned)tag; tag_hi = (unsigned)(tag >> 32);
  }
  const bool clearer = !has_mlp && w == 0;
  if (clearer) {
    const v4u sv = {SENT, SENT, SENT, SENT};
    for (int i = 0; i < 16; ++i) st128sc1(lane * 16 + i * 1024, ws + WS_Y + head * 16384, sv);
    if (lane < 32) st128sc1(lane * 16, mlb, sv);
    if (lane < 16) st32sc1(lane * 1024 + xcd * 128 + (head & 3) * 4, ws + WS_CNT, SENT);
  }
  const unsigned fl_vo = ((lane >> 2) & 7) * 128 + (lane & 3) * 8;
  const unsigned wo_vo = (64 * rowsel + 8 * mg) * 1024 + cg * 16;
  int mode = FB_T1;
#define NOMINAL(TILE) ((TILE) <= 8 ? (TILE) * SCH_K : 8 * SCH_K + ((TILE) - 8) * SCH_V)
#define ANCHOR_LD(TILE) if (w == 0 && ((ANCH_MASK >> (TILE)) & 1)) aanchor(((lane >> 2) & 7) * 128 + (lane & 3) * 4 + ((TILE) - 2) * 1024, ws + WS_CNT); else adummy(0u, (u64)xp);
#define FEEDBACK(TILE) { const unsigned now_ = (unsigned)__builtin_amdgcn_s_memrealtime(); \
    if (w == 0) { \
      if (clearer && ((ANCH_MASK >> ((TILE) + 2)) & 1) && lane == 0) st32sc1((TILE) * 1024 + xcd * 128 + (head & 3) * 4, ws + WS_CNT, now_ & 0xfffffffeu); \
      if ((ANCH_MASK >> (TILE)) & 1) { const unsigned a_ = aread_samples(); const bool ok_ = a_ != SENT; \
        const float sum_ = wave_sum(ok_ ? (float)(int)(a_ - now_) : 0.f); const int n_ = __popcll(__ballot(ok_)); \
        if (n_ >= 32) Lcnt[2] = now_ + (unsigned)((int)(sum_ / (float)n_) - NOMINAL((TILE) - 2) + ANCH_OFF); } } \
    mode = (int)(now_ - (unsigned)__builtin_amdgcn_readfirstlane((int)Lcnt[2])) - NOMINAL(TILE); \
    if ((TILE) < 4 && mode < FB_T1) mode = FB_T1; }
#define FEEDBACK2(TILE) { const unsigned now_ = (unsigned)__builtin_amdgcn_s_memrealtime(); \
    mode = (int)(now_ - (unsigned)__builtin_amdgcn_readfirstlane((int)Lcnt[2])) - NOMINAL(TILE) - ((TILE) < 8 ? SCH_K / 2 : SCH_V / 2); \
    if ((TILE) < 4 && mode < FB_T1) mode = FB_T1; }
#define SETPRIO(TILE) { if (((w >> 2) ^ (TILE)) & 1) __builtin_amdgcn_s_setprio(1); else __builtin_amdgcn_s_setprio(0); }
#pragma nounroll
  for (int tile = 1; tile < 8; ++tile) {
    SETPRIO(tile)
    d[0] = qk(s.step<0, 3, 2>(tile * 4, mode), q); tfeed<0>(d, c);
    switch (tile) {
      case 1: aldw<8, 0>(wo_vo, wo_sb); aldw<9, 1024>(wo_vo, wo_sb); aldw<10, 2048>(wo_vo, wo_sb); break;
      case 2: aldw<11, 3072>(wo_vo, wo_sb); aldw<12, 0>(wo_vo, wo_sb + 4096); aldw<13, 1024>(wo_vo, wo_sb + 4096); break;
      case 3: aldw<14, 2048>(wo_vo, wo_sb + 4096); aldw<15, 3072>(wo_vo, wo_sb + 4096); if (w == 0) ald64sc1<110>(fl_vo, ws + WS_FLAG); else adummy(0u, (u64)xp); break;
      case 4: aldw<16, 0>(wo_vo, wo_sb + 32768); aldw<17, 1024>(wo_vo, wo_sb + 32768); ANCHOR_LD(tile) break;
      case 5: aldw<18, 2048>(wo_vo, wo_sb + 32768); aldw<19, 3072>(wo_vo, wo_sb + 32768); ANCHOR_LD(tile) break;
      case 6: aldw<20, 0>(wo_vo, wo_sb + 36864); aldw<21, 1024>(wo_vo, wo_sb + 36864); ANCHOR_LD(tile) break;
      default: aldw<22, 2048>(wo_vo, wo_sb + 36864); aldw<23, 3072>(wo_vo, wo_sb + 36864); ANCHOR_LD(tile) break;
    }
#define M(U) d[U] = qk(s.step<U, 3, 2>(tile * 4, mode), q); tfeed<U>(d, c); \
    if (U == 7) { \
      { FEEDBACK(tile) } \
      if (tile == 1 && clearer) {   \
        asm volatile("s_waitcnt vmcnt(0)" ::: "memory"); \
        if (lane == 0) st64sc1(0u, ws + WS_FLAG + xcd * 128 + (head & 3) * 8, tag_lo, tag_hi); } \
    } \
    if (U == 15) { FEEDBACK2(tile) } \
    if (U == 8) ald_gu(gu_vo, g_sb, u_sb, tile);
    REP15(M)
#undef M
#pragma unroll
    for (int k = 0; k < 7; ++k) sc[k] = sc[k + 1];
    sc[7] = fin_score(d[0]);
    if (tile == 1) { STAMP(6) }
    if (tile == 3 && w == 0) {
      const v4u e = aread<27>();
      if (!__all(e[2] == tag_lo && e[3] == tag_hi)) {
        for (unsigned spins = 0; spins < SPIN_CAP; ++spins) {
          __builtin_amdgcn_s_sleep(2);
          const v2u f = ld64sc1_wait(fl_vo, ws + WS_FLAG);
          if (__all(f[0] == tag_lo && f[1] == tag_hi)) break;
        }
      }
      Lcnt[3] = 1u;
    }
  }
  STAMP(3)
  float mw;
  {
    float m = sc[0];
#pragma unroll
    for (int k = 1; k < 8; ++k) m = fmaxf(m, sc[k]);
    mw = wave_max(m);
    float l = 0.f;
#pragma unroll
    for (int k = 0; k < 8; ++k) { sc[k] = __builtin_amdgcn_exp2f((sc[k] - mw) * LOG2E); l += sc[k]; }
    const float lw = wave_sum(l);
    for (unsigned spins = 0; Lcnt[3] == 0u && spins < LSPIN_CAP; ++spins) __builtin_amdgcn_s_sleep(1);
    asm volatile("" ::: "memory");
    if (lane == 0) st64sc1(0u, mlb + (split * 8 + w) * 8, __float_as_uint(mw) & 0xfffffffeu, __float_as_uint(lw) & 0xfffffffeu);
  }
  f32x2 acc[4] = {{0.f, 0.f}, {0.f, 0.f}, {0.f, 0.f}, {0.f, 0.f}};
  float scale = 0.f, ymlp[4] = {0.f, 0.f, 0.f, 0.f};
#pragma nounroll
  for (int tile = 8; tile < 15; ++tile) {
    SETPRIO(tile)
    const float p = sc[0];
#define M(U) fma8(acc, bcast<U>(p), s.step<U, 1, 0>(tile * 4, mode)); \
    if (U == 0) { if (tile == 13) ald64sc1<112>(lane * 8, mlb); else ANCHOR_LD(tile) } \
    if (U == 7) { FEEDBACK(tile) } \
    if (U == 15) { FEEDBACK2(tile) } \
    if (U == 10) { \
        \
      switch (tile) { \
        case 8: {   \
          const v4u wg = aread<25>(), wu = aread<26>(); \
          float g[16]; \
          _Pragma("unroll") for (int k = 0; k < 4; ++k) { g[2 * k] = xn * lo16(wg[k]); g[2 * k + 1] = xn * hi16(wg[k]); g[8 + 2 * k] = xn * lo16(wu[k]); g[8 + 2 * k + 1] = xn * hi16(wu[k]); } \
          float t = treduce16(g, c); \
          t = pair16(t, t); t = pair32(t, t); \
          if (r == 0) Lg[w * 16 + c] = t; \
          asm volatile("s_waitcnt lgkmcnt(0)" ::: "memory"); \
          if (lane == 0) __hip_atomic_fetch_add((LAS unsigned*)Lcnt, 1u, __ATOMIC_RELAXED, __HIP_MEMORY_SCOPE_WORKGROUP); \
        } break; \
        case 9: wo_frag<8>(); break; \
        case 10: wo_frag<16>(); break; \
        case 11: {   \
          for (unsigned spins = 0; Lcnt[0] != 8u && spins < LSPIN_CAP; ++spins) __builtin_amdgcn_s_sleep(1); \
          asm volatile("" ::: "memory"); \
          const int f = lane >> 3; \
          float g = 0.f, u = 0.f; \
          _Pragma("unroll") for (int ww = 0; ww < 8; ++ww) { g += Lg[ww * 16 + f]; u += Lg[ww * 16 + 8 + f]; } \
          g = rbf(g); u = rbf(u); \
          float hval = rbf(rbf(1.f / (1.f + __expf(-g))) * u); \
          if (!has_mlp) hval = 0.f; \
          const v4u wd = aread<24>(); \
          float ym[8]; \
          _Pragma("unroll") for (int k = 0; k < 4; ++k) { ym[2 * k] = hval * lo16(wd[k]); ym[2 * k + 1] = hval * hi16(wd[k]); } \
          _Pragma("unroll") for (int k = 0; k < 4; ++k) { float t = pair32(ym[k], ym[k + 4]); t = pair16(t, t); t += dpp<XOR8>(t); \
            ymlp[k] = __builtin_bit_cast(float, __builtin_amdgcn_ds_bpermute((rowsel * 32 + cg) * 4, __builtin_bit_cast(int, t))); }   \
        } break; \
        case 13: {   \
          const v4u e = aread<28>(); \
          unsigned mb = e[0], lb = e[1]; \
          if (!__all(mb != SENT && lb != SENT)) { \
            for (unsigned spins = 0; spins < SPIN_CAP; ++spins) { \
              __builtin_amdgcn_s_sleep(2); \
              const v2u t = ld64sc1_wait(lane * 8, mlb); \
              mb = t[0]; lb = t[1]; \
              if (__all(mb != SENT && lb != SENT)) break; \
            } \
          } \
          const float mi = __uint_as_float(mb), li = __uint_as_float(lb); \
          const float M_ = wave_max(mi); \
          const float L_ = wave_sum(li * __builtin_amdgcn_exp2f((mi - M_) * LOG2E)); \
          scale = __builtin_amdgcn_exp2f((mw - M_) * LOG2E) / L_; \
        } break; \
        default: break; \
      } \
    }
    REP16(M)
#undef M
#pragma unroll
    for (int k = 0; k < 7; ++k) sc[k] = sc[k + 1];
  }
  {
    const float p = sc[0];
    SETPRIO(15)
#define LISSUE(J) { if ((J) >= LT_END) s.issue<J, (J) & 7>(60); else awin<(J) & 7, ((J) & 3) * 1024>(s.voff, s.base(60 + ((J) >> 2)), mode); }
    LISSUE(5) LISSUE(6) LISSUE(7)
#define M(U) { const v4u v_ = aget<U, 7>(); LISSUE(8 + U) fma8(acc, bcast<U>(p), v_); if (U == 3) { SETPRIO(14) } }
    REP8(M)
#undef M
#define M(U) fma8(acc, bcast<8 + U>(p), aget<U, 7 - U>());
    REP8(M)
#undef M
  }
  STAMP(4)
  {
    const float b0 = pair32(acc[0][0], acc[2][0]), b1 = pair32(acc[0][1], acc[2][1]), b2 = pair32(acc[1][0], acc[3][0]), b3 = pair32(acc[1][1], acc[3][1]);
    const float c0 = pair16(b0, b2), c1 = pair16(b1, b3);
    Lo[w * 68 + 4 * c + r] = cvtpk(c0 * scale, c1 * scale);
  }
  BARRIER();
  {
    const unsigned* ap = Lo + (mn & 7) * 68 + 32 * (mn >> 3) + 4 * mg;
    const v4u a0 = *(const v4u*)ap, a1 = *(const v4u*)(ap + 16);
    f32x4 dd[8];
#define MF0(D, B) "v_mfma_f32_16x16x32_bf16 %" #D ", %8, a[" B "], 0\n\t"
#define MF1(D, B) "v_mfma_f32_16x16x32_bf16 %" #D ", %9, a[" B "], %" #D "\n\t"
    asm volatile("s_nop 1\n\t" MF0(0, "32:35") MF0(1, "36:39") MF0(2, "40:43") MF0(3, "44:47") MF0(4, "48:51") MF0(5, "52:55") MF0(6, "56:59") MF0(7, "60:63")
                 MF1(0, "64:67") MF1(1, "68:71") MF1(2, "72:75") MF1(3, "76:79") MF1(4, "80:83") MF1(5, "84:87") MF1(6, "88:91") MF1(7, "92:95") "s_nop 7"
                 : "=&v"(dd[0]), "=&v"(dd[1]), "=&v"(dd[2]), "=&v"(dd[3]), "=&v"(dd[4]), "=&v"(dd[5]), "=&v"(dd[6]), "=&v"(dd[7])
                 : "v"(a0), "v"(a1) : ACLOB);
    v4u o;
#pragma unroll
    for (int k = 0; k < 4; ++k) {
      const float t0 = (dd[k][0] + dd[k][1]) + (dd[k][2] + dd[k][3]), t1 = (dd[k + 4][0] + dd[k + 4][1]) + (dd[k + 4][2] + dd[k + 4][3]);
      auto sw = __builtin_amdgcn_permlane32_swap(__float_as_uint(t0), __float_as_uint(t1), false, false);
      float v = __uint_as_float(rowsel ? sw[1] : sw[0]);
      v += dpp<XOR1>(v);
      v = pair16(v, v);
      auto sv = __builtin_amdgcn_permlane32_swap(__float_as_uint(v), __float_as_uint(v), false, false);
      o[k] = __float_as_uint(__uint_as_float(rowsel ? sv[1] : sv[0]) + ymlp[k]) & 0xfffffffeu;
    }
    if (lane < 16) st128sc1((2 * w + (lane >> 3)) * 32768 + chunk * 128 + (lane & 7) * 16, ws + WS_Y, o);
  }
  STAMP(5)
  if (!has_mlp && (head & 1) == 0) {
    unsigned xv;
    ld16(xv, (lane & 31) * 2, (u64)xp + head * 32);
    adummy(0u, (u64)outp + head * 32);
    const u64 yb = ws + WS_Y + head * 16384 + w * 4096;
    v4u y0, y1, y2, y3;
    for (unsigned spins = 0; spins < SPIN_CAP; ++spins) {
      asm volatile("global_load_dwordx4 %0, %5, %6 sc1\n\tglobal_load_dwordx4 %1, %5, %6 offset:1024 sc1\n\t"
                   "global_load_dwordx4 %2, %5, %6 offset:2048 sc1\n\tglobal_load_dwordx4 %3, %5, %6 offset:3072 sc1\n\ts_waitcnt vmcnt(0)"
                   : "=&v"(y0), "=&v"(y1), "=&v"(y2), "=&v"(y3), "+v"(xv) : "v"(lane * 16), "s"(yb) : "memory");
      const v4u t = (y0 | y1) | (y2 | y3);
      if (__all((((t[0] | t[1]) | (t[2] | t[3])) & 1u) == 0u)) break;
    }
    float v[4];
#pragma unroll
    for (int k = 0; k < 4; ++k) v[k] = (__uint_as_float(y0[k]) + __uint_as_float(y1[k])) + (__uint_as_float(y2[k]) + __uint_as_float(y3[k]));
    float t = pair16(pair32(v[0], v[2]), pair32(v[1], v[3]));
    t += dpp<XOR8>(t);
    if ((lane & 8) == 0) Lr[w * 32 + 4 * (lane & 7) + (lane >> 4)] = t;
    BARRIER();
    if (w == 0 && lane < 32) {
      float sum = 0.f;
#pragma unroll
      for (int ww = 0; ww < 8; ++ww) sum += Lr[ww * 32 + lane];
      const float o = lo16(xv) + sum;
      st16(lane * 2, (u64)outp + head * 32, cvtpk(o, o));
      asm volatile(".Lfd_end:");
      if (head == 0 && lane == 0) st64sc1(0u, ws + WS_EPOCH, tag_lo, tag_hi);
    }
  }
}

extern "C" void kernel_launch(void* const* d_in, const int* in_sizes, int n_in, void* d_out, int out_size, void* d_ws, size_t ws_size,
                              hipStream_t stream) {
  fused_decode<<<NWG, WAVES * 64, 0, stream>>>(d_in[0], d_in[1], d_in[3], d_in[2], d_in[6], d_in[7], d_in[8], d_in[4], d_ws, d_out);
}
```
